```python
import torch

def _softmax(x, axis=-1):
    return torch.softmax(x, dim=axis)
def _gelu(x):
    return torch.nn.functional.gelu(x, approximate="tanh")
def _silu(x):
    return torch.nn.functional.silu(x)
def _sigmoid(x):
    return torch.sigmoid(x)
def _layernorm(x, axis=-1, eps=1e-5):
    m = x.mean(dim=axis, keepdim=True)
    v = x.var(dim=axis, keepdim=True, unbiased=False)
    return (x - m) / torch.sqrt(v + eps)
def _scatter_add(tgt, idx, src, axis=0):
    return tgt.clone().index_add_(axis, idx, src)
def _segment_sum(src, seg, n):
    out = torch.zeros((n,) + src.shape[1:], dtype=src.dtype)
    return out.index_add_(0, seg, src)
def _pad(x, pw):
    flat = tuple(v for pair in reversed(pw) for v in pair)
    return torch.nn.functional.pad(x, flat)

kernel_name = 'standalone__mha_block_cached__near_ridge'

def setup_inputs():

    _w = (lambda a: (lambda t: t.to(torch.bfloat16) if t.is_floating_point() else t)(torch.as_tensor(a)))
    B, Sq, D = 8, 192, 576
    H, Sk, Dh = 32, 512, 128
    return {
        "x": _w((lambda *s: torch.randn(*s, dtype=torch.bfloat16) * 0.8)(B, Sq, D)),
        "k_cache": _w((lambda *s: torch.randn(*s, dtype=torch.bfloat16) * 0.8)(B, H, Sk, Dh)),
        "v_cache": _w((lambda *s: torch.randn(*s, dtype=torch.bfloat16) * 0.05)(B, H, Sk, Dh)),
        "wq": _w((lambda *s: torch.randn(*s, dtype=torch.bfloat16) * 0.8)(D, H * Dh) * (D ** -0.5)),
        "wo": _w((lambda *s: torch.randn(*s, dtype=torch.bfloat16) * 0.05)(H * Dh, D) * ((H * Dh) ** -0.5)),
    }

def reference(x, k_cache, v_cache, wq, wo):
    B, Sq, D = x.shape
    H, Dh = 32, 128
    q = (lambda x, *d: x.permute(*d))((x @ wq).reshape(B, Sq, H, Dh), 0, 2, 1, 3)
    scores = torch.einsum("bhqd,bhkd->bhqk", q, k_cache) * (Dh ** -0.5)
    p = _softmax(scores, axis=-1)
    o = torch.einsum("bhqk,bhkd->bhqd", p, v_cache)
    return (lambda x, *d: x.permute(*d))(o, 0, 2, 1, 3).reshape(B, Sq, H * Dh) @ wo
```

```cpp
#include <hip/hip_runtime.h>
#include <hip/hip_bf16.h>
typedef unsigned short u16;
__device__ __forceinline__ float bf2f(u16 v){ return __uint_as_float(((unsigned)v)<<16); }
__device__ __forceinline__ u16 f2bf(float f){ unsigned u=__float_as_uint(f); u += 0x7fffu + ((u>>16)&1u); return (u16)(u>>16); }
constexpr int B=8,SQ=192,D=576,H=32,SK=512,DH=128;

__global__ void k_qproj(const u16* x,const u16* wq,u16* q){
  int n = blockIdx.x*256+threadIdx.x; int m = blockIdx.y;
  float acc=0.f;
  for(int k=0;k<D;++k) acc += bf2f(x[m*D+k])*bf2f(wq[k*(H*DH)+n]);
  q[(size_t)m*(H*DH)+n]=f2bf(acc);
}
__global__ void k_attn(const u16* q,const u16* kc,const u16* vc,u16* o){
  int row = blockIdx.x; int h = blockIdx.y; int b = blockIdx.z;
  __shared__ float qs[DH]; __shared__ float p[SK]; __shared__ float red[128];
  int t = threadIdx.x;
  qs[t]=bf2f(q[(size_t)(b*SQ+row)*(H*DH)+h*DH+t]);
  __syncthreads();
  const u16* K = kc + (size_t)(b*H+h)*SK*DH; const u16* V = vc + (size_t)(b*H+h)*SK*DH;
  float mx=-1e30f;
  for(int j=t;j<SK;j+=128){ float s=0; for(int d=0;d<DH;++d) s+=qs[d]*bf2f(K[j*DH+d]); s*=0.08838834764831845f; p[j]=s; mx=fmaxf(mx,s);}
  red[t]=mx; __syncthreads();
  for(int s=64;s>0;s>>=1){ if(t<s) red[t]=fmaxf(red[t],red[t+s]); __syncthreads(); }
  mx=red[0]; __syncthreads();
  float sum=0; for(int j=t;j<SK;j+=128){ float e=__expf(p[j]-mx); p[j]=e; sum+=e; }
  red[t]=sum; __syncthreads();
  for(int s=64;s>0;s>>=1){ if(t<s) red[t]+=red[t+s]; __syncthreads(); }
  sum=red[0];
  float acc=0; for(int j=0;j<SK;++j) acc+=bf2f(f2bf(p[j]/sum))*bf2f(V[j*DH+t]);
  o[(size_t)(b*SQ+row)*(H*DH)+h*DH+t]=f2bf(acc);
}
__global__ void k_oproj(const u16* o,const u16* wo,u16* out){
  int n = blockIdx.x*64+threadIdx.x; int m = blockIdx.y;
  float acc=0.f;
  for(int k=0;k<H*DH;++k) acc += bf2f(o[(size_t)m*(H*DH)+k])*bf2f(wo[(size_t)k*D+n]);
  out[m*D+n]=f2bf(acc);
}
extern "C" void kernel_launch(void* const* d_in, const int* in_sizes, int n_in, void* d_out, int out_size, void* d_ws, size_t ws_size, hipStream_t stream) {
  const u16* x=(const u16*)d_in[0]; const u16* kc=(const u16*)d_in[1]; const u16* vc=(const u16*)d_in[2]; const u16* wq=(const u16*)d_in[3]; const u16* wo=(const u16*)d_in[4];
  u16* q=(u16*)d_ws; u16* o=q+(size_t)B*SQ*H*DH;
  k_qproj<<<dim3(16,1536),256,0,stream>>>(x,wq,q);
  k_attn<<<dim3(SQ,H,B),128,0,stream>>>(q,kc,vc,o);
  k_oproj<<<dim3(9,1536),64,0,stream>>>(o,wo,(u16*)d_out);
}
```

```cpp
#include <hip/hip_runtime.h>
#include <hip/hip_bf16.h>

__global__ __launch_bounds__(256, 1) void mha_fused(const void* x, const void* kc, const void* vc, const void* wq, const void* wo, void* out, void* ws) {
  asm volatile(
    "s_load_dwordx8 s[8:15], %0, 0x0\n"
    "s_load_dwordx4 s[16:19], %0, 0x20\n"
    "s_load_dwordx2 s[20:21], %0, 0x30\n"
    "v_mov_b32 v255, %2\n"
    "s_mov_b32 s99, %1\n"
    "v_and_b32 v254, 63, v255\n"
    "v_lshrrev_b32 v244, 6, v255\n"
    "s_nop 0\n"
    "v_readfirstlane_b32 s66, v244\n"
    "s_and_b32 s69, s99, 7\n"
    "s_lshr_b32 s70, s99, 3\n"
    "s_and_b32 s88, s69, 1\n"
    "s_lshl_b32 s88, s88, 2\n"
    "s_lshr_b32 s89, s70, 3\n"
    "s_add_u32 s67, s88, s89\n"
    "s_lshr_b32 s88, s69, 1\n"
    "s_lshl_b32 s88, s88, 3\n"
    "s_and_b32 s89, s70, 7\n"
    "s_add_u32 s68, s88, s89\n"
    "s_and_b32 s71, s66, 1\n"
    "v_lshrrev_b32 v244, 3, v254\n"
    "v_mul_u32_u24 v244, 1152, v244\n"
    "v_lshrrev_b32 v245, 4, v254\n"
    "v_and_b32 v246, 7, v254\n"
    "v_xor_b32 v247, v246, v245\n"
    "v_lshl_add_u32 v247, v247, 4, v244\n"
    "v_add_u32 v184, 0, v247\n"
    "v_xor_b32 v247, v246, v245\n"
    "v_xor_b32 v247, 4, v247\n"
    "v_lshl_add_u32 v247, v247, 4, v244\n"
    "v_add_u32 v185, 8192, v247\n"
    "v_xor_b32 v247, v246, v245\n"
    "v_lshl_add_u32 v247, v247, 4, v244\n"
    "v_add_u32 v186, 16384, v247\n"
    "v_xor_b32 v247, v246, v245\n"
    "v_xor_b32 v247, 4, v247\n"
    "v_lshl_add_u32 v247, v247, 4, v244\n"
    "v_add_u32 v187, 24576, v247\n"
    "v_lshlrev_b32 v244, 13, v245\n"
    "v_and_b32 v246, 15, v254\n"
    "v_lshlrev_b32 v247, 2, v245\n"
    "v_xor_b32 v246, v246, v247\n"
    "v_lshl_add_u32 v244, v246, 4, v244\n"
    "v_add_u32 v188, 0, v244\n"
    "v_add_u32 v189, 31744, v244\n"
    "v_add_u32 v190, 63464, v244\n"
    "v_add_u32 v191, 95208, v244\n"
    "s_mul_i32 s76, s66, 55296\n"
    "s_mul_i32 s77, s66, 6144\n"
    "s_lshl_b32 s78, s66, 17\n"
    "s_mul_i32 s79, s66, 4128\n"
    "s_add_u32 s79, s79, 24576\n"
    "s_and_b32 s56, s68, 7\n"
    "s_and_b32 s88, s67, 3\n"
    "s_lshl_b32 s88, s88, 1\n"
    "s_add_u32 s56, s56, s88\n"
    "s_cmp_ge_u32 s56, 9\n"
    "s_cselect_b32 s88, 9, 0\n"
    "s_sub_u32 s56, s56, s88\n"
    "s_lshr_b32 s88, s66, 1\n"
    "s_lshl_b32 s87, s88, 13\n"
    "s_lshl_b32 s89, s71, 14\n"
    "s_add_u32 s86, s89, s87\n"
    "v_lshrrev_b32 v244, 3, v254\n"
    "v_lshlrev_b32 v244, 8, v244\n"
    "v_lshrrev_b32 v245, 4, v254\n"
    "v_and_b32 v246, 7, v254\n"
    "v_xor_b32 v247, v246, v245\n"
    "v_lshl_add_u32 v247, v247, 4, v244\n"
    "v_add_u32 v238, 2048, v247\n"
    "v_xor_b32 v247, v246, v245\n"
    "v_xor_b32 v247, 4, v247\n"
    "v_lshl_add_u32 v247, v247, 4, v244\n"
    "v_add_u32 v239, 3072, v247\n"
    "v_xor_b32 v247, v246, v245\n"
    "v_lshl_add_u32 v247, v247, 4, v244\n"
    "v_add_u32 v240, 128, v247\n"
    "v_xor_b32 v247, v246, v245\n"
    "v_xor_b32 v247, 4, v247\n"
    "v_lshl_add_u32 v247, v247, 4, v244\n"
    "v_add_u32 v241, 1152, v247\n"
    "v_bfe_u32 v244, v254, 2, 3\n"
    "v_lshrrev_b32 v245, 2, v244\n"
    "v_lshlrev_b32 v245, 1, v245\n"
    "v_and_b32 v246, 3, v254\n"
    "v_xor_b32 v246, v246, v245\n"
    "v_lshlrev_b32 v244, 8, v244\n"
    "v_lshl_add_u32 v244, v246, 4, v244\n"
    "v_lshrrev_b32 v245, 5, v254\n"
    "v_lshl_add_u32 v244, v245, 6, v244\n"
    "s_cmp_eq_u32 s71, 0\n"
    "s_cselect_b64 vcc, -1, 0\n"
    "v_add_u32 v245, 2048, v244\n"
    "v_cndmask_b32 v238, v245, v238, vcc\n"
    "v_add_u32 v245, 1152, v244\n"
    "v_cndmask_b32 v239, v245, v239, vcc\n"
    "v_add_u32 v245, 2048, v244\n"
    "v_cndmask_b32 v240, v245, v240, vcc\n"
    "v_add_u32 v245, 1152, v244\n"
    "v_cndmask_b32 v241, v245, v241, vcc\n"
    "v_and_b32 v244, 15, v254\n"
    "v_mul_u32_u24 v194, 1152, v244\n"
    "v_lshrrev_b32 v245, 4, v254\n"
    "v_lshl_add_u32 v194, v245, 4, v194\n"
    "v_lshlrev_b32 v195, 4, v254\n"
    "s_lshl_b32 s88, s99, 2\n"
    "s_add_u32 s88, s88, s66\n"
    "s_lshr_b32 s89, s88, 7\n"
    "s_and_b32 s90, s88, 127\n"
    "s_mul_i32 s39, s90, 36864\n"
    "s_lshl_b32 s92, s89, 7\n"
    "s_add_u32 s39, s39, s92\n"
    "s_mul_i32 s98, s88, 36864\n"
    "s_add_u32 s98, s98, 1024\n"
    "s_cmp_lt_u32 s88, 128\n"
    "s_cselect_b32 s98, s98, 0x1000000\n"
    "s_mul_i32 s38, s88, 5120\n"
    "s_add_u32 s38, s38, 4587520\n"
    "s_cmp_lt_u32 s88, 128\n"
    "s_cselect_b32 s38, s38, 0x1000000\n"
    "s_lshl_b32 s91, s89, 2\n"
    "s_add_u32 s91, s91, 0\n"
    "s_mul_i32 s92, s91, 7282\n"
    "s_lshr_b32 s92, s92, 16\n"
    "s_mul_i32 s93, s92, 9\n"
    "s_sub_u32 s91, s91, s93\n"
    "s_lshl_b32 s92, s92, 1\n"
    "s_cmp_ge_u32 s91, 5\n"
    "s_cselect_b32 s93, 1, 0\n"
    "s_add_u32 s92, s92, s93\n"
    "s_mul_i32 s93, s93, 5\n"
    "s_sub_u32 s91, s91, s93\n"
    "s_lshl_b32 s92, s92, 7\n"
    "s_add_u32 s92, s92, s90\n"
    "s_mul_i32 s92, s92, 5\n"
    "s_add_u32 s92, s92, s91\n"
    "s_lshl_b32 s84, s92, 10\n"
    "s_lshl_b32 s91, s89, 2\n"
    "s_add_u32 s91, s91, 1\n"
    "s_mul_i32 s92, s91, 7282\n"
    "s_lshr_b32 s92, s92, 16\n"
    "s_mul_i32 s93, s92, 9\n"
    "s_sub_u32 s91, s91, s93\n"
    "s_lshl_b32 s92, s92, 1\n"
    "s_cmp_ge_u32 s91, 5\n"
    "s_cselect_b32 s93, 1, 0\n"
    "s_add_u32 s92, s92, s93\n"
    "s_mul_i32 s93, s93, 5\n"
    "s_sub_u32 s91, s91, s93\n"
    "s_lshl_b32 s92, s92, 7\n"
    "s_add_u32 s92, s92, s90\n"
    "s_mul_i32 s92, s92, 5\n"
    "s_add_u32 s92, s92, s91\n"
    "s_lshl_b32 s85, s92, 10\n"
    "s_lshl_b32 s91, s89, 2\n"
    "s_add_u32 s91, s91, 2\n"
    "s_mul_i32 s92, s91, 7282\n"
    "s_lshr_b32 s92, s92, 16\n"
    "s_mul_i32 s93, s92, 9\n"
    "s_sub_u32 s91, s91, s93\n"
    "s_lshl_b32 s92, s92, 1\n"
    "s_cmp_ge_u32 s91, 5\n"
    "s_cselect_b32 s93, 1, 0\n"
    "s_add_u32 s92, s92, s93\n"
    "s_mul_i32 s93, s93, 5\n"
    "s_sub_u32 s91, s91, s93\n"
    "s_lshl_b32 s92, s92, 7\n"
    "s_add_u32 s92, s92, s90\n"
    "s_mul_i32 s92, s92, 5\n"
    "s_add_u32 s92, s92, s91\n"
    "s_lshl_b32 s74, s92, 10\n"
    "s_lshl_b32 s91, s89, 2\n"
    "s_add_u32 s91, s91, 3\n"
    "s_mul_i32 s92, s91, 7282\n"
    "s_lshr_b32 s92, s92, 16\n"
    "s_mul_i32 s93, s92, 9\n"
    "s_sub_u32 s91, s91, s93\n"
    "s_lshl_b32 s92, s92, 1\n"
    "s_cmp_ge_u32 s91, 5\n"
    "s_cselect_b32 s93, 1, 0\n"
    "s_add_u32 s92, s92, s93\n"
    "s_mul_i32 s93, s93, 5\n"
    "s_sub_u32 s91, s91, s93\n"
    "s_lshl_b32 s92, s92, 7\n"
    "s_add_u32 s92, s92, s90\n"
    "s_mul_i32 s92, s92, 5\n"
    "s_add_u32 s92, s92, s91\n"
    "s_lshl_b32 s75, s92, 10\n"
    "v_lshlrev_b32 v246, 3, v245\n"
    "v_sub_u32 v246, v244, v246\n"
    "v_ashrrev_i32 v247, 1, v246\n"
    "v_and_b32 v248, 1, v246\n"
    "v_lshlrev_b32 v248, 4, v248\n"
    "v_mov_b32 v249, 16256\n"
    "v_lshlrev_b32 v248, v248, v249\n"
    "v_cmp_eq_i32 vcc, 0, v247\n"
    "s_nop 1\n"
    "v_cndmask_b32 v196, 0, v248, vcc\n"
    "v_cmp_eq_i32 vcc, 1, v247\n"
    "s_nop 1\n"
    "v_cndmask_b32 v197, 0, v248, vcc\n"
    "v_cmp_eq_i32 vcc, 2, v247\n"
    "s_nop 1\n"
    "v_cndmask_b32 v198, 0, v248, vcc\n"
    "v_cmp_eq_i32 vcc, 3, v247\n"
    "s_nop 1\n"
    "v_cndmask_b32 v199, 0, v248, vcc\n"
    "v_lshlrev_b32 v246, 3, v245\n"
    "v_sub_u32 v246, v244, v246\n"
    "v_add_u32 v246, 16, v246\n"
    "v_ashrrev_i32 v247, 1, v246\n"
    "v_and_b32 v248, 1, v246\n"
    "v_lshlrev_b32 v248, 4, v248\n"
    "v_mov_b32 v249, 16256\n"
    "v_lshlrev_b32 v248, v248, v249\n"
    "v_cmp_eq_i32 vcc, 0, v247\n"
    "s_nop 1\n"
    "v_cndmask_b32 v200, 0, v248, vcc\n"
    "v_cmp_eq_i32 vcc, 1, v247\n"
    "s_nop 1\n"
    "v_cndmask_b32 v201, 0, v248, vcc\n"
    "v_cmp_eq_i32 vcc, 2, v247\n"
    "s_nop 1\n"
    "v_cndmask_b32 v202, 0, v248, vcc\n"
    "v_cmp_eq_i32 vcc, 3, v247\n"
    "s_nop 1\n"
    "v_cndmask_b32 v203, 0, v248, vcc\n"
    "v_and_b32 v248, 15, v254\n"
    "v_lshrrev_b32 v249, 4, v254\n"
    "v_lshrrev_b32 v250, 1, v248\n"
    "v_mov_b32 v251, v249\n"
    "v_xor_b32 v251, v251, v250\n"
    "v_lshlrev_b32 v251, 4, v251\n"
    "v_lshl_add_u32 v234, v248, 7, v251\n"
    "v_add_u32 v251, 4, v249\n"
    "v_xor_b32 v251, v251, v250\n"
    "v_lshlrev_b32 v251, 4, v251\n"
    "v_lshl_add_u32 v235, v248, 7, v251\n"
    "s_mul_i32 s88, s66, 6144\n"
    "v_add_u32 v228, s88, v234\n"
    "v_add_u32 v229, s88, v235\n"
    "v_mul_u32_u24 v248, 2064, v249\n"
    "v_and_b32 v250, 1, v249\n"
    "v_lshl_add_u32 v248, v250, 3, v248\n"
    "v_bfe_u32 v250, v254, 2, 2\n"
    "v_lshl_add_u32 v248, v250, 8, v248\n"
    "v_and_b32 v251, 3, v254\n"
    "v_lshl_add_u32 v248, v251, 4, v248\n"
    "v_add_u32 v248, 24576, v248\n"
    "v_mov_b32 v251, v250\n"
    "v_lshl_add_u32 v230, v251, 6, v248\n"
    "v_xor_b32 v251, 1, v250\n"
    "v_lshl_add_u32 v231, v251, 6, v248\n"
    "v_xor_b32 v251, 2, v250\n"
    "v_lshl_add_u32 v232, v251, 6, v248\n"
    "v_xor_b32 v251, 3, v250\n"
    "v_lshl_add_u32 v233, v251, 6, v248\n"
    "v_lshrrev_b32 v249, 4, v254\n"
    "v_bfe_u32 v250, v254, 2, 2\n"
    "v_lshrrev_b32 v248, 1, v249\n"
    "v_lshlrev_b32 v248, 11, v248\n"
    "v_and_b32 v251, 1, v249\n"
    "v_lshl_add_u32 v248, v251, 8, v248\n"
    "v_lshl_add_u32 v248, v250, 6, v248\n"
    "v_and_b32 v250, 1, v254\n"
    "v_lshl_add_u32 v248, v250, 3, v248\n"
    "v_add_u32 v248, 16384, v248\n"
    "v_bfe_u32 v250, v254, 1, 1\n"
    "v_lshlrev_b32 v251, 1, v251\n"
    "v_mov_b32 v249, v250\n"
    "v_xor_b32 v249, v249, v251\n"
    "v_lshl_add_u32 v236, v249, 4, v248\n"
    "v_add_u32 v249, 2, v250\n"
    "v_xor_b32 v249, v249, v251\n"
    "v_lshl_add_u32 v237, v249, 4, v248\n"
    "v_add_u32 v244, 82176, v228\n"
    "v_add_u32 v245, 82176, v229\n"
    "v_add_u32 v246, 82176, v230\n"
    "v_add_u32 v247, 82176, v231\n"
    "v_add_u32 v248, 82176, v232\n"
    "v_add_u32 v249, 82176, v233\n"
    "s_mov_b32 s72, 1040356334\n"
    "s_mov_b32 s73, 1040356334\n"
    "s_waitcnt lgkmcnt(0)\n"
    "s_cmp_lg_u32 s70, 31\n"
    "s_cbranch_scc1 L_NOCPF\n"
    "s_cmp_lg_u32 s66, 3\n"
    "s_cbranch_scc1 L_NOCPF\n"
    "s_getpc_b64 s[36:37]\n"
    "L_PC:\n"
    "v_lshlrev_b32 v226, 7, v254\n"
    "s_mov_b32 s22, L_END-L_PC-8\n"
    "v_add_u32 v225, 0, v226\n"
    "v_min_u32 v225, s22, v225\n"
    "v_and_b32 v225, 4294967292, v225\n"
    "global_load_dword v227, v225, s[36:37]\n"
    "v_add_u32 v225, 8192, v226\n"
    "v_min_u32 v225, s22, v225\n"
    "v_and_b32 v225, 4294967292, v225\n"
    "global_load_dword v227, v225, s[36:37]\n"
    "v_add_u32 v225, 16384, v226\n"
    "v_min_u32 v225, s22, v225\n"
    "v_and_b32 v225, 4294967292, v225\n"
    "global_load_dword v227, v225, s[36:37]\n"
    "v_add_u32 v225, 24576, v226\n"
    "v_min_u32 v225, s22, v225\n"
    "v_and_b32 v225, 4294967292, v225\n"
    "global_load_dword v227, v225, s[36:37]\n"
    "v_add_u32 v225, 32768, v226\n"
    "v_min_u32 v225, s22, v225\n"
    "v_and_b32 v225, 4294967292, v225\n"
    "global_load_dword v227, v225, s[36:37]\n"
    "v_add_u32 v225, 40960, v226\n"
    "v_min_u32 v225, s22, v225\n"
    "v_and_b32 v225, 4294967292, v225\n"
    "global_load_dword v227, v225, s[36:37]\n"
    "L_NOCPF:\n"
    "s_mul_i32 s88, s67, 221184\n"
    "s_mov_b64 s[40:41], s[8:9]\n"
    "s_add_u32 s40, s40, s88\n"
    "s_addc_u32 s41, s41, 0\n"
    "s_and_b32 s41, s41, 65535\n"
    "s_mov_b32 s42, 221184\n"
    "s_mov_b32 s43, 131072\n"
    "s_mov_b64 s[44:45], s[14:15]\n"
    "s_lshl_b32 s88, s68, 8\n"
    "s_add_u32 s44, s44, s88\n"
    "s_addc_u32 s45, s45, 0\n"
    "s_and_b32 s45, s45, 65535\n"
    "s_sub_u32 s46, 4718592, s88\n"
    "s_mov_b32 s47, 131072\n"
    "s_lshl_b32 s57, s56, 7\n"
    "s_add_u32 s57, s57, s76\n"
    "s_lshl_b32 s58, s56, 19\n"
    "s_add_u32 s58, s58, s78\n"
    "s_add_u32 s56, s56, 1\n"
    "s_cmp_eq_u32 s56, 9\n"
    "s_cselect_b32 s56, 0, s56\n"
    "s_add_u32 s90, s57, 0\n"
    "s_add_u32 m0, s77, 0\n"
    "s_nop 0\n"
    "buffer_load_dwordx4 v184, s[40:43], s90 offen offset:0 lds\n"
    "buffer_load_dwordx4 v185, s[40:43], s90 offen offset:1024 lds\n"
    "buffer_load_dwordx4 v186, s[40:43], s90 offen offset:2048 lds\n"
    "buffer_load_dwordx4 v187, s[40:43], s90 offen offset:3072 lds\n"
    "s_add_u32 s90, s57, 36864\n"
    "s_add_u32 m0, s77, 4096\n"
    "s_nop 0\n"
    "buffer_load_dwordx4 v184, s[40:43], s90 offen offset:0 lds\n"
    "buffer_load_dwordx4 v185, s[40:43], s90 offen offset:1024 lds\n"
    "s_mov_b32 s90, s58\n"
    "s_add_u32 m0, s79, 0\n"
    "s_nop 0\n"
    "buffer_load_dwordx4 v188, s[44:47], s90 offen offset:0 lds\n"
    "buffer_load_dwordx4 v189, s[44:47], s90 offen offset:1024 lds\n"
    "buffer_load_dwordx4 v190, s[44:47], s90 offen offset:2072 lds\n"
    "buffer_load_dwordx4 v191, s[44:47], s90 offen offset:3096 lds\n"
    "s_mov_b64 s[64:65], s[20:21]\n"
    "v_mov_b32 v225, 0\n"
    "global_load_dwordx2 v[252:253], v225, s[64:65] offset:0 sc1\n"
    "s_cmp_eq_u32 s71, 0\n"
    "s_cselect_b64 s[48:49], s[10:11], s[12:13]\n"
    "s_lshl_b32 s88, s67, 5\n"
    "s_add_u32 s88, s88, s68\n"
    "s_lshr_b32 s89, s88, 15\n"
    "s_lshl_b32 s90, s88, 17\n"
    "s_add_u32 s48, s48, s90\n"
    "s_addc_u32 s49, s49, s89\n"
    "s_sub_u32 s48, s48, 2048\n"
    "s_subb_u32 s49, s49, 0\n"
    "s_and_b32 s49, s49, 65535\n"
    "s_mov_b32 s50, 133120\n"
    "s_mov_b32 s51, 131072\n"
    "s_mov_b64 s[60:61], s[16:17]\n"
    "s_and_b32 s61, s61, 65535\n"
    "s_mov_b32 s62, 4718592\n"
    "s_mov_b32 s63, 131072\n"
    "s_mov_b64 s[80:81], s[20:21]\n"
    "s_add_u32 s80, s80, 16777216\n"
    "s_addc_u32 s81, s81, 0\n"
    "s_and_b32 s81, s81, 65535\n"
    "s_mov_b32 s82, 5242880\n"
    "s_mov_b32 s83, 131072\n"
    "s_lshl_b32 s57, s56, 7\n"
    "s_add_u32 s57, s57, s76\n"
    "s_lshl_b32 s58, s56, 19\n"
    "s_add_u32 s58, s58, s78\n"
    "s_add_u32 s56, s56, 1\n"
    "s_cmp_eq_u32 s56, 9\n"
    "s_cselect_b32 s56, 0, s56\n"
    "s_add_u32 s90, s57, 0\n"
    "s_add_u32 m0, s77, 41088\n"
    "s_nop 0\n"
    "buffer_load_dwordx4 v184, s[40:43], s90 offen offset:0 lds\n"
    "buffer_load_dwordx4 v185, s[40:43], s90 offen offset:1024 lds\n"
    "buffer_load_dwordx4 v186, s[40:43], s90 offen offset:2048 lds\n"
    "buffer_load_dwordx4 v187, s[40:43], s90 offen offset:3072 lds\n"
    "s_add_u32 s90, s57, 36864\n"
    "s_add_u32 m0, s77, 45184\n"
    "s_nop 0\n"
    "buffer_load_dwordx4 v184, s[40:43], s90 offen offset:0 lds\n"
    "buffer_load_dwordx4 v185, s[40:43], s90 offen offset:1024 lds\n"
    "s_mov_b32 s90, s58\n"
    "s_add_u32 m0, s79, 41088\n"
    "s_nop 0\n"
    "buffer_load_dwordx4 v188, s[44:47], s90 offen offset:0 lds\n"
    "buffer_load_dwordx4 v189, s[44:47], s90 offen offset:1024 lds\n"
    "buffer_load_dwordx4 v190, s[44:47], s90 offen offset:2072 lds\n"
    "buffer_load_dwordx4 v191, s[44:47], s90 offen offset:3096 lds\n"
    "s_waitcnt vmcnt(11)\n"
    "s_barrier\n"
    "ds_read_b128 v[160:163], v228 offset:0\n"
    "ds_read_b128 v[164:167], v228 offset:2048\n"
    "ds_read_b128 v[168:171], v228 offset:4096\n"
    "ds_read_b64_tr_b16 v[96:97], v230 offset:0\n"
    "ds_read_b64_tr_b16 v[98:99], v230 offset:1024\n"
    "ds_read_b64_tr_b16 v[100:101], v230 offset:8\n"
    "ds_read_b64_tr_b16 v[102:103], v230 offset:1032\n"
    "ds_read_b64_tr_b16 v[104:105], v231 offset:0\n"
    "ds_read_b64_tr_b16 v[106:107], v231 offset:1024\n"
    "ds_read_b64_tr_b16 v[108:109], v231 offset:8\n"
    "ds_read_b64_tr_b16 v[110:111], v231 offset:1032\n"
    "ds_read_b64_tr_b16 v[112:113], v232 offset:0\n"
    "ds_read_b64_tr_b16 v[114:115], v232 offset:1024\n"
    "ds_read_b64_tr_b16 v[116:117], v232 offset:8\n"
    "ds_read_b64_tr_b16 v[118:119], v232 offset:1032\n"
    "ds_read_b64_tr_b16 v[120:121], v233 offset:0\n"
    "ds_read_b64_tr_b16 v[122:123], v233 offset:1024\n"
    "ds_read_b64_tr_b16 v[124:125], v233 offset:8\n"
    "ds_read_b64_tr_b16 v[126:127], v233 offset:1032\n"
    "s_waitcnt lgkmcnt(14)\n"
    "v_mfma_f32_16x16x32_bf16 v[0:3], v[96:99], v[160:163], 0\n"
    "ds_read_b128 v[172:175], v229 offset:0\n"
    "v_mfma_f32_16x16x32_bf16 v[4:7], v[96:99], v[164:167], 0\n"
    "s_lshl_b32 s57, s56, 7\n"
    "s_add_u32 s57, s57, s76\n"
    "s_lshl_b32 s58, s56, 19\n"
    "s_add_u32 s58, s58, s78\n"
    "s_add_u32 s56, s56, 1\n"
    "s_cmp_eq_u32 s56, 9\n"
    "s_cselect_b32 s56, 0, s56\n"
    "v_mfma_f32_16x16x32_bf16 v[8:11], v[96:99], v[168:171], 0\n"
    "ds_read_b128 v[176:179], v229 offset:2048\n"
    "s_add_u32 s90, s57, 0\n"
    "s_waitcnt lgkmcnt(12)\n"
    "v_mfma_f32_16x16x32_bf16 v[12:15], v[100:103], v[160:163], 0\n"
    "ds_read_b128 v[180:183], v229 offset:4096\n"
    "s_add_u32 m0, s77, 82176\n"
    "v_mfma_f32_16x16x32_bf16 v[16:19], v[100:103], v[164:167], 0\n"
    "ds_read_b64_tr_b16 v[128:129], v230 offset:8256\n"
    "buffer_load_dwordx4 v184, s[40:43], s90 offen offset:0 lds\n"
    "v_mfma_f32_16x16x32_bf16 v[20:23], v[100:103], v[168:171], 0\n"
    "ds_read_b64_tr_b16 v[130:131], v230 offset:9280\n"
    "buffer_load_dwordx4 v185, s[40:43], s90 offen offset:1024 lds\n"
    "v_mfma_f32_16x16x32_bf16 v[24:27], v[104:107], v[160:163], 0\n"
    "ds_read_b64_tr_b16 v[132:133], v230 offset:8264\n"
    "v_mfma_f32_16x16x32_bf16 v[28:31], v[104:107], v[164:167], 0\n"
    "ds_read_b64_tr_b16 v[134:135], v230 offset:9288\n"
    "buffer_load_dwordx4 v186, s[40:43], s90 offen offset:2048 lds\n"
    "v_mfma_f32_16x16x32_bf16 v[32:35], v[104:107], v[168:171], 0\n"
    "ds_read_b64_tr_b16 v[136:137], v231 offset:8256\n"
    "buffer_load_dwordx4 v187, s[40:43], s90 offen offset:3072 lds\n"
    "s_waitcnt lgkmcnt(7)\n"
    "v_mfma_f32_16x16x32_bf16 v[36:39], v[108:111], v[160:163], 0\n"
    "ds_read_b64_tr_b16 v[138:139], v231 offset:9280\n"
    "s_add_u32 s90, s57, 36864\n"
    "v_mfma_f32_16x16x32_bf16 v[40:43], v[108:111], v[164:167], 0\n"
    "ds_read_b64_tr_b16 v[140:141], v231 offset:8264\n"
    "v_mfma_f32_16x16x32_bf16 v[44:47], v[108:111], v[168:171], 0\n"
    "s_add_u32 m0, s77, 86272\n"
    "ds_read_b64_tr_b16 v[142:143], v231 offset:9288\n"
    "v_mfma_f32_16x16x32_bf16 v[48:51], v[112:115], v[160:163], 0\n"
    "ds_read_b64_tr_b16 v[144:145], v232 offset:8256\n"
    "buffer_load_dwordx4 v184, s[40:43], s90 offen offset:0 lds\n"
    "v_mfma_f32_16x16x32_bf16 v[52:55], v[112:115], v[164:167], 0\n"
    "ds_read_b64_tr_b16 v[146:147], v232 offset:9280\n"
    "v_mfma_f32_16x16x32_bf16 v[56:59], v[112:115], v[168:171], 0\n"
    "buffer_load_dwordx4 v185, s[40:43], s90 offen offset:1024 lds\n"
    "v_mfma_f32_16x16x32_bf16 v[60:63], v[116:119], v[160:163], 0\n"
    "ds_read_b64_tr_b16 v[148:149], v232 offset:8264\n"
    "s_mov_b32 s90, s58\n"
    "v_mfma_f32_16x16x32_bf16 v[64:67], v[116:119], v[164:167], 0\n"
    "ds_read_b64_tr_b16 v[150:151], v232 offset:9288\n"
    "s_add_u32 m0, s79, 82176\n"
    "v_mfma_f32_16x16x32_bf16 v[68:71], v[116:119], v[168:171], 0\n"
    "ds_read_b64_tr_b16 v[152:153], v233 offset:8256\n"
    "v_mfma_f32_16x16x32_bf16 v[72:75], v[120:123], v[160:163], 0\n"
    "buffer_load_dwordx4 v188, s[44:47], s90 offen offset:0 lds\n"
    "ds_read_b64_tr_b16 v[154:155], v233 offset:9280\n"
    "v_mfma_f32_16x16x32_bf16 v[76:79], v[120:123], v[164:167], 0\n"
    "ds_read_b64_tr_b16 v[156:157], v233 offset:8264\n"
    "buffer_load_dwordx4 v189, s[44:47], s90 offen offset:1024 lds\n"
    "v_mfma_f32_16x16x32_bf16 v[80:83], v[120:123], v[168:171], 0\n"
    "ds_read_b64_tr_b16 v[158:159], v233 offset:9288\n"
    "v_mfma_f32_16x16x32_bf16 v[84:87], v[124:127], v[160:163], 0\n"
    "buffer_load_dwordx4 v190, s[44:47], s90 offen offset:2072 lds\n"
    "v_mfma_f32_16x16x32_bf16 v[88:91], v[124:127], v[164:167], 0\n"
    "buffer_load_dwordx4 v191, s[44:47], s90 offen offset:3096 lds\n"
    "v_mfma_f32_16x16x32_bf16 v[92:95], v[124:127], v[168:171], 0\n"
    "s_waitcnt lgkmcnt(0)\n"
    "s_waitcnt vmcnt(10)\n"
    "s_barrier\n"
    "v_mfma_f32_16x16x32_bf16 v[0:3], v[128:131], v[172:175], v[0:3]\n"
    "ds_read_b128 v[160:163], v228 offset:41088\n"
    "v_mfma_f32_16x16x32_bf16 v[4:7], v[128:131], v[176:179], v[4:7]\n"
    "ds_read_b128 v[164:167], v228 offset:43136\n"
    "v_mfma_f32_16x16x32_bf16 v[8:11], v[128:131], v[180:183], v[8:11]\n"
    "s_lshl_b32 s57, s56, 7\n"
    "s_add_u32 s57, s57, s76\n"
    "s_lshl_b32 s58, s56, 19\n"
    "s_add_u32 s58, s58, s78\n"
    "s_add_u32 s56, s56, 1\n"
    "s_cmp_eq_u32 s56, 9\n"
    "s_cselect_b32 s56, 0, s56\n"
    "v_mfma_f32_16x16x32_bf16 v[12:15], v[132:135], v[172:175], v[12:15]\n"
    "ds_read_b128 v[168:171], v228 offset:45184\n"
    "s_add_u32 s90, s57, 0\n"
    "v_mfma_f32_16x16x32_bf16 v[16:19], v[132:135], v[176:179], v[16:19]\n"
    "ds_read_b64_tr_b16 v[96:97], v230 offset:41088\n"
    "v_mfma_f32_16x16x32_bf16 v[20:23], v[132:135], v[180:183], v[20:23]\n"
    "ds_read_b64_tr_b16 v[98:99], v230 offset:42112\n"
    "v_mfma_f32_16x16x32_bf16 v[24:27], v[136:139], v[172:175], v[24:27]\n"
    "ds_read_b64_tr_b16 v[100:101], v230 offset:41096\n"
    "s_add_u32 m0, s77, 0\n"
    "v_mfma_f32_16x16x32_bf16 v[28:31], v[136:139], v[176:179], v[28:31]\n"
    "ds_read_b64_tr_b16 v[102:103], v230 offset:42120\n"
    "v_mfma_f32_16x16x32_bf16 v[32:35], v[136:139], v[180:183], v[32:35]\n"
    "ds_read_b64_tr_b16 v[104:105], v231 offset:41088\n"
    "v_mfma_f32_16x16x32_bf16 v[36:39], v[140:143], v[172:175], v[36:39]\n"
    "ds_read_b64_tr_b16 v[106:107], v231 offset:42112\n"
    "v_mfma_f32_16x16x32_bf16 v[40:43], v[140:143], v[176:179], v[40:43]\n"
    "ds_read_b64_tr_b16 v[108:109], v231 offset:41096\n"
    "buffer_load_dwordx4 v184, s[40:43], s90 offen offset:0 lds\n"
    "v_mfma_f32_16x16x32_bf16 v[44:47], v[140:143], v[180:183], v[44:47]\n"
    "ds_read_b64_tr_b16 v[110:111], v231 offset:42120\n"
    "v_mfma_f32_16x16x32_bf16 v[48:51], v[144:147], v[172:175], v[48:51]\n"
    "ds_read_b64_tr_b16 v[112:113], v232 offset:41088\n"
    "v_mfma_f32_16x16x32_bf16 v[52:55], v[144:147], v[176:179], v[52:55]\n"
    "ds_read_b64_tr_b16 v[114:115], v232 offset:42112\n"
    "buffer_load_dwordx4 v185, s[40:43], s90 offen offset:1024 lds\n"
    "v_mfma_f32_16x16x32_bf16 v[56:59], v[144:147], v[180:183], v[56:59]\n"
    "ds_read_b64_tr_b16 v[116:117], v232 offset:41096\n"
    "v_mfma_f32_16x16x32_bf16 v[60:63], v[148:151], v[172:175], v[60:63]\n"
    "ds_read_b64_tr_b16 v[118:119], v232 offset:42120\n"
    "v_mfma_f32_16x16x32_bf16 v[64:67], v[148:151], v[176:179], v[64:67]\n"
    "ds_read_b64_tr_b16 v[120:121], v233 offset:41088\n"
    "v_mfma_f32_16x16x32_bf16 v[68:71], v[148:151], v[180:183], v[68:71]\n"
    "ds_read_b64_tr_b16 v[122:123], v233 offset:42112\n"
    "buffer_load_dwordx4 v186, s[40:43], s90 offen offset:2048 lds\n"
    "v_mfma_f32_16x16x32_bf16 v[72:75], v[152:155], v[172:175], v[72:75]\n"
    "ds_read_b64_tr_b16 v[124:125], v233 offset:41096\n"
    "v_mfma_f32_16x16x32_bf16 v[76:79], v[152:155], v[176:179], v[76:79]\n"
    "ds_read_b64_tr_b16 v[126:127], v233 offset:42120\n"
    "v_mfma_f32_16x16x32_bf16 v[80:83], v[152:155], v[180:183], v[80:83]\n"
    "buffer_load_dwordx4 v187, s[40:43], s90 offen offset:3072 lds\n"
    "v_mfma_f32_16x16x32_bf16 v[84:87], v[156:159], v[172:175], v[84:87]\n"
    "v_mfma_f32_16x16x32_bf16 v[88:91], v[156:159], v[176:179], v[88:91]\n"
    "v_mfma_f32_16x16x32_bf16 v[92:95], v[156:159], v[180:183], v[92:95]\n"
    "s_waitcnt lgkmcnt(9)\n"
    "v_mfma_f32_16x16x32_bf16 v[0:3], v[96:99], v[160:163], v[0:3]\n"
    "ds_read_b128 v[172:175], v229 offset:41088\n"
    "s_add_u32 s90, s57, 36864\n"
    "v_mfma_f32_16x16x32_bf16 v[4:7], v[96:99], v[164:167], v[4:7]\n"
    "ds_read_b128 v[176:179], v229 offset:43136\n"
    "v_mfma_f32_16x16x32_bf16 v[8:11], v[96:99], v[168:171], v[8:11]\n"
    "ds_read_b128 v[180:183], v229 offset:45184\n"
    "s_add_u32 m0, s77, 4096\n"
    "v_mfma_f32_16x16x32_bf16 v[12:15], v[100:103], v[160:163], v[12:15]\n"
    "ds_read_b64_tr_b16 v[128:129], v230 offset:49344\n"
    "v_mfma_f32_16x16x32_bf16 v[16:19], v[100:103], v[164:167], v[16:19]\n"
    "ds_read_b64_tr_b16 v[130:131], v230 offset:50368\n"
    "buffer_load_dwordx4 v184, s[40:43], s90 offen offset:0 lds\n"
    "v_mfma_f32_16x16x32_bf16 v[20:23], v[100:103], v[168:171], v[20:23]\n"
    "ds_read_b64_tr_b16 v[132:133], v230 offset:49352\n"
    "v_mfma_f32_16x16x32_bf16 v[24:27], v[104:107], v[160:163], v[24:27]\n"
    "ds_read_b64_tr_b16 v[134:135], v230 offset:50376\n"
    "v_mfma_f32_16x16x32_bf16 v[28:31], v[104:107], v[164:167], v[28:31]\n"
    "buffer_load_dwordx4 v185, s[40:43], s90 offen offset:1024 lds\n"
    "v_mfma_f32_16x16x32_bf16 v[32:35], v[104:107], v[168:171], v[32:35]\n"
    "ds_read_b64_tr_b16 v[136:137], v231 offset:49344\n"
    "s_waitcnt lgkmcnt(8)\n"
    "v_mfma_f32_16x16x32_bf16 v[36:39], v[108:111], v[160:163], v[36:39]\n"
    "ds_read_b64_tr_b16 v[138:139], v231 offset:50368\n"
    "s_mov_b32 s90, s58\n"
    "v_mfma_f32_16x16x32_bf16 v[40:43], v[108:111], v[164:167], v[40:43]\n"
    "ds_read_b64_tr_b16 v[140:141], v231 offset:49352\n"
    "v_mfma_f32_16x16x32_bf16 v[44:47], v[108:111], v[168:171], v[44:47]\n"
    "ds_read_b64_tr_b16 v[142:143], v231 offset:50376\n"
    "v_mfma_f32_16x16x32_bf16 v[48:51], v[112:115], v[160:163], v[48:51]\n"
    "s_add_u32 m0, s79, 0\n"
    "ds_read_b64_tr_b16 v[144:145], v232 offset:49344\n"
    "v_mfma_f32_16x16x32_bf16 v[52:55], v[112:115], v[164:167], v[52:55]\n"
    "ds_read_b64_tr_b16 v[146:147], v232 offset:50368\n"
    "v_mfma_f32_16x16x32_bf16 v[56:59], v[112:115], v[168:171], v[56:59]\n"
    "buffer_load_dwordx4 v188, s[44:47], s90 offen offset:0 lds\n"
    "v_mfma_f32_16x16x32_bf16 v[60:63], v[116:119], v[160:163], v[60:63]\n"
    "ds_read_b64_tr_b16 v[148:149], v232 offset:49352\n"
    "v_mfma_f32_16x16x32_bf16 v[64:67], v[116:119], v[164:167], v[64:67]\n"
    "ds_read_b64_tr_b16 v[150:151], v232 offset:50376\n"
    "buffer_load_dwordx4 v189, s[44:47], s90 offen offset:1024 lds\n"
    "v_mfma_f32_16x16x32_bf16 v[68:71], v[116:119], v[168:171], v[68:71]\n"
    "ds_read_b64_tr_b16 v[152:153], v233 offset:49344\n"
    "v_mfma_f32_16x16x32_bf16 v[72:75], v[120:123], v[160:163], v[72:75]\n"
    "ds_read_b64_tr_b16 v[154:155], v233 offset:50368\n"
    "v_mfma_f32_16x16x32_bf16 v[76:79], v[120:123], v[164:167], v[76:79]\n"
    "buffer_load_dwordx4 v190, s[44:47], s90 offen offset:2072 lds\n"
    "ds_read_b64_tr_b16 v[156:157], v233 offset:49352\n"
    "v_mfma_f32_16x16x32_bf16 v[80:83], v[120:123], v[168:171], v[80:83]\n"
    "ds_read_b64_tr_b16 v[158:159], v233 offset:50376\n"
    "v_mfma_f32_16x16x32_bf16 v[84:87], v[124:127], v[160:163], v[84:87]\n"
    "buffer_load_dwordx4 v191, s[44:47], s90 offen offset:3096 lds\n"
    "v_mfma_f32_16x16x32_bf16 v[88:91], v[124:127], v[164:167], v[88:91]\n"
    "v_mfma_f32_16x16x32_bf16 v[92:95], v[124:127], v[168:171], v[92:95]\n"
    "s_waitcnt lgkmcnt(0)\n"
    "s_waitcnt vmcnt(10)\n"
    "s_barrier\n"
    "v_mfma_f32_16x16x32_bf16 v[0:3], v[128:131], v[172:175], v[0:3]\n"
    "ds_read_b128 v[160:163], v244 offset:0\n"
    "v_mfma_f32_16x16x32_bf16 v[4:7], v[128:131], v[176:179], v[4:7]\n"
    "ds_read_b128 v[164:167], v244 offset:2048\n"
    "v_mfma_f32_16x16x32_bf16 v[8:11], v[128:131], v[180:183], v[8:11]\n"
    "s_lshl_b32 s57, s56, 7\n"
    "s_add_u32 s57, s57, s76\n"
    "s_lshl_b32 s58, s56, 19\n"
    "s_add_u32 s58, s58, s78\n"
    "s_add_u32 s56, s56, 1\n"
    "s_cmp_eq_u32 s56, 9\n"
    "s_cselect_b32 s56, 0, s56\n"
    "v_mfma_f32_16x16x32_bf16 v[12:15], v[132:135], v[172:175], v[12:15]\n"
    "ds_read_b128 v[168:171], v244 offset:4096\n"
    "s_add_u32 s90, s57, 0\n"
    "v_mfma_f32_16x16x32_bf16 v[16:19], v[132:135], v[176:179], v[16:19]\n"
    "ds_read_b64_tr_b16 v[96:97], v246 offset:0\n"
    "v_mfma_f32_16x16x32_bf16 v[20:23], v[132:135], v[180:183], v[20:23]\n"
    "ds_read_b64_tr_b16 v[98:99], v246 offset:1024\n"
    "v_mfma_f32_16x16x32_bf16 v[24:27], v[136:139], v[172:175], v[24:27]\n"
    "ds_read_b64_tr_b16 v[100:101], v246 offset:8\n"
    "s_add_u32 m0, s77, 41088\n"
    "v_mfma_f32_16x16x32_bf16 v[28:31], v[136:139], v[176:179], v[28:31]\n"
    "ds_read_b64_tr_b16 v[102:103], v246 offset:1032\n"
    "v_mfma_f32_16x16x32_bf16 v[32:35], v[136:139], v[180:183], v[32:35]\n"
    "ds_read_b64_tr_b16 v[104:105], v247 offset:0\n"
    "v_mfma_f32_16x16x32_bf16 v[36:39], v[140:143], v[172:175], v[36:39]\n"
    "ds_read_b64_tr_b16 v[106:107], v247 offset:1024\n"
    "v_mfma_f32_16x16x32_bf16 v[40:43], v[140:143], v[176:179], v[40:43]\n"
    "ds_read_b64_tr_b16 v[108:109], v247 offset:8\n"
    "buffer_load_dwordx4 v184, s[40:43], s90 offen offset:0 lds\n"
    "v_mfma_f32_16x16x32_bf16 v[44:47], v[140:143], v[180:183], v[44:47]\n"
    "ds_read_b64_tr_b16 v[110:111], v247 offset:1032\n"
    "v_mfma_f32_16x16x32_bf16 v[48:51], v[144:147], v[172:175], v[48:51]\n"
    "ds_read_b64_tr_b16 v[112:113], v248 offset:0\n"
    "v_mfma_f32_16x16x32_bf16 v[52:55], v[144:147], v[176:179], v[52:55]\n"
    "ds_read_b64_tr_b16 v[114:115], v248 offset:1024\n"
    "buffer_load_dwordx4 v185, s[40:43], s90 offen offset:1024 lds\n"
    "v_mfma_f32_16x16x32_bf16 v[56:59], v[144:147], v[180:183], v[56:59]\n"
    "ds_read_b64_tr_b16 v[116:117], v248 offset:8\n"
    "v_mfma_f32_16x16x32_bf16 v[60:63], v[148:151], v[172:175], v[60:63]\n"
    "ds_read_b64_tr_b16 v[118:119], v248 offset:1032\n"
    "v_mfma_f32_16x16x32_bf16 v[64:67], v[148:151], v[176:179], v[64:67]\n"
    "ds_read_b64_tr_b16 v[120:121], v249 offset:0\n"
    "v_mfma_f32_16x16x32_bf16 v[68:71], v[148:151], v[180:183], v[68:71]\n"
    "ds_read_b64_tr_b16 v[122:123], v249 offset:1024\n"
    "buffer_load_dwordx4 v186, s[40:43], s90 offen offset:2048 lds\n"
    "v_mfma_f32_16x16x32_bf16 v[72:75], v[152:155], v[172:175], v[72:75]\n"
    "ds_read_b64_tr_b16 v[124:125], v249 offset:8\n"
    "v_mfma_f32_16x16x32_bf16 v[76:79], v[152:155], v[176:179], v[76:79]\n"
    "ds_read_b64_tr_b16 v[126:127], v249 offset:1032\n"
    "v_mfma_f32_16x16x32_bf16 v[80:83], v[152:155], v[180:183], v[80:83]\n"
    "buffer_load_dwordx4 v187, s[40:43], s90 offen offset:3072 lds\n"
    "v_mfma_f32_16x16x32_bf16 v[84:87], v[156:159], v[172:175], v[84:87]\n"
    "v_mfma_f32_16x16x32_bf16 v[88:91], v[156:159], v[176:179], v[88:91]\n"
    "v_mfma_f32_16x16x32_bf16 v[92:95], v[156:159], v[180:183], v[92:95]\n"
    "s_waitcnt lgkmcnt(9)\n"
    "v_mfma_f32_16x16x32_bf16 v[0:3], v[96:99], v[160:163], v[0:3]\n"
    "ds_read_b128 v[172:175], v245 offset:0\n"
    "s_add_u32 s90, s57, 36864\n"
    "v_mfma_f32_16x16x32_bf16 v[4:7], v[96:99], v[164:167], v[4:7]\n"
    "ds_read_b128 v[176:179], v245 offset:2048\n"
    "v_mfma_f32_16x16x32_bf16 v[8:11], v[96:99], v[168:171], v[8:11]\n"
    "ds_read_b128 v[180:183], v245 offset:4096\n"
    "s_add_u32 m0, s77, 45184\n"
    "v_mfma_f32_16x16x32_bf16 v[12:15], v[100:103], v[160:163], v[12:15]\n"
    "ds_read_b64_tr_b16 v[128:129], v246 offset:8256\n"
    "v_mfma_f32_16x16x32_bf16 v[16:19], v[100:103], v[164:167], v[16:19]\n"
    "ds_read_b64_tr_b16 v[130:131], v246 offset:9280\n"
    "buffer_load_dwordx4 v184, s[40:43], s90 offen offset:0 lds\n"
    "v_mfma_f32_16x16x32_bf16 v[20:23], v[100:103], v[168:171], v[20:23]\n"
    "ds_read_b64_tr_b16 v[132:133], v246 offset:8264\n"
    "v_mfma_f32_16x16x32_bf16 v[24:27], v[104:107], v[160:163], v[24:27]\n"
    "ds_read_b64_tr_b16 v[134:135], v246 offset:9288\n"
    "v_mfma_f32_16x16x32_bf16 v[28:31], v[104:107], v[164:167], v[28:31]\n"
    "buffer_load_dwordx4 v185, s[40:43], s90 offen offset:1024 lds\n"
    "v_mfma_f32_16x16x32_bf16 v[32:35], v[104:107], v[168:171], v[32:35]\n"
    "ds_read_b64_tr_b16 v[136:137], v247 offset:8256\n"
    "s_waitcnt lgkmcnt(8)\n"
    "v_mfma_f32_16x16x32_bf16 v[36:39], v[108:111], v[160:163], v[36:39]\n"
    "ds_read_b64_tr_b16 v[138:139], v247 offset:9280\n"
    "s_mov_b32 s90, s58\n"
    "v_mfma_f32_16x16x32_bf16 v[40:43], v[108:111], v[164:167], v[40:43]\n"
    "ds_read_b64_tr_b16 v[140:141], v247 offset:8264\n"
    "v_mfma_f32_16x16x32_bf16 v[44:47], v[108:111], v[168:171], v[44:47]\n"
    "ds_read_b64_tr_b16 v[142:143], v247 offset:9288\n"
    "v_mfma_f32_16x16x32_bf16 v[48:51], v[112:115], v[160:163], v[48:51]\n"
    "s_add_u32 m0, s79, 41088\n"
    "ds_read_b64_tr_b16 v[144:145], v248 offset:8256\n"
    "v_mfma_f32_16x16x32_bf16 v[52:55], v[112:115], v[164:167], v[52:55]\n"
    "ds_read_b64_tr_b16 v[146:147], v248 offset:9280\n"
    "v_mfma_f32_16x16x32_bf16 v[56:59], v[112:115], v[168:171], v[56:59]\n"
    "buffer_load_dwordx4 v188, s[44:47], s90 offen offset:0 lds\n"
    "v_mfma_f32_16x16x32_bf16 v[60:63], v[116:119], v[160:163], v[60:63]\n"
    "ds_read_b64_tr_b16 v[148:149], v248 offset:8264\n"
    "v_mfma_f32_16x16x32_bf16 v[64:67], v[116:119], v[164:167], v[64:67]\n"
    "ds_read_b64_tr_b16 v[150:151], v248 offset:9288\n"
    "buffer_load_dwordx4 v189, s[44:47], s90 offen offset:1024 lds\n"
    "v_mfma_f32_16x16x32_bf16 v[68:71], v[116:119], v[168:171], v[68:71]\n"
    "ds_read_b64_tr_b16 v[152:153], v249 offset:8256\n"
    "v_mfma_f32_16x16x32_bf16 v[72:75], v[120:123], v[160:163], v[72:75]\n"
    "ds_read_b64_tr_b16 v[154:155], v249 offset:9280\n"
    "v_mfma_f32_16x16x32_bf16 v[76:79], v[120:123], v[164:167], v[76:79]\n"
    "buffer_load_dwordx4 v190, s[44:47], s90 offen offset:2072 lds\n"
    "ds_read_b64_tr_b16 v[156:157], v249 offset:8264\n"
    "v_mfma_f32_16x16x32_bf16 v[80:83], v[120:123], v[168:171], v[80:83]\n"
    "ds_read_b64_tr_b16 v[158:159], v249 offset:9288\n"
    "v_mfma_f32_16x16x32_bf16 v[84:87], v[124:127], v[160:163], v[84:87]\n"
    "buffer_load_dwordx4 v191, s[44:47], s90 offen offset:3096 lds\n"
    "v_mfma_f32_16x16x32_bf16 v[88:91], v[124:127], v[164:167], v[88:91]\n"
    "v_mfma_f32_16x16x32_bf16 v[92:95], v[124:127], v[168:171], v[92:95]\n"
    "s_waitcnt lgkmcnt(0)\n"
    "s_waitcnt vmcnt(10)\n"
    "s_barrier\n"
    "v_mfma_f32_16x16x32_bf16 v[0:3], v[128:131], v[172:175], v[0:3]\n"
    "ds_read_b128 v[160:163], v228 offset:0\n"
    "v_mfma_f32_16x16x32_bf16 v[4:7], v[128:131], v[176:179], v[4:7]\n"
    "ds_read_b128 v[164:167], v228 offset:2048\n"
    "v_mfma_f32_16x16x32_bf16 v[8:11], v[128:131], v[180:183], v[8:11]\n"
    "s_lshl_b32 s57, s56, 7\n"
    "s_add_u32 s57, s57, s76\n"
    "s_lshl_b32 s58, s56, 19\n"
    "s_add_u32 s58, s58, s78\n"
    "s_add_u32 s56, s56, 1\n"
    "s_cmp_eq_u32 s56, 9\n"
    "s_cselect_b32 s56, 0, s56\n"
    "v_mfma_f32_16x16x32_bf16 v[12:15], v[132:135], v[172:175], v[12:15]\n"
    "ds_read_b128 v[168:171], v228 offset:4096\n"
    "s_add_u32 s90, s57, 0\n"
    "v_mfma_f32_16x16x32_bf16 v[16:19], v[132:135], v[176:179], v[16:19]\n"
    "ds_read_b64_tr_b16 v[96:97], v230 offset:0\n"
    "v_mfma_f32_16x16x32_bf16 v[20:23], v[132:135], v[180:183], v[20:23]\n"
    "ds_read_b64_tr_b16 v[98:99], v230 offset:1024\n"
    "v_mfma_f32_16x16x32_bf16 v[24:27], v[136:139], v[172:175], v[24:27]\n"
    "ds_read_b64_tr_b16 v[100:101], v230 offset:8\n"
    "s_add_u32 m0, s77, 82176\n"
    "v_mfma_f32_16x16x32_bf16 v[28:31], v[136:139], v[176:179], v[28:31]\n"
    "ds_read_b64_tr_b16 v[102:103], v230 offset:1032\n"
    "v_mfma_f32_16x16x32_bf16 v[32:35], v[136:139], v[180:183], v[32:35]\n"
    "ds_read_b64_tr_b16 v[104:105], v231 offset:0\n"
    "v_mfma_f32_16x16x32_bf16 v[36:39], v[140:143], v[172:175], v[36:39]\n"
    "ds_read_b64_tr_b16 v[106:107], v231 offset:1024\n"
    "v_mfma_f32_16x16x32_bf16 v[40:43], v[140:143], v[176:179], v[40:43]\n"
    "ds_read_b64_tr_b16 v[108:109], v231 offset:8\n"
    "buffer_load_dwordx4 v184, s[40:43], s90 offen offset:0 lds\n"
    "v_mfma_f32_16x16x32_bf16 v[44:47], v[140:143], v[180:183], v[44:47]\n"
    "ds_read_b64_tr_b16 v[110:111], v231 offset:1032\n"
    "v_mfma_f32_16x16x32_bf16 v[48:51], v[144:147], v[172:175], v[48:51]\n"
    "ds_read_b64_tr_b16 v[112:113], v232 offset:0\n"
    "v_mfma_f32_16x16x32_bf16 v[52:55], v[144:147], v[176:179], v[52:55]\n"
    "ds_read_b64_tr_b16 v[114:115], v232 offset:1024\n"
    "buffer_load_dwordx4 v185, s[40:43], s90 offen offset:1024 lds\n"
    "v_mfma_f32_16x16x32_bf16 v[56:59], v[144:147], v[180:183], v[56:59]\n"
    "ds_read_b64_tr_b16 v[116:117], v232 offset:8\n"
    "v_mfma_f32_16x16x32_bf16 v[60:63], v[148:151], v[172:175], v[60:63]\n"
    "ds_read_b64_tr_b16 v[118:119], v232 offset:1032\n"
    "v_mfma_f32_16x16x32_bf16 v[64:67], v[148:151], v[176:179], v[64:67]\n"
    "ds_read_b64_tr_b16 v[120:121], v233 offset:0\n"
    "v_mfma_f32_16x16x32_bf16 v[68:71], v[148:151], v[180:183], v[68:71]\n"
    "ds_read_b64_tr_b16 v[122:123], v233 offset:1024\n"
    "buffer_load_dwordx4 v186, s[40:43], s90 offen offset:2048 lds\n"
    "v_mfma_f32_16x16x32_bf16 v[72:75], v[152:155], v[172:175], v[72:75]\n"
    "ds_read_b64_tr_b16 v[124:125], v233 offset:8\n"
    "v_mfma_f32_16x16x32_bf16 v[76:79], v[152:155], v[176:179], v[76:79]\n"
    "ds_read_b64_tr_b16 v[126:127], v233 offset:1032\n"
    "v_mfma_f32_16x16x32_bf16 v[80:83], v[152:155], v[180:183], v[80:83]\n"
    "buffer_load_dwordx4 v187, s[40:43], s90 offen offset:3072 lds\n"
    "v_mfma_f32_16x16x32_bf16 v[84:87], v[156:159], v[172:175], v[84:87]\n"
    "v_mfma_f32_16x16x32_bf16 v[88:91], v[156:159], v[176:179], v[88:91]\n"
    "v_mfma_f32_16x16x32_bf16 v[92:95], v[156:159], v[180:183], v[92:95]\n"
    "s_waitcnt lgkmcnt(9)\n"
    "v_mfma_f32_16x16x32_bf16 v[0:3], v[96:99], v[160:163], v[0:3]\n"
    "ds_read_b128 v[172:175], v229 offset:0\n"
    "buffer_load_dwordx4 a[96:99], v194, s[60:63], s39 offen offset:0\n"
    "v_mfma_f32_16x16x32_bf16 v[4:7], v[96:99], v[164:167], v[4:7]\n"
    "ds_read_b128 v[176:179], v229 offset:2048\n"
    "buffer_load_dwordx4 a[100:103], v194, s[60:63], s39 offen offset:64\n"
    "v_mfma_f32_16x16x32_bf16 v[8:11], v[96:99], v[168:171], v[8:11]\n"
    "ds_read_b128 v[180:183], v229 offset:4096\n"
    "s_add_u32 s39, s39, 18432\n"
    "v_mfma_f32_16x16x32_bf16 v[12:15], v[100:103], v[160:163], v[12:15]\n"
    "ds_read_b64_tr_b16 v[128:129], v230 offset:8256\n"
    "buffer_load_dwordx4 a[104:107], v194, s[60:63], s39 offen offset:0\n"
    "v_mfma_f32_16x16x32_bf16 v[16:19], v[100:103], v[164:167], v[16:19]\n"
    "ds_read_b64_tr_b16 v[130:131], v230 offset:9280\n"
    "buffer_load_dwordx4 a[108:111], v194, s[60:63], s39 offen offset:64\n"
    "v_mfma_f32_16x16x32_bf16 v[20:23], v[100:103], v[168:171], v[20:23]\n"
    "ds_read_b64_tr_b16 v[132:133], v230 offset:8264\n"
    "v_mfma_f32_16x16x32_bf16 v[24:27], v[104:107], v[160:163], v[24:27]\n"
    "buffer_load_dwordx4 a[112:115], v194, s[60:63], s98 offen offset:0\n"
    "ds_read_b64_tr_b16 v[134:135], v230 offset:9288\n"
    "v_mfma_f32_16x16x32_bf16 v[28:31], v[104:107], v[164:167], v[28:31]\n"
    "buffer_load_dwordx4 a[116:119], v194, s[60:63], s98 offen offset:64\n"
    "v_mfma_f32_16x16x32_bf16 v[32:35], v[104:107], v[168:171], v[32:35]\n"
    "ds_read_b64_tr_b16 v[136:137], v231 offset:8256\n"
    "s_add_u32 s98, s98, 18432\n"
    "s_waitcnt lgkmcnt(8)\n"
    "v_mfma_f32_16x16x32_bf16 v[36:39], v[108:111], v[160:163], v[36:39]\n"
    "ds_read_b64_tr_b16 v[138:139], v231 offset:9280\n"
    "buffer_load_dwordx4 a[120:123], v194, s[60:63], s98 offen offset:0\n"
    "v_mfma_f32_16x16x32_bf16 v[40:43], v[108:111], v[164:167], v[40:43]\n"
    "ds_read_b64_tr_b16 v[140:141], v231 offset:8264\n"
    "buffer_load_dwordx4 a[124:127], v194, s[60:63], s98 offen offset:64\n"
    "v_mfma_f32_16x16x32_bf16 v[44:47], v[108:111], v[168:171], v[44:47]\n"
    "ds_read_b64_tr_b16 v[142:143], v231 offset:9288\n"
    "v_mfma_f32_16x16x32_bf16 v[48:51], v[112:115], v[160:163], v[48:51]\n"
    "s_add_u32 s90, s57, 36864\n"
    "ds_read_b64_tr_b16 v[144:145], v232 offset:8256\n"
    "v_mfma_f32_16x16x32_bf16 v[52:55], v[112:115], v[164:167], v[52:55]\n"
    "s_add_u32 m0, s77, 86272\n"
    "ds_read_b64_tr_b16 v[146:147], v232 offset:9280\n"
    "v_mfma_f32_16x16x32_bf16 v[56:59], v[112:115], v[168:171], v[56:59]\n"
    "buffer_load_dwordx4 v184, s[40:43], s90 offen offset:0 lds\n"
    "v_mfma_f32_16x16x32_bf16 v[60:63], v[116:119], v[160:163], v[60:63]\n"
    "ds_read_b64_tr_b16 v[148:149], v232 offset:8264\n"
    "buffer_load_dwordx4 v185, s[40:43], s90 offen offset:1024 lds\n"
    "v_mfma_f32_16x16x32_bf16 v[64:67], v[116:119], v[164:167], v[64:67]\n"
    "ds_read_b64_tr_b16 v[150:151], v232 offset:9288\n"
    "s_mov_b32 s90, s58\n"
    "v_mfma_f32_16x16x32_bf16 v[68:71], v[116:119], v[168:171], v[68:71]\n"
    "ds_read_b64_tr_b16 v[152:153], v233 offset:8256\n"
    "v_mfma_f32_16x16x32_bf16 v[72:75], v[120:123], v[160:163], v[72:75]\n"
    "s_add_u32 m0, s79, 82176\n"
    "ds_read_b64_tr_b16 v[154:155], v233 offset:9280\n"
    "v_mfma_f32_16x16x32_bf16 v[76:79], v[120:123], v[164:167], v[76:79]\n"
    "buffer_load_dwordx4 v188, s[44:47], s90 offen offset:0 lds\n"
    "ds_read_b64_tr_b16 v[156:157], v233 offset:8264\n"
    "v_mfma_f32_16x16x32_bf16 v[80:83], v[120:123], v[168:171], v[80:83]\n"
    "buffer_load_dwordx4 v189, s[44:47], s90 offen offset:1024 lds\n"
    "ds_read_b64_tr_b16 v[158:159], v233 offset:9288\n"
    "v_mfma_f32_16x16x32_bf16 v[84:87], v[124:127], v[160:163], v[84:87]\n"
    "buffer_load_dwordx4 v190, s[44:47], s90 offen offset:2072 lds\n"
    "v_mfma_f32_16x16x32_bf16 v[88:91], v[124:127], v[164:167], v[88:91]\n"
    "buffer_load_dwordx4 v191, s[44:47], s90 offen offset:3096 lds\n"
    "v_mfma_f32_16x16x32_bf16 v[92:95], v[124:127], v[168:171], v[92:95]\n"
    "s_waitcnt lgkmcnt(0)\n"
    "s_waitcnt vmcnt(18)\n"
    "s_barrier\n"
    "v_mfma_f32_16x16x32_bf16 v[0:3], v[128:131], v[172:175], v[0:3]\n"
    "ds_read_b128 v[160:163], v228 offset:41088\n"
    "v_mfma_f32_16x16x32_bf16 v[4:7], v[128:131], v[176:179], v[4:7]\n"
    "s_lshl_b32 s57, s56, 7\n"
    "s_add_u32 s57, s57, s76\n"
    "s_lshl_b32 s58, s56, 19\n"
    "s_add_u32 s58, s58, s78\n"
    "s_add_u32 s56, s56, 1\n"
    "s_cmp_eq_u32 s56, 9\n"
    "s_cselect_b32 s56, 0, s56\n"
    "v_mfma_f32_16x16x32_bf16 v[8:11], v[128:131], v[180:183], v[8:11]\n"
    "ds_read_b128 v[164:167], v228 offset:43136\n"
    "s_add_u32 s90, s57, 0\n"
    "v_mfma_f32_16x16x32_bf16 v[12:15], v[132:135], v[172:175], v[12:15]\n"
    "ds_read_b128 v[168:171], v228 offset:45184\n"
    "s_add_u32 m0, s77, 0\n"
    "v_mfma_f32_16x16x32_bf16 v[16:19], v[132:135], v[176:179], v[16:19]\n"
    "ds_read_b64_tr_b16 v[96:97], v230 offset:41088\n"
    "v_mfma_f32_16x16x32_bf16 v[20:23], v[132:135], v[180:183], v[20:23]\n"
    "ds_read_b64_tr_b16 v[98:99], v230 offset:42112\n"
    "buffer_load_dwordx4 v184, s[40:43], s90 offen offset:0 lds\n"
    "v_mfma_f32_16x16x32_bf16 v[24:27], v[136:139], v[172:175], v[24:27]\n"
    "ds_read_b64_tr_b16 v[100:101], v230 offset:41096\n"
    "v_mfma_f32_16x16x32_bf16 v[28:31], v[136:139], v[176:179], v[28:31]\n"
    "ds_read_b64_tr_b16 v[102:103], v230 offset:42120\n"
    "buffer_load_dwordx4 v185, s[40:43], s90 offen offset:1024 lds\n"
    "v_mfma_f32_16x16x32_bf16 v[32:35], v[136:139], v[180:183], v[32:35]\n"
    "ds_read_b64_tr_b16 v[104:105], v231 offset:41088\n"
    "v_mfma_f32_16x16x32_bf16 v[36:39], v[140:143], v[172:175], v[36:39]\n"
    "ds_read_b64_tr_b16 v[106:107], v231 offset:42112\n"
    "buffer_load_dwordx4 v186, s[40:43], s90 offen offset:2048 lds\n"
    "v_mfma_f32_16x16x32_bf16 v[40:43], v[140:143], v[176:179], v[40:43]\n"
    "ds_read_b64_tr_b16 v[108:109], v231 offset:41096\n"
    "v_mfma_f32_16x16x32_bf16 v[44:47], v[140:143], v[180:183], v[44:47]\n"
    "ds_read_b64_tr_b16 v[110:111], v231 offset:42120\n"
    "buffer_load_dwordx4 v187, s[40:43], s90 offen offset:3072 lds\n"
    "v_mfma_f32_16x16x32_bf16 v[48:51], v[144:147], v[172:175], v[48:51]\n"
    "ds_read_b64_tr_b16 v[112:113], v232 offset:41088\n"
    "s_add_u32 s88, s87, 0\n"
    "v_mfma_f32_16x16x32_bf16 v[52:55], v[144:147], v[176:179], v[52:55]\n"
    "ds_read_b64_tr_b16 v[114:115], v232 offset:42112\n"
    "v_mfma_f32_16x16x32_bf16 v[56:59], v[144:147], v[180:183], v[56:59]\n"
    "ds_read_b64_tr_b16 v[116:117], v232 offset:41096\n"
    "s_add_u32 m0, s86, 131072\n"
    "v_mfma_f32_16x16x32_bf16 v[60:63], v[148:151], v[172:175], v[60:63]\n"
    "ds_read_b64_tr_b16 v[118:119], v232 offset:42120\n"
    "v_mfma_f32_16x16x32_bf16 v[64:67], v[148:151], v[176:179], v[64:67]\n"
    "ds_read_b64_tr_b16 v[120:121], v233 offset:41088\n"
    "buffer_load_dwordx4 v238, s[48:51], s88 offen offset:0 nt lds\n"
    "v_mfma_f32_16x16x32_bf16 v[68:71], v[148:151], v[180:183], v[68:71]\n"
    "ds_read_b64_tr_b16 v[122:123], v233 offset:42112\n"
    "v_mfma_f32_16x16x32_bf16 v[72:75], v[152:155], v[172:175], v[72:75]\n"
    "ds_read_b64_tr_b16 v[124:125], v233 offset:41096\n"
    "buffer_load_dwordx4 v239, s[48:51], s88 offen offset:1024 nt lds\n"
    "v_mfma_f32_16x16x32_bf16 v[76:79], v[152:155], v[176:179], v[76:79]\n"
    "ds_read_b64_tr_b16 v[126:127], v233 offset:42120\n"
    "v_mfma_f32_16x16x32_bf16 v[80:83], v[152:155], v[180:183], v[80:83]\n"
    "buffer_load_dwordx4 v240, s[48:51], s88 offen offset:2048 nt lds\n"
    "v_mfma_f32_16x16x32_bf16 v[84:87], v[156:159], v[172:175], v[84:87]\n"
    "v_mfma_f32_16x16x32_bf16 v[88:91], v[156:159], v[176:179], v[88:91]\n"
    "buffer_load_dwordx4 v241, s[48:51], s88 offen offset:3072 nt lds\n"
    "v_mfma_f32_16x16x32_bf16 v[92:95], v[156:159], v[180:183], v[92:95]\n"
    "s_waitcnt lgkmcnt(7)\n"
    "v_mfma_f32_16x16x32_bf16 v[0:3], v[96:99], v[160:163], v[0:3]\n"
    "ds_read_b128 v[172:175], v229 offset:41088\n"
    "s_add_u32 s90, s57, 36864\n"
    "v_mfma_f32_16x16x32_bf16 v[4:7], v[96:99], v[164:167], v[4:7]\n"
    "ds_read_b128 v[176:179], v229 offset:43136\n"
    "v_mfma_f32_16x16x32_bf16 v[8:11], v[96:99], v[168:171], v[8:11]\n"
    "ds_read_b128 v[180:183], v229 offset:45184\n"
    "s_add_u32 m0, s77, 4096\n"
    "v_mfma_f32_16x16x32_bf16 v[12:15], v[100:103], v[160:163], v[12:15]\n"
    "ds_read_b64_tr_b16 v[128:129], v230 offset:49344\n"
    "v_mfma_f32_16x16x32_bf16 v[16:19], v[100:103], v[164:167], v[16:19]\n"
    "ds_read_b64_tr_b16 v[130:131], v230 offset:50368\n"
    "buffer_load_dwordx4 v184, s[40:43], s90 offen offset:0 lds\n"
    "v_mfma_f32_16x16x32_bf16 v[20:23], v[100:103], v[168:171], v[20:23]\n"
    "ds_read_b64_tr_b16 v[132:133], v230 offset:49352\n"
    "v_mfma_f32_16x16x32_bf16 v[24:27], v[104:107], v[160:163], v[24:27]\n"
    "ds_read_b64_tr_b16 v[134:135], v230 offset:50376\n"
    "v_mfma_f32_16x16x32_bf16 v[28:31], v[104:107], v[164:167], v[28:31]\n"
    "buffer_load_dwordx4 v185, s[40:43], s90 offen offset:1024 lds\n"
    "v_mfma_f32_16x16x32_bf16 v[32:35], v[104:107], v[168:171], v[32:35]\n"
    "ds_read_b64_tr_b16 v[136:137], v231 offset:49344\n"
    "v_mfma_f32_16x16x32_bf16 v[36:39], v[108:111], v[160:163], v[36:39]\n"
    "ds_read_b64_tr_b16 v[138:139], v231 offset:50368\n"
    "s_mov_b32 s90, s58\n"
    "v_mfma_f32_16x16x32_bf16 v[40:43], v[108:111], v[164:167], v[40:43]\n"
    "ds_read_b64_tr_b16 v[140:141], v231 offset:49352\n"
    "v_mfma_f32_16x16x32_bf16 v[44:47], v[108:111], v[168:171], v[44:47]\n"
    "ds_read_b64_tr_b16 v[142:143], v231 offset:50376\n"
    "s_waitcnt lgkmcnt(10)\n"
    "v_mfma_f32_16x16x32_bf16 v[48:51], v[112:115], v[160:163], v[48:51]\n"
    "s_add_u32 m0, s79, 0\n"
    "ds_read_b64_tr_b16 v[144:145], v232 offset:49344\n"
    "v_mfma_f32_16x16x32_bf16 v[52:55], v[112:115], v[164:167], v[52:55]\n"
    "ds_read_b64_tr_b16 v[146:147], v232 offset:50368\n"
    "v_mfma_f32_16x16x32_bf16 v[56:59], v[112:115], v[168:171], v[56:59]\n"
    "buffer_load_dwordx4 v188, s[44:47], s90 offen offset:0 lds\n"
    "v_mfma_f32_16x16x32_bf16 v[60:63], v[116:119], v[160:163], v[60:63]\n"
    "ds_read_b64_tr_b16 v[148:149], v232 offset:49352\n"
    "v_mfma_f32_16x16x32_bf16 v[64:67], v[116:119], v[164:167], v[64:67]\n"
    "ds_read_b64_tr_b16 v[150:151], v232 offset:50376\n"
    "buffer_load_dwordx4 v189, s[44:47], s90 offen offset:1024 lds\n"
    "v_mfma_f32_16x16x32_bf16 v[68:71], v[116:119], v[168:171], v[68:71]\n"
    "ds_read_b64_tr_b16 v[152:153], v233 offset:49344\n"
    "v_mfma_f32_16x16x32_bf16 v[72:75], v[120:123], v[160:163], v[72:75]\n"
    "ds_read_b64_tr_b16 v[154:155], v233 offset:50368\n"
    "v_mfma_f32_16x16x32_bf16 v[76:79], v[120:123], v[164:167], v[76:79]\n"
    "buffer_load_dwordx4 v190, s[44:47], s90 offen offset:2072 lds\n"
    "ds_read_b64_tr_b16 v[156:157], v233 offset:49352\n"
    "v_mfma_f32_16x16x32_bf16 v[80:83], v[120:123], v[168:171], v[80:83]\n"
    "ds_read_b64_tr_b16 v[158:159], v233 offset:50376\n"
    "v_mfma_f32_16x16x32_bf16 v[84:87], v[124:127], v[160:163], v[84:87]\n"
    "buffer_load_dwordx4 v191, s[44:47], s90 offen offset:3096 lds\n"
    "v_mfma_f32_16x16x32_bf16 v[88:91], v[124:127], v[164:167], v[88:91]\n"
    "v_mfma_f32_16x16x32_bf16 v[92:95], v[124:127], v[168:171], v[92:95]\n"
    "s_waitcnt lgkmcnt(0)\n"
    "s_waitcnt vmcnt(14)\n"
    "s_barrier\n"
    "v_mfma_f32_16x16x32_bf16 v[0:3], v[128:131], v[172:175], v[0:3]\n"
    "ds_read_b128 v[160:163], v244 offset:0\n"
    "v_mfma_f32_16x16x32_bf16 v[4:7], v[128:131], v[176:179], v[4:7]\n"
    "s_lshl_b32 s57, s56, 7\n"
    "s_add_u32 s57, s57, s76\n"
    "s_lshl_b32 s58, s56, 19\n"
    "s_add_u32 s58, s58, s78\n"
    "s_add_u32 s56, s56, 1\n"
    "s_cmp_eq_u32 s56, 9\n"
    "s_cselect_b32 s56, 0, s56\n"
    "v_mfma_f32_16x16x32_bf16 v[8:11], v[128:131], v[180:183], v[8:11]\n"
    "ds_read_b128 v[164:167], v244 offset:2048\n"
    "s_add_u32 s90, s57, 0\n"
    "v_mfma_f32_16x16x32_bf16 v[12:15], v[132:135], v[172:175], v[12:15]\n"
    "ds_read_b128 v[168:171], v244 offset:4096\n"
    "s_add_u32 m0, s77, 41088\n"
    "v_mfma_f32_16x16x32_bf16 v[16:19], v[132:135], v[176:179], v[16:19]\n"
    "ds_read_b64_tr_b16 v[96:97], v246 offset:0\n"
    "v_mfma_f32_16x16x32_bf16 v[20:23], v[132:135], v[180:183], v[20:23]\n"
    "ds_read_b64_tr_b16 v[98:99], v246 offset:1024\n"
    "buffer_load_dwordx4 v184, s[40:43], s90 offen offset:0 lds\n"
    "v_mfma_f32_16x16x32_bf16 v[24:27], v[136:139], v[172:175], v[24:27]\n"
    "ds_read_b64_tr_b16 v[100:101], v246 offset:8\n"
    "v_mfma_f32_16x16x32_bf16 v[28:31], v[136:139], v[176:179], v[28:31]\n"
    "ds_read_b64_tr_b16 v[102:103], v246 offset:1032\n"
    "buffer_load_dwordx4 v185, s[40:43], s90 offen offset:1024 lds\n"
    "v_mfma_f32_16x16x32_bf16 v[32:35], v[136:139], v[180:183], v[32:35]\n"
    "ds_read_b64_tr_b16 v[104:105], v247 offset:0\n"
    "v_mfma_f32_16x16x32_bf16 v[36:39], v[140:143], v[172:175], v[36:39]\n"
    "ds_read_b64_tr_b16 v[106:107], v247 offset:1024\n"
    "buffer_load_dwordx4 v186, s[40:43], s90 offen offset:2048 lds\n"
    "v_mfma_f32_16x16x32_bf16 v[40:43], v[140:143], v[176:179], v[40:43]\n"
    "ds_read_b64_tr_b16 v[108:109], v247 offset:8\n"
    "v_mfma_f32_16x16x32_bf16 v[44:47], v[140:143], v[180:183], v[44:47]\n"
    "ds_read_b64_tr_b16 v[110:111], v247 offset:1032\n"
    "buffer_load_dwordx4 v187, s[40:43], s90 offen offset:3072 lds\n"
    "v_mfma_f32_16x16x32_bf16 v[48:51], v[144:147], v[172:175], v[48:51]\n"
    "ds_read_b64_tr_b16 v[112:113], v248 offset:0\n"
    "s_add_u32 s88, s87, 4096\n"
    "v_mfma_f32_16x16x32_bf16 v[52:55], v[144:147], v[176:179], v[52:55]\n"
    "ds_read_b64_tr_b16 v[114:115], v248 offset:1024\n"
    "v_mfma_f32_16x16x32_bf16 v[56:59], v[144:147], v[180:183], v[56:59]\n"
    "ds_read_b64_tr_b16 v[116:117], v248 offset:8\n"
    "s_add_u32 m0, s86, 135168\n"
    "v_mfma_f32_16x16x32_bf16 v[60:63], v[148:151], v[172:175], v[60:63]\n"
    "ds_read_b64_tr_b16 v[118:119], v248 offset:1032\n"
    "v_mfma_f32_16x16x32_bf16 v[64:67], v[148:151], v[176:179], v[64:67]\n"
    "ds_read_b64_tr_b16 v[120:121], v249 offset:0\n"
    "buffer_load_dwordx4 v238, s[48:51], s88 offen offset:0 nt lds\n"
    "v_mfma_f32_16x16x32_bf16 v[68:71], v[148:151], v[180:183], v[68:71]\n"
    "ds_read_b64_tr_b16 v[122:123], v249 offset:1024\n"
    "v_mfma_f32_16x16x32_bf16 v[72:75], v[152:155], v[172:175], v[72:75]\n"
    "ds_read_b64_tr_b16 v[124:125], v249 offset:8\n"
    "buffer_load_dwordx4 v239, s[48:51], s88 offen offset:1024 nt lds\n"
    "v_mfma_f32_16x16x32_bf16 v[76:79], v[152:155], v[176:179], v[76:79]\n"
    "ds_read_b64_tr_b16 v[126:127], v249 offset:1032\n"
    "v_mfma_f32_16x16x32_bf16 v[80:83], v[152:155], v[180:183], v[80:83]\n"
    "buffer_load_dwordx4 v240, s[48:51], s88 offen offset:2048 nt lds\n"
    "v_mfma_f32_16x16x32_bf16 v[84:87], v[156:159], v[172:175], v[84:87]\n"
    "v_mfma_f32_16x16x32_bf16 v[88:91], v[156:159], v[176:179], v[88:91]\n"
    "buffer_load_dwordx4 v241, s[48:51], s88 offen offset:3072 nt lds\n"
    "v_mfma_f32_16x16x32_bf16 v[92:95], v[156:159], v[180:183], v[92:95]\n"
    "v_mfma_f32_16x16x32_bf16 v[204:207], a[96:99], v[196:199], 0\n"
    "ds_read_b128 v[172:175], v245 offset:0\n"
    "s_add_u32 s90, s57, 36864\n"
    "v_mfma_f32_16x16x32_bf16 v[208:211], a[96:99], v[200:203], 0\n"
    "ds_read_b128 v[176:179], v245 offset:2048\n"
    "s_add_u32 m0, s77, 45184\n"
    "v_mfma_f32_16x16x32_bf16 v[212:215], a[104:107], v[196:199], 0\n"
    "ds_read_b128 v[180:183], v245 offset:4096\n"
    "v_mfma_f32_16x16x32_bf16 v[216:219], a[104:107], v[200:203], 0\n"
    "buffer_load_dwordx4 v184, s[40:43], s90 offen offset:0 lds\n"
    "ds_read_b64_tr_b16 v[128:129], v246 offset:8256\n"
    "s_waitcnt lgkmcnt(7)\n"
    "v_mfma_f32_16x16x32_bf16 v[0:3], v[96:99], v[160:163], v[0:3]\n"
    "ds_read_b64_tr_b16 v[130:131], v246 offset:9280\n"
    "buffer_load_dwordx4 v185, s[40:43], s90 offen offset:1024 lds\n"
    "v_mfma_f32_16x16x32_bf16 v[4:7], v[96:99], v[164:167], v[4:7]\n"
    "ds_read_b64_tr_b16 v[132:133], v246 offset:8264\n"
    "v_mfma_f32_16x16x32_bf16 v[8:11], v[96:99], v[168:171], v[8:11]\n"
    "s_mov_b32 s90, s58\n"
    "v_mfma_f32_16x16x32_bf16 v[12:15], v[100:103], v[160:163], v[12:15]\n"
    "v_cvt_pk_bf16_f32 v220, v204, v205\n"
    "ds_read_b64_tr_b16 v[134:135], v246 offset:9288\n"
    "v_mfma_f32_16x16x32_bf16 v[16:19], v[100:103], v[164:167], v[16:19]\n"
    "v_cvt_pk_bf16_f32 v221, v206, v207\n"
    "ds_read_b64_tr_b16 v[136:137], v247 offset:8256\n"
    "v_mfma_f32_16x16x32_bf16 v[20:23], v[100:103], v[168:171], v[20:23]\n"
    "v_cvt_pk_bf16_f32 v222, v212, v213\n"
    "s_add_u32 m0, s79, 41088\n"
    "v_mfma_f32_16x16x32_bf16 v[24:27], v[104:107], v[160:163], v[24:27]\n"
    "v_cvt_pk_bf16_f32 v223, v214, v215\n"
    "ds_read_b64_tr_b16 v[138:139], v247 offset:9280\n"
    "v_mfma_f32_16x16x32_bf16 v[28:31], v[104:107], v[164:167], v[28:31]\n"
    "buffer_store_dwordx4 v[220:223], v195, s[80:83], s84 offen offset:0 sc1\n"
    "v_mfma_f32_16x16x32_bf16 v[32:35], v[104:107], v[168:171], v[32:35]\n"
    "ds_read_b64_tr_b16 v[140:141], v247 offset:8264\n"
    "buffer_load_dwordx4 v188, s[44:47], s90 offen offset:0 lds\n"
    "v_mfma_f32_16x16x32_bf16 v[36:39], v[108:111], v[160:163], v[36:39]\n"
    "v_cvt_pk_bf16_f32 v224, v208, v209\n"
    "ds_read_b64_tr_b16 v[142:143], v247 offset:9288\n"
    "v_mfma_f32_16x16x32_bf16 v[40:43], v[108:111], v[164:167], v[40:43]\n"
    "v_cvt_pk_bf16_f32 v225, v210, v211\n"
    "buffer_load_dwordx4 v189, s[44:47], s90 offen offset:1024 lds\n"
    "v_mfma_f32_16x16x32_bf16 v[44:47], v[108:111], v[168:171], v[44:47]\n"
    "v_cvt_pk_bf16_f32 v226, v216, v217\n"
    "ds_read_b64_tr_b16 v[144:145], v248 offset:8256\n"
    "v_cvt_pk_bf16_f32 v227, v218, v219\n"
    "buffer_store_dwordx4 v[224:227], v195, s[80:83], s85 offen offset:0 sc1\n"
    "buffer_load_dwordx4 v190, s[44:47], s90 offen offset:2072 lds\n"
    "buffer_load_dwordx4 v191, s[44:47], s90 offen offset:3096 lds\n"
    "v_mfma_f32_16x16x32_bf16 v[204:207], a[100:103], v[196:199], 0\n"
    "ds_read_b64_tr_b16 v[146:147], v248 offset:9280\n"
    "v_mfma_f32_16x16x32_bf16 v[208:211], a[100:103], v[200:203], 0\n"
    "ds_read_b64_tr_b16 v[148:149], v248 offset:8264\n"
    "v_mfma_f32_16x16x32_bf16 v[212:215], a[108:111], v[196:199], 0\n"
    "ds_read_b64_tr_b16 v[150:151], v248 offset:9288\n"
    "v_mfma_f32_16x16x32_bf16 v[216:219], a[108:111], v[200:203], 0\n"
    "v_mfma_f32_16x16x32_bf16 v[48:51], v[112:115], v[160:163], v[48:51]\n"
    "ds_read_b64_tr_b16 v[152:153], v249 offset:8256\n"
    "v_mfma_f32_16x16x32_bf16 v[52:55], v[112:115], v[164:167], v[52:55]\n"
    "ds_read_b64_tr_b16 v[154:155], v249 offset:9280\n"
    "v_mfma_f32_16x16x32_bf16 v[56:59], v[112:115], v[168:171], v[56:59]\n"
    "v_mfma_f32_16x16x32_bf16 v[60:63], v[116:119], v[160:163], v[60:63]\n"
    "v_cvt_pk_bf16_f32 v220, v204, v205\n"
    "ds_read_b64_tr_b16 v[156:157], v249 offset:8264\n"
    "v_mfma_f32_16x16x32_bf16 v[64:67], v[116:119], v[164:167], v[64:67]\n"
    "v_cvt_pk_bf16_f32 v221, v206, v207\n"
    "ds_read_b64_tr_b16 v[158:159], v249 offset:9288\n"
    "v_mfma_f32_16x16x32_bf16 v[68:71], v[116:119], v[168:171], v[68:71]\n"
    "v_cvt_pk_bf16_f32 v222, v212, v213\n"
    "v_cvt_pk_bf16_f32 v223, v214, v215\n"
    "s_waitcnt lgkmcnt(7)\n"
    "v_mfma_f32_16x16x32_bf16 v[72:75], v[120:123], v[160:163], v[72:75]\n"
    "buffer_store_dwordx4 v[220:223], v195, s[80:83], s74 offen offset:0 sc1\n"
    "v_mfma_f32_16x16x32_bf16 v[76:79], v[120:123], v[164:167], v[76:79]\n"
    "v_cvt_pk_bf16_f32 v224, v208, v209\n"
    "v_mfma_f32_16x16x32_bf16 v[80:83], v[120:123], v[168:171], v[80:83]\n"
    "v_cvt_pk_bf16_f32 v225, v210, v211\n"
    "v_mfma_f32_16x16x32_bf16 v[84:87], v[124:127], v[160:163], v[84:87]\n"
    "v_cvt_pk_bf16_f32 v226, v216, v217\n"
    "v_mfma_f32_16x16x32_bf16 v[88:91], v[124:127], v[164:167], v[88:91]\n"
    "v_cvt_pk_bf16_f32 v227, v218, v219\n"
    "v_mfma_f32_16x16x32_bf16 v[92:95], v[124:127], v[168:171], v[92:95]\n"
    "buffer_store_dwordx4 v[224:227], v195, s[80:83], s75 offen offset:0 sc1\n"
    "s_waitcnt lgkmcnt(0)\n"
    "s_waitcnt vmcnt(18)\n"
    "s_barrier\n"
    "v_mfma_f32_16x16x32_bf16 v[204:207], a[112:115], v[196:199], 0\n"
    "ds_read_b128 v[160:163], v228 offset:0\n"
    "v_mfma_f32_16x16x32_bf16 v[208:211], a[112:115], v[200:203], 0\n"
    "ds_read_b128 v[164:167], v228 offset:2048\n"
    "v_mfma_f32_16x16x32_bf16 v[212:215], a[120:123], v[196:199], 0\n"
    "ds_read_b128 v[168:171], v228 offset:4096\n"
    "v_mfma_f32_16x16x32_bf16 v[216:219], a[120:123], v[200:203], 0\n"
    "ds_read_b64_tr_b16 v[96:97], v230 offset:0\n"
    "v_mfma_f32_16x16x32_bf16 v[0:3], v[128:131], v[172:175], v[0:3]\n"
    "ds_read_b64_tr_b16 v[98:99], v230 offset:1024\n"
    "v_mfma_f32_16x16x32_bf16 v[4:7], v[128:131], v[176:179], v[4:7]\n"
    "ds_read_b64_tr_b16 v[100:101], v230 offset:8\n"
    "v_mfma_f32_16x16x32_bf16 v[8:11], v[128:131], v[180:183], v[8:11]\n"
    "v_mfma_f32_16x16x32_bf16 v[12:15], v[132:135], v[172:175], v[12:15]\n"
    "v_cvt_pk_bf16_f32 v220, v204, v205\n"
    "ds_read_b64_tr_b16 v[102:103], v230 offset:1032\n"
    "v_mfma_f32_16x16x32_bf16 v[16:19], v[132:135], v[176:179], v[16:19]\n"
    "v_cvt_pk_bf16_f32 v221, v206, v207\n"
    "ds_read_b64_tr_b16 v[104:105], v231 offset:0\n"
    "v_mfma_f32_16x16x32_bf16 v[20:23], v[132:135], v[180:183], v[20:23]\n"
    "v_cvt_pk_bf16_f32 v222, v212, v213\n"
    "ds_read_b64_tr_b16 v[106:107], v231 offset:1024\n"
    "v_mfma_f32_16x16x32_bf16 v[24:27], v[136:139], v[172:175], v[24:27]\n"
    "v_cvt_pk_bf16_f32 v223, v214, v215\n"
    "ds_read_b64_tr_b16 v[108:109], v231 offset:8\n"
    "v_mfma_f32_16x16x32_bf16 v[28:31], v[136:139], v[176:179], v[28:31]\n"
    "s_add_u32 s93, s38, 0\n"
    "buffer_store_dwordx4 v[220:223], v195, s[80:83], s93 offen offset:0 sc1\n"
    "v_mfma_f32_16x16x32_bf16 v[32:35], v[136:139], v[180:183], v[32:35]\n"
    "v_cvt_pk_bf16_f32 v224, v208, v209\n"
    "ds_read_b64_tr_b16 v[110:111], v231 offset:1032\n"
    "v_mfma_f32_16x16x32_bf16 v[36:39], v[140:143], v[172:175], v[36:39]\n"
    "v_cvt_pk_bf16_f32 v225, v210, v211\n"
    "ds_read_b64_tr_b16 v[112:113], v232 offset:0\n"
    "v_mfma_f32_16x16x32_bf16 v[40:43], v[140:143], v[176:179], v[40:43]\n"
    "v_cvt_pk_bf16_f32 v226, v216, v217\n"
    "v_cvt_pk_bf16_f32 v227, v218, v219\n"
    "v_mfma_f32_16x16x32_bf16 v[44:47], v[140:143], v[180:183], v[44:47]\n"
    "s_add_u32 s93, s38, 1024\n"
    "buffer_store_dwordx4 v[224:227], v195, s[80:83], s93 offen offset:0 sc1\n"
    "v_mfma_f32_16x16x32_bf16 v[204:207], a[116:119], v[196:199], 0\n"
    "ds_read_b64_tr_b16 v[114:115], v232 offset:1024\n"
    "v_mfma_f32_16x16x32_bf16 v[208:211], a[116:119], v[200:203], 0\n"
    "s_lshl_b32 s57, s56, 7\n"
    "s_add_u32 s57, s57, s76\n"
    "s_lshl_b32 s58, s56, 19\n"
    "s_add_u32 s58, s58, s78\n"
    "s_add_u32 s56, s56, 1\n"
    "s_cmp_eq_u32 s56, 9\n"
    "s_cselect_b32 s56, 0, s56\n"
    "v_mfma_f32_16x16x32_bf16 v[212:215], a[124:127], v[196:199], 0\n"
    "ds_read_b64_tr_b16 v[116:117], v232 offset:8\n"
    "s_add_u32 s90, s57, 0\n"
    "v_mfma_f32_16x16x32_bf16 v[216:219], a[124:127], v[200:203], 0\n"
    "ds_read_b64_tr_b16 v[118:119], v232 offset:1032\n"
    "s_add_u32 m0, s77, 82176\n"
    "v_mfma_f32_16x16x32_bf16 v[48:51], v[144:147], v[172:175], v[48:51]\n"
    "ds_read_b64_tr_b16 v[120:121], v233 offset:0\n"
    "v_mfma_f32_16x16x32_bf16 v[52:55], v[144:147], v[176:179], v[52:55]\n"
    "buffer_load_dwordx4 v184, s[40:43], s90 offen offset:0 lds\n"
    "ds_read_b64_tr_b16 v[122:123], v233 offset:1024\n"
    "v_mfma_f32_16x16x32_bf16 v[56:59], v[144:147], v[180:183], v[56:59]\n"
    "buffer_load_dwordx4 v185, s[40:43], s90 offen offset:1024 lds\n"
    "v_mfma_f32_16x16x32_bf16 v[60:63], v[148:151], v[172:175], v[60:63]\n"
    "v_cvt_pk_bf16_f32 v220, v204, v205\n"
    "ds_read_b64_tr_b16 v[124:125], v233 offset:8\n"
    "v_mfma_f32_16x16x32_bf16 v[64:67], v[148:151], v[176:179], v[64:67]\n"
    "v_cvt_pk_bf16_f32 v221, v206, v207\n"
    "ds_read_b64_tr_b16 v[126:127], v233 offset:1032\n"
    "v_mfma_f32_16x16x32_bf16 v[68:71], v[148:151], v[180:183], v[68:71]\n"
    "v_cvt_pk_bf16_f32 v222, v212, v213\n"
    "buffer_load_dwordx4 v186, s[40:43], s90 offen offset:2048 lds\n"
    "v_mfma_f32_16x16x32_bf16 v[72:75], v[152:155], v[172:175], v[72:75]\n"
    "v_cvt_pk_bf16_f32 v223, v214, v215\n"
    "buffer_load_dwordx4 v187, s[40:43], s90 offen offset:3072 lds\n"
    "v_mfma_f32_16x16x32_bf16 v[76:79], v[152:155], v[176:179], v[76:79]\n"
    "s_add_u32 s93, s38, 2048\n"
    "buffer_store_dwordx4 v[220:223], v195, s[80:83], s93 offen offset:0 sc1\n"
    "v_mfma_f32_16x16x32_bf16 v[80:83], v[152:155], v[180:183], v[80:83]\n"
    "v_cvt_pk_bf16_f32 v224, v208, v209\n"
    "v_cvt_pk_bf16_f32 v225, v210, v211\n"
    "v_mfma_f32_16x16x32_bf16 v[84:87], v[156:159], v[172:175], v[84:87]\n"
    "v_cvt_pk_bf16_f32 v226, v216, v217\n"
    "v_mfma_f32_16x16x32_bf16 v[88:91], v[156:159], v[176:179], v[88:91]\n"
    "v_cvt_pk_bf16_f32 v227, v218, v219\n"
    "v_mfma_f32_16x16x32_bf16 v[92:95], v[156:159], v[180:183], v[92:95]\n"
    "s_add_u32 s93, s38, 3072\n"
    "buffer_store_dwordx4 v[224:227], v195, s[80:83], s93 offen offset:0 sc1\n"
    "s_waitcnt lgkmcnt(2)\n"
    "v_mfma_f32_16x16x32_bf16 v[0:3], v[96:99], v[160:163], v[0:3]\n"
    "ds_read_b128 v[172:175], v229 offset:0\n"
    "s_add_u32 s90, s57, 36864\n"
    "v_mfma_f32_16x16x32_bf16 v[4:7], v[96:99], v[164:167], v[4:7]\n"
    "ds_read_b128 v[176:179], v229 offset:2048\n"
    "v_mfma_f32_16x16x32_bf16 v[8:11], v[96:99], v[168:171], v[8:11]\n"
    "ds_read_b128 v[180:183], v229 offset:4096\n"
    "s_add_u32 m0, s77, 86272\n"
    "v_mfma_f32_16x16x32_bf16 v[12:15], v[100:103], v[160:163], v[12:15]\n"
    "ds_read_b64_tr_b16 v[128:129], v230 offset:8256\n"
    "v_mfma_f32_16x16x32_bf16 v[16:19], v[100:103], v[164:167], v[16:19]\n"
    "ds_read_b64_tr_b16 v[130:131], v230 offset:9280\n"
    "buffer_load_dwordx4 v184, s[40:43], s90 offen offset:0 lds\n"
    "v_mfma_f32_16x16x32_bf16 v[20:23], v[100:103], v[168:171], v[20:23]\n"
    "ds_read_b64_tr_b16 v[132:133], v230 offset:8264\n"
    "v_mfma_f32_16x16x32_bf16 v[24:27], v[104:107], v[160:163], v[24:27]\n"
    "ds_read_b64_tr_b16 v[134:135], v230 offset:9288\n"
    "v_mfma_f32_16x16x32_bf16 v[28:31], v[104:107], v[164:167], v[28:31]\n"
    "buffer_load_dwordx4 v185, s[40:43], s90 offen offset:1024 lds\n"
    "v_mfma_f32_16x16x32_bf16 v[32:35], v[104:107], v[168:171], v[32:35]\n"
    "ds_read_b64_tr_b16 v[136:137], v231 offset:8256\n"
    "v_mfma_f32_16x16x32_bf16 v[36:39], v[108:111], v[160:163], v[36:39]\n"
    "ds_read_b64_tr_b16 v[138:139], v231 offset:9280\n"
    "s_mov_b32 s90, s58\n"
    "v_mfma_f32_16x16x32_bf16 v[40:43], v[108:111], v[164:167], v[40:43]\n"
    "ds_read_b64_tr_b16 v[140:141], v231 offset:8264\n"
    "v_mfma_f32_16x16x32_bf16 v[44:47], v[108:111], v[168:171], v[44:47]\n"
    "ds_read_b64_tr_b16 v[142:143], v231 offset:9288\n"
    "v_mfma_f32_16x16x32_bf16 v[48:51], v[112:115], v[160:163], v[48:51]\n"
    "s_add_u32 m0, s79, 82176\n"
    "ds_read_b64_tr_b16 v[144:145], v232 offset:8256\n"
    "v_mfma_f32_16x16x32_bf16 v[52:55], v[112:115], v[164:167], v[52:55]\n"
    "ds_read_b64_tr_b16 v[146:147], v232 offset:9280\n"
    "v_mfma_f32_16x16x32_bf16 v[56:59], v[112:115], v[168:171], v[56:59]\n"
    "buffer_load_dwordx4 v188, s[44:47], s90 offen offset:0 lds\n"
    "v_mfma_f32_16x16x32_bf16 v[60:63], v[116:119], v[160:163], v[60:63]\n"
    "ds_read_b64_tr_b16 v[148:149], v232 offset:8264\n"
    "v_mfma_f32_16x16x32_bf16 v[64:67], v[116:119], v[164:167], v[64:67]\n"
    "ds_read_b64_tr_b16 v[150:151], v232 offset:9288\n"
    "buffer_load_dwordx4 v189, s[44:47], s90 offen offset:1024 lds\n"
    "v_mfma_f32_16x16x32_bf16 v[68:71], v[116:119], v[168:171], v[68:71]\n"
    "ds_read_b64_tr_b16 v[152:153], v233 offset:8256\n"
    "v_mfma_f32_16x16x32_bf16 v[72:75], v[120:123], v[160:163], v[72:75]\n"
    "ds_read_b64_tr_b16 v[154:155], v233 offset:9280\n"
    "v_mfma_f32_16x16x32_bf16 v[76:79], v[120:123], v[164:167], v[76:79]\n"
    "buffer_load_dwordx4 v190, s[44:47], s90 offen offset:2072 lds\n"
    "ds_read_b64_tr_b16 v[156:157], v233 offset:8264\n"
    "v_mfma_f32_16x16x32_bf16 v[80:83], v[120:123], v[168:171], v[80:83]\n"
    "ds_read_b64_tr_b16 v[158:159], v233 offset:9288\n"
    "s_waitcnt lgkmcnt(10)\n"
    "v_mfma_f32_16x16x32_bf16 v[84:87], v[124:127], v[160:163], v[84:87]\n"
    "buffer_load_dwordx4 v191, s[44:47], s90 offen offset:3096 lds\n"
    "v_mfma_f32_16x16x32_bf16 v[88:91], v[124:127], v[164:167], v[88:91]\n"
    "v_mfma_f32_16x16x32_bf16 v[92:95], v[124:127], v[168:171], v[92:95]\n"
    "s_waitcnt lgkmcnt(0)\n"
    "s_waitcnt vmcnt(16)\n"
    "s_barrier\n"
    "v_mfma_f32_16x16x32_bf16 v[0:3], v[128:131], v[172:175], v[0:3]\n"
    "ds_read_b128 v[160:163], v228 offset:41088\n"
    "s_add_u32 s88, s87, 16384\n"
    "v_mfma_f32_16x16x32_bf16 v[4:7], v[128:131], v[176:179], v[4:7]\n"
    "ds_read_b128 v[164:167], v228 offset:43136\n"
    "v_mfma_f32_16x16x32_bf16 v[8:11], v[128:131], v[180:183], v[8:11]\n"
    "ds_read_b128 v[168:171], v228 offset:45184\n"
    "v_mfma_f32_16x16x32_bf16 v[12:15], v[132:135], v[172:175], v[12:15]\n"
    "ds_read_b64_tr_b16 v[96:97], v230 offset:41088\n"
    "v_mfma_f32_16x16x32_bf16 v[16:19], v[132:135], v[176:179], v[16:19]\n"
    "ds_read_b64_tr_b16 v[98:99], v230 offset:42112\n"
    "s_add_u32 m0, s86, 0\n"
    "v_mfma_f32_16x16x32_bf16 v[20:23], v[132:135], v[180:183], v[20:23]\n"
    "ds_read_b64_tr_b16 v[100:101], v230 offset:41096\n"
    "v_mfma_f32_16x16x32_bf16 v[24:27], v[136:139], v[172:175], v[24:27]\n"
    "ds_read_b64_tr_b16 v[102:103], v230 offset:42120\n"
    "v_mfma_f32_16x16x32_bf16 v[28:31], v[136:139], v[176:179], v[28:31]\n"
    "ds_read_b64_tr_b16 v[104:105], v231 offset:41088\n"
    "v_mfma_f32_16x16x32_bf16 v[32:35], v[136:139], v[180:183], v[32:35]\n"
    "ds_read_b64_tr_b16 v[106:107], v231 offset:42112\n"
    "buffer_load_dwordx4 v238, s[48:51], s88 offen offset:0 nt lds\n"
    "v_mfma_f32_16x16x32_bf16 v[36:39], v[140:143], v[172:175], v[36:39]\n"
    "ds_read_b64_tr_b16 v[108:109], v231 offset:41096\n"
    "v_mfma_f32_16x16x32_bf16 v[40:43], v[140:143], v[176:179], v[40:43]\n"
    "ds_read_b64_tr_b16 v[110:111], v231 offset:42120\n"
    "v_mfma_f32_16x16x32_bf16 v[44:47], v[140:143], v[180:183], v[44:47]\n"
    "ds_read_b64_tr_b16 v[112:113], v232 offset:41088\n"
    "v_mfma_f32_16x16x32_bf16 v[48:51], v[144:147], v[172:175], v[48:51]\n"
    "ds_read_b64_tr_b16 v[114:115], v232 offset:42112\n"
    "buffer_load_dwordx4 v239, s[48:51], s88 offen offset:1024 nt lds\n"
    "v_mfma_f32_16x16x32_bf16 v[52:55], v[144:147], v[176:179], v[52:55]\n"
    "ds_read_b64_tr_b16 v[116:117], v232 offset:41096\n"
    "v_mfma_f32_16x16x32_bf16 v[56:59], v[144:147], v[180:183], v[56:59]\n"
    "ds_read_b64_tr_b16 v[118:119], v232 offset:42120\n"
    "v_mfma_f32_16x16x32_bf16 v[60:63], v[148:151], v[172:175], v[60:63]\n"
    "ds_read_b64_tr_b16 v[120:121], v233 offset:41088\n"
    "v_mfma_f32_16x16x32_bf16 v[64:67], v[148:151], v[176:179], v[64:67]\n"
    "buffer_load_dwordx4 v240, s[48:51], s88 offen offset:2048 nt lds\n"
    "ds_read_b64_tr_b16 v[122:123], v233 offset:42112\n"
    "v_mfma_f32_16x16x32_bf16 v[68:71], v[148:151], v[180:183], v[68:71]\n"
    "ds_read_b64_tr_b16 v[124:125], v233 offset:41096\n"
    "v_mfma_f32_16x16x32_bf16 v[72:75], v[152:155], v[172:175], v[72:75]\n"
    "ds_read_b64_tr_b16 v[126:127], v233 offset:42120\n"
    "v_mfma_f32_16x16x32_bf16 v[76:79], v[152:155], v[176:179], v[76:79]\n"
    "v_mfma_f32_16x16x32_bf16 v[80:83], v[152:155], v[180:183], v[80:83]\n"
    "buffer_load_dwordx4 v241, s[48:51], s88 offen offset:3072 nt lds\n"
    "v_mfma_f32_16x16x32_bf16 v[84:87], v[156:159], v[172:175], v[84:87]\n"
    "v_mfma_f32_16x16x32_bf16 v[88:91], v[156:159], v[176:179], v[88:91]\n"
    "v_mfma_f32_16x16x32_bf16 v[92:95], v[156:159], v[180:183], v[92:95]\n"
    "s_waitcnt lgkmcnt(8)\n"
    "v_mfma_f32_16x16x32_bf16 v[0:3], v[96:99], v[160:163], v[0:3]\n"
    "ds_read_b128 v[172:175], v229 offset:41088\n"
    "v_mfma_f32_16x16x32_bf16 v[4:7], v[96:99], v[164:167], v[4:7]\n"
    "ds_read_b128 v[176:179], v229 offset:43136\n"
    "v_mfma_f32_16x16x32_bf16 v[8:11], v[96:99], v[168:171], v[8:11]\n"
    "ds_read_b128 v[180:183], v229 offset:45184\n"
    "v_mfma_f32_16x16x32_bf16 v[12:15], v[100:103], v[160:163], v[12:15]\n"
    "ds_read_b64_tr_b16 v[128:129], v230 offset:49344\n"
    "v_mfma_f32_16x16x32_bf16 v[16:19], v[100:103], v[164:167], v[16:19]\n"
    "ds_read_b64_tr_b16 v[130:131], v230 offset:50368\n"
    "v_mfma_f32_16x16x32_bf16 v[20:23], v[100:103], v[168:171], v[20:23]\n"
    "ds_read_b64_tr_b16 v[132:133], v230 offset:49352\n"
    "v_mfma_f32_16x16x32_bf16 v[24:27], v[104:107], v[160:163], v[24:27]\n"
    "ds_read_b64_tr_b16 v[134:135], v230 offset:50376\n"
    "v_mfma_f32_16x16x32_bf16 v[28:31], v[104:107], v[164:167], v[28:31]\n"
    "v_mfma_f32_16x16x32_bf16 v[32:35], v[104:107], v[168:171], v[32:35]\n"
    "ds_read_b64_tr_b16 v[136:137], v231 offset:49344\n"
    "v_mfma_f32_16x16x32_bf16 v[36:39], v[108:111], v[160:163], v[36:39]\n"
    "ds_read_b64_tr_b16 v[138:139], v231 offset:50368\n"
    "v_mfma_f32_16x16x32_bf16 v[40:43], v[108:111], v[164:167], v[40:43]\n"
    "ds_read_b64_tr_b16 v[140:141], v231 offset:49352\n"
    "v_mfma_f32_16x16x32_bf16 v[44:47], v[108:111], v[168:171], v[44:47]\n"
    "ds_read_b64_tr_b16 v[142:143], v231 offset:50376\n"
    "s_waitcnt lgkmcnt(11)\n"
    "v_mfma_f32_16x16x32_bf16 v[48:51], v[112:115], v[160:163], v[48:51]\n"
    "ds_read_b64_tr_b16 v[144:145], v232 offset:49344\n"
    "v_mfma_f32_16x16x32_bf16 v[52:55], v[112:115], v[164:167], v[52:55]\n"
    "ds_read_b64_tr_b16 v[146:147], v232 offset:50368\n"
    "v_mfma_f32_16x16x32_bf16 v[56:59], v[112:115], v[168:171], v[56:59]\n"
    "v_mfma_f32_16x16x32_bf16 v[60:63], v[116:119], v[160:163], v[60:63]\n"
    "ds_read_b64_tr_b16 v[148:149], v232 offset:49352\n"
    "v_mfma_f32_16x16x32_bf16 v[64:67], v[116:119], v[164:167], v[64:67]\n"
    "ds_read_b64_tr_b16 v[150:151], v232 offset:50376\n"
    "v_mfma_f32_16x16x32_bf16 v[68:71], v[116:119], v[168:171], v[68:71]\n"
    "ds_read_b64_tr_b16 v[152:153], v233 offset:49344\n"
    "v_mfma_f32_16x16x32_bf16 v[72:75], v[120:123], v[160:163], v[72:75]\n"
    "ds_read_b64_tr_b16 v[154:155], v233 offset:50368\n"
    "v_mfma_f32_16x16x32_bf16 v[76:79], v[120:123], v[164:167], v[76:79]\n"
    "ds_read_b64_tr_b16 v[156:157], v233 offset:49352\n"
    "v_mfma_f32_16x16x32_bf16 v[80:83], v[120:123], v[168:171], v[80:83]\n"
    "ds_read_b64_tr_b16 v[158:159], v233 offset:50376\n"
    "v_mfma_f32_16x16x32_bf16 v[84:87], v[124:127], v[160:163], v[84:87]\n"
    "v_mfma_f32_16x16x32_bf16 v[88:91], v[124:127], v[164:167], v[88:91]\n"
    "v_mfma_f32_16x16x32_bf16 v[92:95], v[124:127], v[168:171], v[92:95]\n"
    "s_waitcnt lgkmcnt(0)\n"
    "s_waitcnt vmcnt(4)\n"
    "s_barrier\n"
    "v_mfma_f32_16x16x32_bf16 v[0:3], v[128:131], v[172:175], v[0:3]\n"
    "ds_read_b128 v[160:163], v244 offset:0\n"
    "s_add_u32 s88, s87, 20480\n"
    "v_mfma_f32_16x16x32_bf16 v[4:7], v[128:131], v[176:179], v[4:7]\n"
    "ds_read_b128 v[164:167], v244 offset:2048\n"
    "v_mfma_f32_16x16x32_bf16 v[8:11], v[128:131], v[180:183], v[8:11]\n"
    "ds_read_b128 v[168:171], v244 offset:4096\n"
    "v_mfma_f32_16x16x32_bf16 v[12:15], v[132:135], v[172:175], v[12:15]\n"
    "ds_read_b64_tr_b16 v[96:97], v246 offset:0\n"
    "v_mfma_f32_16x16x32_bf16 v[16:19], v[132:135], v[176:179], v[16:19]\n"
    "ds_read_b64_tr_b16 v[98:99], v246 offset:1024\n"
    "s_add_u32 m0, s86, 4096\n"
    "v_mfma_f32_16x16x32_bf16 v[20:23], v[132:135], v[180:183], v[20:23]\n"
    "ds_read_b64_tr_b16 v[100:101], v246 offset:8\n"
    "v_mfma_f32_16x16x32_bf16 v[24:27], v[136:139], v[172:175], v[24:27]\n"
    "ds_read_b64_tr_b16 v[102:103], v246 offset:1032\n"
    "v_mfma_f32_16x16x32_bf16 v[28:31], v[136:139], v[176:179], v[28:31]\n"
    "ds_read_b64_tr_b16 v[104:105], v247 offset:0\n"
    "v_mfma_f32_16x16x32_bf16 v[32:35], v[136:139], v[180:183], v[32:35]\n"
    "ds_read_b64_tr_b16 v[106:107], v247 offset:1024\n"
    "buffer_load_dwordx4 v238, s[48:51], s88 offen offset:0 nt lds\n"
    "v_mfma_f32_16x16x32_bf16 v[36:39], v[140:143], v[172:175], v[36:39]\n"
    "ds_read_b64_tr_b16 v[108:109], v247 offset:8\n"
    "v_mfma_f32_16x16x32_bf16 v[40:43], v[140:143], v[176:179], v[40:43]\n"
    "ds_read_b64_tr_b16 v[110:111], v247 offset:1032\n"
    "v_mfma_f32_16x16x32_bf16 v[44:47], v[140:143], v[180:183], v[44:47]\n"
    "ds_read_b64_tr_b16 v[112:113], v248 offset:0\n"
    "v_mfma_f32_16x16x32_bf16 v[48:51], v[144:147], v[172:175], v[48:51]\n"
    "ds_read_b64_tr_b16 v[114:115], v248 offset:1024\n"
    "buffer_load_dwordx4 v239, s[48:51], s88 offen offset:1024 nt lds\n"
    "v_mfma_f32_16x16x32_bf16 v[52:55], v[144:147], v[176:179], v[52:55]\n"
    "ds_read_b64_tr_b16 v[116:117], v248 offset:8\n"
    "v_mfma_f32_16x16x32_bf16 v[56:59], v[144:147], v[180:183], v[56:59]\n"
    "ds_read_b64_tr_b16 v[118:119], v248 offset:1032\n"
    "v_mfma_f32_16x16x32_bf16 v[60:63], v[148:151], v[172:175], v[60:63]\n"
    "ds_read_b64_tr_b16 v[120:121], v249 offset:0\n"
    "v_mfma_f32_16x16x32_bf16 v[64:67], v[148:151], v[176:179], v[64:67]\n"
    "buffer_load_dwordx4 v240, s[48:51], s88 offen offset:2048 nt lds\n"
    "ds_read_b64_tr_b16 v[122:123], v249 offset:1024\n"
    "v_mfma_f32_16x16x32_bf16 v[68:71], v[148:151], v[180:183], v[68:71]\n"
    "ds_read_b64_tr_b16 v[124:125], v249 offset:8\n"
    "v_mfma_f32_16x16x32_bf16 v[72:75], v[152:155], v[172:175], v[72:75]\n"
    "ds_read_b64_tr_b16 v[126:127], v249 offset:1032\n"
    "v_mfma_f32_16x16x32_bf16 v[76:79], v[152:155], v[176:179], v[76:79]\n"
    "v_mfma_f32_16x16x32_bf16 v[80:83], v[152:155], v[180:183], v[80:83]\n"
    "buffer_load_dwordx4 v241, s[48:51], s88 offen offset:3072 nt lds\n"
    "v_mfma_f32_16x16x32_bf16 v[84:87], v[156:159], v[172:175], v[84:87]\n"
    "v_mfma_f32_16x16x32_bf16 v[88:91], v[156:159], v[176:179], v[88:91]\n"
    "v_mfma_f32_16x16x32_bf16 v[92:95], v[156:159], v[180:183], v[92:95]\n"
    "s_waitcnt lgkmcnt(8)\n"
    "v_mfma_f32_16x16x32_bf16 v[0:3], v[96:99], v[160:163], v[0:3]\n"
    "ds_read_b128 v[172:175], v245 offset:0\n"
    "v_mfma_f32_16x16x32_bf16 v[4:7], v[96:99], v[164:167], v[4:7]\n"
    "ds_read_b128 v[176:179], v245 offset:2048\n"
    "v_mfma_f32_16x16x32_bf16 v[8:11], v[96:99], v[168:171], v[8:11]\n"
    "ds_read_b128 v[180:183], v245 offset:4096\n"
    "v_mfma_f32_16x16x32_bf16 v[12:15], v[100:103], v[160:163], v[12:15]\n"
    "ds_read_b64_tr_b16 v[128:129], v246 offset:8256\n"
    "v_mfma_f32_16x16x32_bf16 v[16:19], v[100:103], v[164:167], v[16:19]\n"
    "ds_read_b64_tr_b16 v[130:131], v246 offset:9280\n"
    "v_mfma_f32_16x16x32_bf16 v[20:23], v[100:103], v[168:171], v[20:23]\n"
    "ds_read_b64_tr_b16 v[132:133], v246 offset:8264\n"
    "v_mfma_f32_16x16x32_bf16 v[24:27], v[104:107], v[160:163], v[24:27]\n"
    "ds_read_b64_tr_b16 v[134:135], v246 offset:9288\n"
    "v_mfma_f32_16x16x32_bf16 v[28:31], v[104:107], v[164:167], v[28:31]\n"
    "v_mfma_f32_16x16x32_bf16 v[32:35], v[104:107], v[168:171], v[32:35]\n"
    "ds_read_b64_tr_b16 v[136:137], v247 offset:8256\n"
    "v_mfma_f32_16x16x32_bf16 v[36:39], v[108:111], v[160:163], v[36:39]\n"
    "ds_read_b64_tr_b16 v[138:139], v247 offset:9280\n"
    "v_mfma_f32_16x16x32_bf16 v[40:43], v[108:111], v[164:167], v[40:43]\n"
    "ds_read_b64_tr_b16 v[140:141], v247 offset:8264\n"
    "v_mfma_f32_16x16x32_bf16 v[44:47], v[108:111], v[168:171], v[44:47]\n"
    "ds_read_b64_tr_b16 v[142:143], v247 offset:9288\n"
    "s_waitcnt lgkmcnt(11)\n"
    "v_mfma_f32_16x16x32_bf16 v[48:51], v[112:115], v[160:163], v[48:51]\n"
    "ds_read_b64_tr_b16 v[144:145], v248 offset:8256\n"
    "v_mfma_f32_16x16x32_bf16 v[52:55], v[112:115], v[164:167], v[52:55]\n"
    "ds_read_b64_tr_b16 v[146:147], v248 offset:9280\n"
    "v_mfma_f32_16x16x32_bf16 v[56:59], v[112:115], v[168:171], v[56:59]\n"
    "v_mfma_f32_16x16x32_bf16 v[60:63], v[116:119], v[160:163], v[60:63]\n"
    "ds_read_b64_tr_b16 v[148:149], v248 offset:8264\n"
    "v_mfma_f32_16x16x32_bf16 v[64:67], v[116:119], v[164:167], v[64:67]\n"
    "ds_read_b64_tr_b16 v[150:151], v248 offset:9288\n"
    "v_mfma_f32_16x16x32_bf16 v[68:71], v[116:119], v[168:171], v[68:71]\n"
    "ds_read_b64_tr_b16 v[152:153], v249 offset:8256\n"
    "v_mfma_f32_16x16x32_bf16 v[72:75], v[120:123], v[160:163], v[72:75]\n"
    "ds_read_b64_tr_b16 v[154:155], v249 offset:9280\n"
    "v_mfma_f32_16x16x32_bf16 v[76:79], v[120:123], v[164:167], v[76:79]\n"
    "ds_read_b64_tr_b16 v[156:157], v249 offset:8264\n"
    "v_mfma_f32_16x16x32_bf16 v[80:83], v[120:123], v[168:171], v[80:83]\n"
    "ds_read_b64_tr_b16 v[158:159], v249 offset:9288\n"
    "v_mfma_f32_16x16x32_bf16 v[84:87], v[124:127], v[160:163], v[84:87]\n"
    "v_mfma_f32_16x16x32_bf16 v[88:91], v[124:127], v[164:167], v[88:91]\n"
    "v_mfma_f32_16x16x32_bf16 v[92:95], v[124:127], v[168:171], v[92:95]\n"
    "s_waitcnt lgkmcnt(0)\n"
    "s_barrier\n"
    "v_add_u32 v234, 131072, v234\n"
    "v_add_u32 v235, 131072, v235\n"
    "v_mfma_f32_16x16x32_bf16 v[0:3], v[128:131], v[172:175], v[0:3]\n"
    "ds_read_b128 a[0:3], v234 offset:0\n"
    "s_add_u32 s88, s87, 32768\n"
    "v_mfma_f32_16x16x32_bf16 v[4:7], v[128:131], v[176:179], v[4:7]\n"
    "v_mfma_f32_16x16x32_bf16 v[8:11], v[128:131], v[180:183], v[8:11]\n"
    "ds_read_b128 a[4:7], v234 offset:4096\n"
    "v_mfma_f32_16x16x32_bf16 v[12:15], v[132:135], v[172:175], v[12:15]\n"
    "v_mfma_f32_16x16x32_bf16 v[16:19], v[132:135], v[176:179], v[16:19]\n"
    "s_add_u32 m0, s86, 32768\n"
    "v_mfma_f32_16x16x32_bf16 v[20:23], v[132:135], v[180:183], v[20:23]\n"
    "ds_read_b128 a[8:11], v235 offset:0\n"
    "v_mfma_f32_16x16x32_bf16 v[24:27], v[136:139], v[172:175], v[24:27]\n"
    "v_mfma_f32_16x16x32_bf16 v[28:31], v[136:139], v[176:179], v[28:31]\n"
    "ds_read_b128 a[12:15], v235 offset:4096\n"
    "v_mfma_f32_16x16x32_bf16 v[32:35], v[136:139], v[180:183], v[32:35]\n"
    "v_pk_mul_f32 v[0:1], v[0:1], s[72:73]\n"
    "v_pk_mul_f32 v[2:3], v[2:3], s[72:73]\n"
    "v_mfma_f32_16x16x32_bf16 v[36:39], v[140:143], v[172:175], v[36:39]\n"
    "v_pk_mul_f32 v[12:13], v[12:13], s[72:73]\n"
    "v_pk_mul_f32 v[14:15], v[14:15], s[72:73]\n"
    "v_mfma_f32_16x16x32_bf16 v[40:43], v[140:143], v[176:179], v[40:43]\n"
    "v_cvt_pk_bf16_f32 v96, v0, v1\n"
    "v_cvt_pk_bf16_f32 v97, v2, v3\n"
    "v_mfma_f32_16x16x32_bf16 v[44:47], v[140:143], v[180:183], v[44:47]\n"
    "v_cvt_pk_bf16_f32 v98, v12, v13\n"
    "v_cvt_pk_bf16_f32 v99, v14, v15\n"
    "v_mfma_f32_16x16x32_bf16 v[48:51], v[144:147], v[172:175], v[48:51]\n"
    "v_pk_mul_f32 v[4:5], v[4:5], s[72:73]\n"
    "v_pk_mul_f32 v[6:7], v[6:7], s[72:73]\n"
    "v_mfma_f32_16x16x32_bf16 v[52:55], v[144:147], v[176:179], v[52:55]\n"
    "v_pk_mul_f32 v[16:17], v[16:17], s[72:73]\n"
    "v_pk_mul_f32 v[18:19], v[18:19], s[72:73]\n"
    "v_mfma_f32_16x16x32_bf16 v[56:59], v[144:147], v[180:183], v[56:59]\n"
    "v_cvt_pk_bf16_f32 v100, v4, v5\n"
    "v_cvt_pk_bf16_f32 v101, v6, v7\n"
    "v_mfma_f32_16x16x32_bf16 v[60:63], v[148:151], v[172:175], v[60:63]\n"
    "v_cvt_pk_bf16_f32 v102, v16, v17\n"
    "v_cvt_pk_bf16_f32 v103, v18, v19\n"
    "v_mfma_f32_16x16x32_bf16 v[64:67], v[148:151], v[176:179], v[64:67]\n"
    "v_pk_mul_f32 v[8:9], v[8:9], s[72:73]\n"
    "v_pk_mul_f32 v[10:11], v[10:11], s[72:73]\n"
    "v_mfma_f32_16x16x32_bf16 v[68:71], v[148:151], v[180:183], v[68:71]\n"
    "v_pk_mul_f32 v[20:21], v[20:21], s[72:73]\n"
    "v_pk_mul_f32 v[22:23], v[22:23], s[72:73]\n"
    "v_mfma_f32_16x16x32_bf16 v[72:75], v[152:155], v[172:175], v[72:75]\n"
    "v_cvt_pk_bf16_f32 v104, v8, v9\n"
    "v_cvt_pk_bf16_f32 v105, v10, v11\n"
    "v_mfma_f32_16x16x32_bf16 v[76:79], v[152:155], v[176:179], v[76:79]\n"
    "v_cvt_pk_bf16_f32 v106, v20, v21\n"
    "v_cvt_pk_bf16_f32 v107, v22, v23\n"
    "v_mfma_f32_16x16x32_bf16 v[80:83], v[152:155], v[180:183], v[80:83]\n"
    "v_pk_mul_f32 v[24:25], v[24:25], s[72:73]\n"
    "v_pk_mul_f32 v[26:27], v[26:27], s[72:73]\n"
    "v_mfma_f32_16x16x32_bf16 v[84:87], v[156:159], v[172:175], v[84:87]\n"
    "v_pk_mul_f32 v[36:37], v[36:37], s[72:73]\n"
    "v_pk_mul_f32 v[38:39], v[38:39], s[72:73]\n"
    "v_mfma_f32_16x16x32_bf16 v[88:91], v[156:159], v[176:179], v[88:91]\n"
    "v_cvt_pk_bf16_f32 v108, v24, v25\n"
    "v_cvt_pk_bf16_f32 v109, v26, v27\n"
    "v_mfma_f32_16x16x32_bf16 v[92:95], v[156:159], v[180:183], v[92:95]\n"
    "v_cvt_pk_bf16_f32 v110, v36, v37\n"
    "v_cvt_pk_bf16_f32 v111, v38, v39\n"
    "ds_read_b128 a[16:19], v234 offset:2048\n"
    "ds_read_b128 a[20:23], v234 offset:6144\n"
    "ds_read_b128 a[24:27], v235 offset:2048\n"
    "ds_read_b128 a[28:31], v235 offset:6144\n"
    "buffer_load_dwordx4 v238, s[48:51], s88 offen offset:0 nt lds\n"
    "buffer_load_dwordx4 v239, s[48:51], s88 offen offset:1024 nt lds\n"
    "buffer_load_dwordx4 v240, s[48:51], s88 offen offset:2048 nt lds\n"
    "buffer_load_dwordx4 v241, s[48:51], s88 offen offset:3072 nt lds\n"
    "v_pk_mul_f32 v[28:29], v[28:29], s[72:73]\n"
    "v_pk_mul_f32 v[30:31], v[30:31], s[72:73]\n"
    "v_pk_mul_f32 v[40:41], v[40:41], s[72:73]\n"
    "v_pk_mul_f32 v[42:43], v[42:43], s[72:73]\n"
    "v_cvt_pk_bf16_f32 v112, v28, v29\n"
    "v_cvt_pk_bf16_f32 v113, v30, v31\n"
    "v_cvt_pk_bf16_f32 v114, v40, v41\n"
    "v_cvt_pk_bf16_f32 v115, v42, v43\n"
    "v_pk_mul_f32 v[32:33], v[32:33], s[72:73]\n"
    "v_pk_mul_f32 v[34:35], v[34:35], s[72:73]\n"
    "v_pk_mul_f32 v[44:45], v[44:45], s[72:73]\n"
    "v_pk_mul_f32 v[46:47], v[46:47], s[72:73]\n"
    "v_cvt_pk_bf16_f32 v116, v32, v33\n"
    "v_cvt_pk_bf16_f32 v117, v34, v35\n"
    "v_cvt_pk_bf16_f32 v118, v44, v45\n"
    "v_cvt_pk_bf16_f32 v119, v46, v47\n"
    "s_waitcnt lgkmcnt(0)\n"
    "v_mov_b32 v251, 1065369472\n"
    "v_accvgpr_write_b32 a76, v251\n"
    "v_accvgpr_write_b32 a77, v251\n"
    "v_accvgpr_write_b32 a78, v251\n"
    "v_accvgpr_write_b32 a79, v251\n"
    "v_mfma_f32_16x16x32_bf16 v[144:147], a[0:3], v[96:99], 0\n"
    "v_pk_mul_f32 v[48:49], v[48:49], s[72:73]\n"
    "v_pk_mul_f32 v[50:51], v[50:51], s[72:73]\n"
    "v_pk_mul_f32 v[60:61], v[60:61], s[72:73]\n"
    "v_pk_mul_f32 v[62:63], v[62:63], s[72:73]\n"
    "v_mfma_f32_16x16x32_bf16 v[148:151], a[0:3], v[100:103], 0\n"
    "v_cvt_pk_bf16_f32 v120, v48, v49\n"
    "v_cvt_pk_bf16_f32 v121, v50, v51\n"
    "v_cvt_pk_bf16_f32 v122, v60, v61\n"
    "v_cvt_pk_bf16_f32 v123, v62, v63\n"
    "v_mfma_f32_16x16x32_bf16 v[152:155], a[0:3], v[104:107], 0\n"
    "v_pk_mul_f32 v[52:53], v[52:53], s[72:73]\n"
    "v_pk_mul_f32 v[54:55], v[54:55], s[72:73]\n"
    "v_pk_mul_f32 v[64:65], v[64:65], s[72:73]\n"
    "v_pk_mul_f32 v[66:67], v[66:67], s[72:73]\n"
    "v_mfma_f32_16x16x32_bf16 v[156:159], a[4:7], v[96:99], 0\n"
    "v_cvt_pk_bf16_f32 v124, v52, v53\n"
    "v_cvt_pk_bf16_f32 v125, v54, v55\n"
    "v_cvt_pk_bf16_f32 v126, v64, v65\n"
    "v_cvt_pk_bf16_f32 v127, v66, v67\n"
    "v_mfma_f32_16x16x32_bf16 v[160:163], a[4:7], v[100:103], 0\n"
    "v_pk_mul_f32 v[56:57], v[56:57], s[72:73]\n"
    "v_pk_mul_f32 v[58:59], v[58:59], s[72:73]\n"
    "v_pk_mul_f32 v[68:69], v[68:69], s[72:73]\n"
    "v_pk_mul_f32 v[70:71], v[70:71], s[72:73]\n"
    "v_mfma_f32_16x16x32_bf16 v[164:167], a[4:7], v[104:107], 0\n"
    "v_cvt_pk_bf16_f32 v128, v56, v57\n"
    "v_cvt_pk_bf16_f32 v129, v58, v59\n"
    "v_cvt_pk_bf16_f32 v130, v68, v69\n"
    "v_cvt_pk_bf16_f32 v131, v70, v71\n"
    "v_mfma_f32_16x16x32_bf16 v[144:147], a[8:11], v[108:111], v[144:147]\n"
    "v_pk_mul_f32 v[72:73], v[72:73], s[72:73]\n"
    "v_pk_mul_f32 v[74:75], v[74:75], s[72:73]\n"
    "v_pk_mul_f32 v[84:85], v[84:85], s[72:73]\n"
    "v_pk_mul_f32 v[86:87], v[86:87], s[72:73]\n"
    "v_mfma_f32_16x16x32_bf16 v[148:151], a[8:11], v[112:115], v[148:151]\n"
    "v_cvt_pk_bf16_f32 v132, v72, v73\n"
    "v_cvt_pk_bf16_f32 v133, v74, v75\n"
    "v_cvt_pk_bf16_f32 v134, v84, v85\n"
    "v_cvt_pk_bf16_f32 v135, v86, v87\n"
    "v_mfma_f32_16x16x32_bf16 v[152:155], a[8:11], v[116:119], v[152:155]\n"
    "v_pk_mul_f32 v[76:77], v[76:77], s[72:73]\n"
    "v_pk_mul_f32 v[78:79], v[78:79], s[72:73]\n"
    "v_pk_mul_f32 v[88:89], v[88:89], s[72:73]\n"
    "v_pk_mul_f32 v[90:91], v[90:91], s[72:73]\n"
    "v_mfma_f32_16x16x32_bf16 v[156:159], a[12:15], v[108:111], v[156:159]\n"
    "v_cvt_pk_bf16_f32 v136, v76, v77\n"
    "v_cvt_pk_bf16_f32 v137, v78, v79\n"
    "v_cvt_pk_bf16_f32 v138, v88, v89\n"
    "v_cvt_pk_bf16_f32 v139, v90, v91\n"
    "v_mfma_f32_16x16x32_bf16 v[160:163], a[12:15], v[112:115], v[160:163]\n"
    "v_pk_mul_f32 v[80:81], v[80:81], s[72:73]\n"
    "v_pk_mul_f32 v[82:83], v[82:83], s[72:73]\n"
    "v_pk_mul_f32 v[92:93], v[92:93], s[72:73]\n"
    "v_pk_mul_f32 v[94:95], v[94:95], s[72:73]\n"
    "v_mfma_f32_16x16x32_bf16 v[164:167], a[12:15], v[116:119], v[164:167]\n"
    "v_cvt_pk_bf16_f32 v140, v80, v81\n"
    "v_cvt_pk_bf16_f32 v141, v82, v83\n"
    "v_cvt_pk_bf16_f32 v142, v92, v93\n"
    "v_cvt_pk_bf16_f32 v143, v94, v95\n"
    "v_mfma_f32_16x16x32_bf16 v[144:147], a[16:19], v[120:123], v[144:147]\n"
    "s_add_u32 s88, s87, 36864\n"
    "s_add_u32 m0, s86, 36864\n"
    "v_mfma_f32_16x16x32_bf16 v[148:151], a[16:19], v[124:127], v[148:151]\n"
    "buffer_load_dwordx4 v238, s[48:51], s88 offen offset:0 nt lds\n"
    "buffer_load_dwordx4 v239, s[48:51], s88 offen offset:1024 nt lds\n"
    "v_mfma_f32_16x16x32_bf16 v[152:155], a[16:19], v[128:131], v[152:155]\n"
    "buffer_load_dwordx4 v240, s[48:51], s88 offen offset:2048 nt lds\n"
    "buffer_load_dwordx4 v241, s[48:51], s88 offen offset:3072 nt lds\n"
    "v_mfma_f32_16x16x32_bf16 v[156:159], a[20:23], v[120:123], v[156:159]\n"
    "v_mfma_f32_16x16x32_bf16 v[160:163], a[20:23], v[124:127], v[160:163]\n"
    "v_mfma_f32_16x16x32_bf16 v[164:167], a[20:23], v[128:131], v[164:167]\n"
    "v_mfma_f32_16x16x32_bf16 v[144:147], a[24:27], v[132:135], v[144:147]\n"
    "v_mfma_f32_16x16x32_bf16 v[148:151], a[24:27], v[136:139], v[148:151]\n"
    "v_mfma_f32_16x16x32_bf16 v[152:155], a[24:27], v[140:143], v[152:155]\n"
    "v_mfma_f32_16x16x32_bf16 v[156:159], a[28:31], v[132:135], v[156:159]\n"
    "v_mfma_f32_16x16x32_bf16 v[160:163], a[28:31], v[136:139], v[160:163]\n"
    "v_mfma_f32_16x16x32_bf16 v[164:167], a[28:31], v[140:143], v[164:167]\n"
    "v_add_u32 v236, 131072, v236\n"
    "v_add_u32 v237, 131072, v237\n"
    "ds_read_b128 a[0:3], v234 offset:8192\n"
    "ds_read_b128 a[4:7], v234 offset:12288\n"
    "s_waitcnt lgkmcnt(1)\n"
    "v_mfma_f32_16x16x32_bf16 v[168:171], a[0:3], v[96:99], 0\n"
    "ds_read_b128 a[8:11], v235 offset:8192\n"
    "ds_read_b128 a[12:15], v235 offset:12288\n"
    "ds_read_b128 a[16:19], v234 offset:10240\n"
    "v_mfma_f32_16x16x32_bf16 v[172:175], a[0:3], v[100:103], 0\n"
    "ds_read_b128 a[20:23], v234 offset:14336\n"
    "ds_read_b128 a[24:27], v235 offset:10240\n"
    "ds_read_b128 a[28:31], v235 offset:14336\n"
    "v_mfma_f32_16x16x32_bf16 v[176:179], a[0:3], v[104:107], 0\n"
    "v_max3_f32 v244, v144, v145, v146\n"
    "v_max3_f32 v244, v244, v147, v156\n"
    "v_max3_f32 v244, v244, v157, v158\n"
    "s_waitcnt lgkmcnt(6)\n"
    "v_mfma_f32_16x16x32_bf16 v[180:183], a[4:7], v[96:99], 0\n"
    "v_max_f32 v244, v244, v159\n"
    "v_max3_f32 v246, v148, v149, v150\n"
    "v_max3_f32 v246, v246, v151, v160\n"
    "v_mfma_f32_16x16x32_bf16 v[184:187], a[4:7], v[100:103], 0\n"
    "v_max3_f32 v246, v246, v161, v162\n"
    "v_max_f32 v246, v246, v163\n"
    "v_max3_f32 v248, v152, v153, v154\n"
    "v_mfma_f32_16x16x32_bf16 v[188:191], a[4:7], v[104:107], 0\n"
    "v_max3_f32 v248, v248, v155, v164\n"
    "v_max3_f32 v248, v248, v165, v166\n"
    "v_max_f32 v248, v248, v167\n"
    "s_waitcnt lgkmcnt(5)\n"
    "v_mfma_f32_16x16x32_bf16 v[168:171], a[8:11], v[108:111], v[168:171]\n"
    "v_mov_b32 v245, v244\n"
    "v_mov_b32 v247, v246\n"
    "v_mov_b32 v249, v248\n"
    "v_mfma_f32_16x16x32_bf16 v[172:175], a[8:11], v[112:115], v[172:175]\n"
    "v_permlane32_swap_b32 v244, v245\n"
    "v_permlane32_swap_b32 v246, v247\n"
    "v_permlane32_swap_b32 v248, v249\n"
    "v_mfma_f32_16x16x32_bf16 v[176:179], a[8:11], v[116:119], v[176:179]\n"
    "v_max_f32 v244, v244, v245\n"
    "v_max_f32 v246, v246, v247\n"
    "v_max_f32 v248, v248, v249\n"
    "s_waitcnt lgkmcnt(0)\n"
    "v_mfma_f32_16x16x32_bf16 v[180:183], a[12:15], v[108:111], v[180:183]\n"
    "v_mov_b32 v245, v244\n"
    "v_mov_b32 v247, v246\n"
    "v_mov_b32 v249, v248\n"
    "v_mfma_f32_16x16x32_bf16 v[184:187], a[12:15], v[112:115], v[184:187]\n"
    "v_permlane16_swap_b32 v244, v245\n"
    "v_permlane16_swap_b32 v246, v247\n"
    "v_permlane16_swap_b32 v248, v249\n"
    "v_mfma_f32_16x16x32_bf16 v[188:191], a[12:15], v[116:119], v[188:191]\n"
    "v_max_f32 v244, v244, v245\n"
    "v_max_f32 v246, v246, v247\n"
    "v_max_f32 v248, v248, v249\n"
    "v_mfma_f32_16x16x32_bf16 v[168:171], a[16:19], v[120:123], v[168:171]\n"
    "v_sub_f32 v216, 0, v244\n"
    "v_sub_f32 v217, 0, v244\n"
    "v_sub_f32 v218, 0, v244\n"
    "v_mfma_f32_16x16x32_bf16 v[172:175], a[16:19], v[124:127], v[172:175]\n"
    "v_sub_f32 v219, 0, v244\n"
    "v_sub_f32 v220, 0, v246\n"
    "v_sub_f32 v221, 0, v246\n"
    "v_mfma_f32_16x16x32_bf16 v[176:179], a[16:19], v[128:131], v[176:179]\n"
    "v_sub_f32 v222, 0, v246\n"
    "v_sub_f32 v223, 0, v246\n"
    "v_sub_f32 v224, 0, v248\n"
    "v_mfma_f32_16x16x32_bf16 v[180:183], a[20:23], v[120:123], v[180:183]\n"
    "v_sub_f32 v225, 0, v248\n"
    "v_sub_f32 v226, 0, v248\n"
    "v_sub_f32 v227, 0, v248\n"
    "v_mfma_f32_16x16x32_bf16 v[184:187], a[20:23], v[124:127], v[184:187]\n"
    "s_add_u32 s88, s87, 49152\n"
    "s_add_u32 m0, s86, 65536\n"
    "s_nop 0\n"
    "buffer_load_dwordx4 v238, s[48:51], s88 offen offset:0 nt lds\n"
    "v_mfma_f32_16x16x32_bf16 v[188:191], a[20:23], v[128:131], v[188:191]\n"
    "buffer_load_dwordx4 v239, s[48:51], s88 offen offset:1024 nt lds\n"
    "buffer_load_dwordx4 v240, s[48:51], s88 offen offset:2048 nt lds\n"
    "buffer_load_dwordx4 v241, s[48:51], s88 offen offset:3072 nt lds\n"
    "v_mfma_f32_16x16x32_bf16 v[168:171], a[24:27], v[132:135], v[168:171]\n"
    "v_pk_add_f32 v[144:145], v[144:145], v[216:217]\n"
    "v_pk_add_f32 v[146:147], v[146:147], v[216:217]\n"
    "ds_read_b64_tr_b16 a[32:33], v236 offset:0\n"
    "v_mfma_f32_16x16x32_bf16 v[172:175], a[24:27], v[136:139], v[172:175]\n"
    "v_exp_f32 v144, v144\n"
    "v_mfma_f32_16x16x32_bf16 v[176:179], a[24:27], v[140:143], v[176:179]\n"
    "v_exp_f32 v145, v145\n"
    "v_mfma_f32_16x16x32_bf16 v[180:183], a[28:31], v[132:135], v[180:183]\n"
    "v_exp_f32 v146, v146\n"
    "ds_read_b64_tr_b16 a[34:35], v236 offset:4096\n"
    "v_mfma_f32_16x16x32_bf16 v[184:187], a[28:31], v[136:139], v[184:187]\n"
    "v_exp_f32 v147, v147\n"
    "v_cvt_pk_bf16_f32 v192, v144, v145\n"
    "v_mfma_f32_16x16x32_bf16 v[188:191], a[28:31], v[140:143], v[188:191]\n"
    "v_cvt_pk_bf16_f32 v193, v146, v147\n"
    "ds_read_b64_tr_b16 a[36:37], v237 offset:0\n"
    "v_pk_add_f32 v[156:157], v[156:157], v[216:217]\n"
    "v_pk_add_f32 v[158:159], v[158:159], v[216:217]\n"
    "v_exp_f32 v156, v156\n"
    "ds_read_b64_tr_b16 a[38:39], v237 offset:4096\n"
    "v_exp_f32 v157, v157\n"
    "v_exp_f32 v158, v158\n"
    "v_exp_f32 v159, v159\n"
    "ds_read_b64_tr_b16 a[40:41], v236 offset:512\n"
    "v_cvt_pk_bf16_f32 v194, v156, v157\n"
    "v_cvt_pk_bf16_f32 v195, v158, v159\n"
    "v_pk_add_f32 v[148:149], v[148:149], v[220:221]\n"
    "ds_read_b64_tr_b16 a[42:43], v236 offset:4608\n"
    "v_pk_add_f32 v[150:151], v[150:151], v[220:221]\n"
    "v_exp_f32 v148, v148\n"
    "v_exp_f32 v149, v149\n"
    "ds_read_b64_tr_b16 a[44:45], v237 offset:512\n"
    "v_exp_f32 v150, v150\n"
    "v_exp_f32 v151, v151\n"
    "v_cvt_pk_bf16_f32 v196, v148, v149\n"
    "ds_read_b64_tr_b16 a[46:47], v237 offset:4608\n"
    "v_cvt_pk_bf16_f32 v197, v150, v151\n"
    "v_pk_add_f32 v[160:161], v[160:161], v[220:221]\n"
    "v_pk_add_f32 v[162:163], v[162:163], v[220:221]\n"
    "ds_read_b64_tr_b16 a[48:49], v236 offset:1024\n"
    "v_exp_f32 v160, v160\n"
    "v_exp_f32 v161, v161\n"
    "v_exp_f32 v162, v162\n"
    "ds_read_b64_tr_b16 a[50:51], v236 offset:5120\n"
    "v_exp_f32 v163, v163\n"
    "v_cvt_pk_bf16_f32 v198, v160, v161\n"
    "v_cvt_pk_bf16_f32 v199, v162, v163\n"
    "ds_read_b64_tr_b16 a[52:53], v237 offset:1024\n"
    "v_pk_add_f32 v[152:153], v[152:153], v[224:225]\n"
    "v_pk_add_f32 v[154:155], v[154:155], v[224:225]\n"
    "v_exp_f32 v152, v152\n"
    "ds_read_b64_tr_b16 a[54:55], v237 offset:5120\n"
    "v_exp_f32 v153, v153\n"
    "v_exp_f32 v154, v154\n"
    "v_exp_f32 v155, v155\n"
    "ds_read_b64_tr_b16 a[56:57], v236 offset:1536\n"
    "v_cvt_pk_bf16_f32 v200, v152, v153\n"
    "v_cvt_pk_bf16_f32 v201, v154, v155\n"
    "v_pk_add_f32 v[164:165], v[164:165], v[224:225]\n"
    "ds_read_b64_tr_b16 a[58:59], v236 offset:5632\n"
    "v_pk_add_f32 v[166:167], v[166:167], v[224:225]\n"
    "v_exp_f32 v164, v164\n"
    "v_exp_f32 v165, v165\n"
    "ds_read_b64_tr_b16 a[60:61], v237 offset:1536\n"
    "v_exp_f32 v166, v166\n"
    "v_exp_f32 v167, v167\n"
    "v_cvt_pk_bf16_f32 v202, v164, v165\n"
    "ds_read_b64_tr_b16 a[62:63], v237 offset:5632\n"
    "v_cvt_pk_bf16_f32 v203, v166, v167\n"
    "s_waitcnt vmcnt(12)\n"
    "s_barrier\n"
    "v_readfirstlane_b32 s94, v252\n"
    "v_readfirstlane_b32 s95, v253\n"
    "s_nop 2\n"
    "s_add_u32 s94, s94, 1\n"
    "s_addc_u32 s95, s95, 0\n"
    "s_lshl_b32 s88, s99, 3\n"
    "s_add_u32 s88, s88, 6144\n"
    "v_mov_b32 v244, s88\n"
    "v_mov_b32 v246, s94\n"
    "v_mov_b32 v247, s95\n"
    "s_cmp_eq_u32 s66, 0\n"
    "s_cselect_b64 exec, 1, 0\n"
    "global_store_dwordx2 v244, v[246:247], s[64:65] offset:0 sc1\n"
    "s_mov_b64 exec, -1\n"
    "v_subrev_u32 v234, 131072, v234\n"
    "v_subrev_u32 v235, 131072, v235\n"
    "v_mfma_f32_16x16x32_bf16 a[64:67], a[76:79], v[192:195], 0\n"
    "ds_read_b128 a[0:3], v234 offset:0\n"
    "v_mfma_f32_16x16x32_bf16 a[68:71], a[76:79], v[196:199], 0\n"
    "ds_read_b128 a[4:7], v234 offset:4096\n"
    "v_pk_add_f32 v[168:169], v[168:169], v[216:217]\n"
    "v_mfma_f32_16x16x32_bf16 a[72:75], a[76:79], v[200:203], 0\n"
    "ds_read_b128 a[8:11], v235 offset:0\n"
    "v_pk_add_f32 v[170:171], v[170:171], v[216:217]\n"
    "s_waitcnt lgkmcnt(3)\n"
    "v_mfma_f32_16x16x32_bf16 v[0:3], a[32:35], v[192:195], 0\n"
    "ds_read_b128 a[12:15], v235 offset:4096\n"
    "v_mfma_f32_16x16x32_bf16 v[4:7], a[32:35], v[196:199], 0\n"
    "v_exp_f32 v168, v168\n"
    "v_mfma_f32_16x16x32_bf16 v[8:11], a[32:35], v[200:203], 0\n"
    "ds_read_b128 a[16:19], v234 offset:2048\n"
    "s_add_u32 s88, s87, 53248\n"
    "v_mfma_f32_16x16x32_bf16 v[12:15], a[36:39], v[192:195], 0\n"
    "v_exp_f32 v169, v169\n"
    "v_mfma_f32_16x16x32_bf16 v[16:19], a[36:39], v[196:199], 0\n"
    "ds_read_b128 a[20:23], v234 offset:6144\n"
    "v_mfma_f32_16x16x32_bf16 v[20:23], a[36:39], v[200:203], 0\n"
    "v_exp_f32 v170, v170\n"
    "v_mfma_f32_16x16x32_bf16 v[24:27], a[40:43], v[192:195], 0\n"
    "ds_read_b128 a[24:27], v235 offset:2048\n"
    "v_mfma_f32_16x16x32_bf16 v[28:31], a[40:43], v[196:199], 0\n"
    "v_exp_f32 v171, v171\n"
    "v_mfma_f32_16x16x32_bf16 v[32:35], a[40:43], v[200:203], 0\n"
    "ds_read_b128 a[28:31], v235 offset:6144\n"
    "v_cvt_pk_bf16_f32 v204, v168, v169\n"
    "v_mfma_f32_16x16x32_bf16 v[36:39], a[44:47], v[192:195], 0\n"
    "v_cvt_pk_bf16_f32 v205, v170, v171\n"
    "v_pk_add_f32 v[180:181], v[180:181], v[216:217]\n"
    "v_mfma_f32_16x16x32_bf16 v[40:43], a[44:47], v[196:199], 0\n"
    "v_pk_add_f32 v[182:183], v[182:183], v[216:217]\n"
    "ds_read_b64_tr_b16 a[32:33], v236 offset:8192\n"
    "v_mfma_f32_16x16x32_bf16 v[44:47], a[44:47], v[200:203], 0\n"
    "v_exp_f32 v180, v180\n"
    "v_mfma_f32_16x16x32_bf16 v[48:51], a[48:51], v[192:195], 0\n"
    "v_exp_f32 v181, v181\n"
    "v_mfma_f32_16x16x32_bf16 v[52:55], a[48:51], v[196:199], 0\n"
    "v_exp_f32 v182, v182\n"
    "v_mfma_f32_16x16x32_bf16 v[56:59], a[48:51], v[200:203], 0\n"
    "v_exp_f32 v183, v183\n"
    "v_mfma_f32_16x16x32_bf16 v[60:63], a[52:55], v[192:195], 0\n"
    "v_cvt_pk_bf16_f32 v206, v180, v181\n"
    "ds_read_b64_tr_b16 a[34:35], v236 offset:12288\n"
    "v_mfma_f32_16x16x32_bf16 v[64:67], a[52:55], v[196:199], 0\n"
    "v_cvt_pk_bf16_f32 v207, v182, v183\n"
    "v_pk_add_f32 v[172:173], v[172:173], v[220:221]\n"
    "v_mfma_f32_16x16x32_bf16 v[68:71], a[52:55], v[200:203], 0\n"
    "ds_read_b64_tr_b16 a[36:37], v237 offset:8192\n"
    "v_pk_add_f32 v[174:175], v[174:175], v[220:221]\n"
    "v_mfma_f32_16x16x32_bf16 v[72:75], a[56:59], v[192:195], 0\n"
    "v_exp_f32 v172, v172\n"
    "v_mfma_f32_16x16x32_bf16 v[76:79], a[56:59], v[196:199], 0\n"
    "ds_read_b64_tr_b16 a[38:39], v237 offset:12288\n"
    "s_add_u32 m0, s86, 69632\n"
    "v_mfma_f32_16x16x32_bf16 v[80:83], a[56:59], v[200:203], 0\n"
    "v_exp_f32 v173, v173\n"
    "v_mfma_f32_16x16x32_bf16 v[84:87], a[60:63], v[192:195], 0\n"
    "v_exp_f32 v174, v174\n"
    "v_mfma_f32_16x16x32_bf16 v[88:91], a[60:63], v[196:199], 0\n"
    "v_exp_f32 v175, v175\n"
    "v_mfma_f32_16x16x32_bf16 v[92:95], a[60:63], v[200:203], 0\n"
    "v_cvt_pk_bf16_f32 v208, v172, v173\n"
    "ds_read_b64_tr_b16 a[40:41], v236 offset:8704\n"
    "s_waitcnt lgkmcnt(4)\n"
    "v_mfma_f32_16x16x32_bf16 v[144:147], a[0:3], v[96:99], v[216:219]\n"
    "v_cvt_pk_bf16_f32 v209, v174, v175\n"
    "ds_read_b64_tr_b16 a[42:43], v236 offset:12800\n"
    "v_mfma_f32_16x16x32_bf16 v[148:151], a[0:3], v[100:103], v[220:223]\n"
    "v_pk_add_f32 v[184:185], v[184:185], v[220:221]\n"
    "v_pk_add_f32 v[186:187], v[186:187], v[220:221]\n"
    "v_mfma_f32_16x16x32_bf16 v[152:155], a[0:3], v[104:107], v[224:227]\n"
    "v_exp_f32 v184, v184\n"
    "v_mfma_f32_16x16x32_bf16 v[156:159], a[4:7], v[96:99], v[216:219]\n"
    "ds_read_b64_tr_b16 a[44:45], v237 offset:8704\n"
    "buffer_load_dwordx4 v238, s[48:51], s88 offen offset:0 nt lds\n"
    "v_mfma_f32_16x16x32_bf16 v[160:163], a[4:7], v[100:103], v[220:223]\n"
    "v_exp_f32 v185, v185\n"
    "v_mfma_f32_16x16x32_bf16 v[164:167], a[4:7], v[104:107], v[224:227]\n"
    "v_exp_f32 v186, v186\n"
    "v_mfma_f32_16x16x32_bf16 v[144:147], a[8:11], v[108:111], v[144:147]\n"
    "v_exp_f32 v187, v187\n"
    "v_mfma_f32_16x16x32_bf16 v[148:151], a[8:11], v[112:115], v[148:151]\n"
    "v_cvt_pk_bf16_f32 v210, v184, v185\n"
    "ds_read_b64_tr_b16 a[46:47], v237 offset:12800\n"
    "v_mfma_f32_16x16x32_bf16 v[152:155], a[8:11], v[116:119], v[152:155]\n"
    "v_cvt_pk_bf16_f32 v211, v186, v187\n"
    "ds_read_b64_tr_b16 a[48:49], v236 offset:9216\n"
    "v_mfma_f32_16x16x32_bf16 v[156:159], a[12:15], v[108:111], v[156:159]\n"
    "v_pk_add_f32 v[176:177], v[176:177], v[224:225]\n"
    "v_pk_add_f32 v[178:179], v[178:179], v[224:225]\n"
    "v_mfma_f32_16x16x32_bf16 v[160:163], a[12:15], v[112:115], v[160:163]\n"
    "ds_read_b64_tr_b16 a[50:51], v236 offset:13312\n"
    "buffer_load_dwordx4 v239, s[48:51], s88 offen offset:1024 nt lds\n"
    "v_mfma_f32_16x16x32_bf16 v[164:167], a[12:15], v[116:119], v[164:167]\n"
    "v_exp_f32 v176, v176\n"
    "v_mfma_f32_16x16x32_bf16 v[144:147], a[16:19], v[120:123], v[144:147]\n"
    "v_exp_f32 v177, v177\n"
    "v_mfma_f32_16x16x32_bf16 v[148:151], a[16:19], v[124:127], v[148:151]\n"
    "v_exp_f32 v178, v178\n"
    "v_mfma_f32_16x16x32_bf16 v[152:155], a[16:19], v[128:131], v[152:155]\n"
    "v_exp_f32 v179, v179\n"
    "v_mfma_f32_16x16x32_bf16 v[156:159], a[20:23], v[120:123], v[156:159]\n"
    "ds_read_b64_tr_b16 a[52:53], v237 offset:9216\n"
    "v_cvt_pk_bf16_f32 v212, v176, v177\n"
    "v_mfma_f32_16x16x32_bf16 v[160:163], a[20:23], v[124:127], v[160:163]\n"
    "v_cvt_pk_bf16_f32 v213, v178, v179\n"
    "ds_read_b64_tr_b16 a[54:55], v237 offset:13312\n"
    "v_mfma_f32_16x16x32_bf16 v[164:167], a[20:23], v[128:131], v[164:167]\n"
    "v_pk_add_f32 v[188:189], v[188:189], v[224:225]\n"
    "v_pk_add_f32 v[190:191], v[190:191], v[224:225]\n"
    "v_mfma_f32_16x16x32_bf16 v[144:147], a[24:27], v[132:135], v[144:147]\n"
    "ds_read_b64_tr_b16 a[56:57], v236 offset:9728\n"
    "buffer_load_dwordx4 v240, s[48:51], s88 offen offset:2048 nt lds\n"
    "v_mfma_f32_16x16x32_bf16 v[148:151], a[24:27], v[136:139], v[148:151]\n"
    "v_exp_f32 v188, v188\n"
    "v_mfma_f32_16x16x32_bf16 v[152:155], a[24:27], v[140:143], v[152:155]\n"
    "v_exp_f32 v189, v189\n"
    "v_mfma_f32_16x16x32_bf16 v[156:159], a[28:31], v[132:135], v[156:159]\n"
    "v_exp_f32 v190, v190\n"
    "v_mfma_f32_16x16x32_bf16 v[160:163], a[28:31], v[136:139], v[160:163]\n"
    "v_exp_f32 v191, v191\n"
    "v_mfma_f32_16x16x32_bf16 v[164:167], a[28:31], v[140:143], v[164:167]\n"
    "ds_read_b64_tr_b16 a[58:59], v236 offset:13824\n"
    "v_cvt_pk_bf16_f32 v214, v188, v189\n"
    "ds_read_b64_tr_b16 a[60:61], v237 offset:9728\n"
    "ds_read_b64_tr_b16 a[62:63], v237 offset:13824\n"
    "v_cvt_pk_bf16_f32 v215, v190, v191\n"
    "buffer_load_dwordx4 v241, s[48:51], s88 offen offset:3072 nt lds\n"
    "v_subrev_u32 v236, 131072, v236\n"
    "v_subrev_u32 v237, 131072, v237\n"
    "v_mfma_f32_16x16x32_bf16 a[64:67], a[76:79], v[204:207], a[64:67]\n"
    "ds_read_b128 a[0:3], v234 offset:8192\n"
    "v_mfma_f32_16x16x32_bf16 a[68:71], a[76:79], v[208:211], a[68:71]\n"
    "ds_read_b128 a[4:7], v234 offset:12288\n"
    "v_mfma_f32_16x16x32_bf16 a[72:75], a[76:79], v[212:215], a[72:75]\n"
    "v_exp_f32 v144, v144\n"
    "s_waitcnt lgkmcnt(5)\n"
    "v_mfma_f32_16x16x32_bf16 v[0:3], a[32:35], v[204:207], v[0:3]\n"
    "ds_read_b128 a[8:11], v235 offset:8192\n"
    "v_mfma_f32_16x16x32_bf16 v[4:7], a[32:35], v[208:211], v[4:7]\n"
    "ds_read_b128 a[12:15], v235 offset:12288\n"
    "s_add_u32 s88, s87, 65536\n"
    "v_mfma_f32_16x16x32_bf16 v[8:11], a[32:35], v[212:215], v[8:11]\n"
    "v_exp_f32 v145, v145\n"
    "v_mfma_f32_16x16x32_bf16 v[12:15], a[36:39], v[204:207], v[12:15]\n"
    "ds_read_b128 a[16:19], v234 offset:10240\n"
    "s_add_u32 m0, s86, 98304\n"
    "v_mfma_f32_16x16x32_bf16 v[16:19], a[36:39], v[208:211], v[16:19]\n"
    "v_exp_f32 v146, v146\n"
    "v_mfma_f32_16x16x32_bf16 v[20:23], a[36:39], v[212:215], v[20:23]\n"
    "ds_read_b128 a[20:23], v234 offset:14336\n"
    "buffer_load_dwordx4 v238, s[48:51], s88 offen offset:0 nt lds\n"
    "v_mfma_f32_16x16x32_bf16 v[24:27], a[40:43], v[204:207], v[24:27]\n"
    "ds_read_b128 a[24:27], v235 offset:10240\n"
    "v_mfma_f32_16x16x32_bf16 v[28:31], a[40:43], v[208:211], v[28:31]\n"
    "v_exp_f32 v147, v147\n"
    "v_mfma_f32_16x16x32_bf16 v[32:35], a[40:43], v[212:215], v[32:35]\n"
    "ds_read_b128 a[28:31], v235 offset:14336\n"
    "v_cvt_pk_bf16_f32 v192, v144, v145\n"
    "v_mfma_f32_16x16x32_bf16 v[36:39], a[44:47], v[204:207], v[36:39]\n"
    "v_cvt_pk_bf16_f32 v193, v146, v147\n"
    "ds_read_b64_tr_b16 a[32:33], v236 offset:0\n"
    "v_mfma_f32_16x16x32_bf16 v[40:43], a[44:47], v[208:211], v[40:43]\n"
    "v_exp_f32 v156, v156\n"
    "v_mfma_f32_16x16x32_bf16 v[44:47], a[44:47], v[212:215], v[44:47]\n"
    "v_exp_f32 v157, v157\n"
    "v_mfma_f32_16x16x32_bf16 v[48:51], a[48:51], v[204:207], v[48:51]\n"
    "v_exp_f32 v158, v158\n"
    "v_mfma_f32_16x16x32_bf16 v[52:55], a[48:51], v[208:211], v[52:55]\n"
    "v_exp_f32 v159, v159\n"
    "v_mfma_f32_16x16x32_bf16 v[56:59], a[48:51], v[212:215], v[56:59]\n"
    "ds_read_b64_tr_b16 a[34:35], v236 offset:4096\n"
    "buffer_load_dwordx4 v239, s[48:51], s88 offen offset:1024 nt lds\n"
    "v_mfma_f32_16x16x32_bf16 v[60:63], a[52:55], v[204:207], v[60:63]\n"
    "v_cvt_pk_bf16_f32 v194, v156, v157\n"
    "v_cvt_pk_bf16_f32 v195, v158, v159\n"
    "v_mfma_f32_16x16x32_bf16 v[64:67], a[52:55], v[208:211], v[64:67]\n"
    "buffer_load_dwordx4 v240, s[48:51], s88 offen offset:2048 nt lds\n"
    "ds_read_b64_tr_b16 a[36:37], v237 offset:0\n"
    "v_mfma_f32_16x16x32_bf16 v[68:71], a[52:55], v[212:215], v[68:71]\n"
    "v_exp_f32 v148, v148\n"
    "s_waitcnt lgkmcnt(4)\n"
    "v_mfma_f32_16x16x32_bf16 v[72:75], a[56:59], v[204:207], v[72:75]\n"
    "v_exp_f32 v149, v149\n"
    "v_mfma_f32_16x16x32_bf16 v[76:79], a[56:59], v[208:211], v[76:79]\n"
    "buffer_load_dwordx4 v241, s[48:51], s88 offen offset:3072 nt lds\n"
    "ds_read_b64_tr_b16 a[38:39], v237 offset:4096\n"
    "v_mfma_f32_16x16x32_bf16 v[80:83], a[56:59], v[212:215], v[80:83]\n"
    "v_exp_f32 v150, v150\n"
    "v_mfma_f32_16x16x32_bf16 v[84:87], a[60:63], v[204:207], v[84:87]\n"
    "v_exp_f32 v151, v151\n"
    "v_mfma_f32_16x16x32_bf16 v[88:91], a[60:63], v[208:211], v[88:91]\n"
    "s_and_b32 s75, s69, 1\n"
    "ds_read_b64_tr_b16 a[40:41], v236 offset:512\n"
    "v_mfma_f32_16x16x32_bf16 v[92:95], a[60:63], v[212:215], v[92:95]\n"
    "v_cvt_pk_bf16_f32 v196, v148, v149\n"
    "s_lshr_b32 s47, s69, 1\n"
    "v_mfma_f32_16x16x32_bf16 v[168:171], a[0:3], v[96:99], v[216:219]\n"
    "v_cvt_pk_bf16_f32 v197, v150, v151\n"
    "ds_read_b64_tr_b16 a[42:43], v236 offset:4608\n"
    "v_mfma_f32_16x16x32_bf16 v[172:175], a[0:3], v[100:103], v[220:223]\n"
    "v_exp_f32 v160, v160\n"
    "v_mfma_f32_16x16x32_bf16 v[176:179], a[0:3], v[104:107], v[224:227]\n"
    "s_lshl_b32 s47, s47, 2\n"
    "ds_read_b64_tr_b16 a[44:45], v237 offset:512\n"
    "v_mfma_f32_16x16x32_bf16 v[180:183], a[4:7], v[96:99], v[216:219]\n"
    "v_exp_f32 v161, v161\n"
    "v_mfma_f32_16x16x32_bf16 v[184:187], a[4:7], v[100:103], v[220:223]\n"
    "v_exp_f32 v162, v162\n"
    "v_mfma_f32_16x16x32_bf16 v[188:191], a[4:7], v[104:107], v[224:227]\n"
    "s_lshr_b32 s89, s70, 3\n"
    "ds_read_b64_tr_b16 a[46:47], v237 offset:4608\n"
    "v_mfma_f32_16x16x32_bf16 v[168:171], a[8:11], v[108:111], v[168:171]\n"
    "v_exp_f32 v163, v163\n"
    "v_mfma_f32_16x16x32_bf16 v[172:175], a[8:11], v[112:115], v[172:175]\n"
    "v_cvt_pk_bf16_f32 v198, v160, v161\n"
    "s_add_u32 s47, s47, s89\n"
    "v_mfma_f32_16x16x32_bf16 v[176:179], a[8:11], v[116:119], v[176:179]\n"
    "v_cvt_pk_bf16_f32 v199, v162, v163\n"
    "ds_read_b64_tr_b16 a[48:49], v236 offset:1024\n"
    "v_mfma_f32_16x16x32_bf16 v[180:183], a[12:15], v[108:111], v[180:183]\n"
    "s_lshr_b32 s84, s47, 1\n"
    "ds_read_b64_tr_b16 a[50:51], v236 offset:5120\n"
    "v_mfma_f32_16x16x32_bf16 v[184:187], a[12:15], v[112:115], v[184:187]\n"
    "v_exp_f32 v152, v152\n"
    "v_mfma_f32_16x16x32_bf16 v[188:191], a[12:15], v[116:119], v[188:191]\n"
    "v_exp_f32 v153, v153\n"
    "v_mfma_f32_16x16x32_bf16 v[168:171], a[16:19], v[120:123], v[168:171]\n"
    "s_and_b32 s47, s47, 1\n"
    "ds_read_b64_tr_b16 a[52:53], v237 offset:1024\n"
    "v_mfma_f32_16x16x32_bf16 v[172:175], a[16:19], v[124:127], v[172:175]\n"
    "v_exp_f32 v154, v154\n"
    "v_mfma_f32_16x16x32_bf16 v[176:179], a[16:19], v[128:131], v[176:179]\n"
    "v_exp_f32 v155, v155\n"
    "v_mfma_f32_16x16x32_bf16 v[180:183], a[20:23], v[120:123], v[180:183]\n"
    "s_lshl_b32 s47, s47, 1\n"
    "v_cvt_pk_bf16_f32 v200, v152, v153\n"
    "v_mfma_f32_16x16x32_bf16 v[184:187], a[20:23], v[124:127], v[184:187]\n"
    "ds_read_b64_tr_b16 a[54:55], v237 offset:5120\n"
    "v_cvt_pk_bf16_f32 v201, v154, v155\n"
    "v_mfma_f32_16x16x32_bf16 v[188:191], a[20:23], v[128:131], v[188:191]\n"
    "s_add_u32 s85, s47, s75\n"
    "ds_read_b64_tr_b16 a[56:57], v236 offset:1536\n"
    "v_mfma_f32_16x16x32_bf16 v[168:171], a[24:27], v[132:135], v[168:171]\n"
    "v_exp_f32 v164, v164\n"
    "v_mfma_f32_16x16x32_bf16 v[172:175], a[24:27], v[136:139], v[172:175]\n"
    "v_exp_f32 v165, v165\n"
    "v_mfma_f32_16x16x32_bf16 v[176:179], a[24:27], v[140:143], v[176:179]\n"
    "s_xor_b32 s93, s85, 1\n"
    "ds_read_b64_tr_b16 a[58:59], v236 offset:5632\n"
    "s_waitcnt lgkmcnt(4)\n"
    "v_mfma_f32_16x16x32_bf16 v[180:183], a[28:31], v[132:135], v[180:183]\n"
    "v_exp_f32 v166, v166\n"
    "v_mfma_f32_16x16x32_bf16 v[184:187], a[28:31], v[136:139], v[184:187]\n"
    "v_exp_f32 v167, v167\n"
    "v_mfma_f32_16x16x32_bf16 v[188:191], a[28:31], v[140:143], v[188:191]\n"
    "s_and_b32 s74, s70, 7\n"
    "v_cvt_pk_bf16_f32 v202, v164, v165\n"
    "ds_read_b64_tr_b16 a[60:61], v237 offset:1536\n"
    "ds_read_b64_tr_b16 a[62:63], v237 offset:5632\n"
    "v_cvt_pk_bf16_f32 v203, v166, v167\n"
    "s_lshl_b32 s89, s75, 6\n"
    "s_lshl_b32 s47, s66, 4\n"
    "s_add_u32 s89, s89, s47\n"
    "s_waitcnt vmcnt(13)\n"
    "s_barrier\n"
    "v_add_u32 v234, 32768, v234\n"
    "v_add_u32 v235, 32768, v235\n"
    "v_mfma_f32_16x16x32_bf16 a[64:67], a[76:79], v[192:195], a[64:67]\n"
    "ds_read_b128 a[0:3], v234 offset:0\n"
    "v_mfma_f32_16x16x32_bf16 a[68:71], a[76:79], v[196:199], a[68:71]\n"
    "ds_read_b128 a[4:7], v234 offset:4096\n"
    "v_mfma_f32_16x16x32_bf16 a[72:75], a[76:79], v[200:203], a[72:75]\n"
    "v_exp_f32 v168, v168\n"
    "v_mfma_f32_16x16x32_bf16 v[0:3], a[32:35], v[192:195], v[0:3]\n"
    "ds_read_b128 a[8:11], v235 offset:0\n"
    "v_mfma_f32_16x16x32_bf16 v[4:7], a[32:35], v[196:199], v[4:7]\n"
    "ds_read_b128 a[12:15], v235 offset:4096\n"
    "s_add_u32 s88, s87, 69632\n"
    "v_mfma_f32_16x16x32_bf16 v[8:11], a[32:35], v[200:203], v[8:11]\n"
    "v_exp_f32 v169, v169\n"
    "v_mfma_f32_16x16x32_bf16 v[12:15], a[36:39], v[192:195], v[12:15]\n"
    "ds_read_b128 a[16:19], v234 offset:2048\n"
    "s_add_u32 m0, s86, 102400\n"
    "v_mfma_f32_16x16x32_bf16 v[16:19], a[36:39], v[196:199], v[16:19]\n"
    "v_exp_f32 v170, v170\n"
    "v_mfma_f32_16x16x32_bf16 v[20:23], a[36:39], v[200:203], v[20:23]\n"
    "ds_read_b128 a[20:23], v234 offset:6144\n"
    "buffer_load_dwordx4 v238, s[48:51], s88 offen offset:0 nt lds\n"
    "v_mfma_f32_16x16x32_bf16 v[24:27], a[40:43], v[192:195], v[24:27]\n"
    "ds_read_b128 a[24:27], v235 offset:2048\n"
    "v_mfma_f32_16x16x32_bf16 v[28:31], a[40:43], v[196:199], v[28:31]\n"
    "v_exp_f32 v171, v171\n"
    "v_mfma_f32_16x16x32_bf16 v[32:35], a[40:43], v[200:203], v[32:35]\n"
    "ds_read_b128 a[28:31], v235 offset:6144\n"
    "v_cvt_pk_bf16_f32 v204, v168, v169\n"
    "v_mfma_f32_16x16x32_bf16 v[36:39], a[44:47], v[192:195], v[36:39]\n"
    "v_cvt_pk_bf16_f32 v205, v170, v171\n"
    "ds_read_b64_tr_b16 a[32:33], v236 offset:8192\n"
    "v_mfma_f32_16x16x32_bf16 v[40:43], a[44:47], v[196:199], v[40:43]\n"
    "v_exp_f32 v180, v180\n"
    "v_mfma_f32_16x16x32_bf16 v[44:47], a[44:47], v[200:203], v[44:47]\n"
    "v_exp_f32 v181, v181\n"
    "v_mfma_f32_16x16x32_bf16 v[48:51], a[48:51], v[192:195], v[48:51]\n"
    "v_exp_f32 v182, v182\n"
    "v_mfma_f32_16x16x32_bf16 v[52:55], a[48:51], v[196:199], v[52:55]\n"
    "v_exp_f32 v183, v183\n"
    "v_mfma_f32_16x16x32_bf16 v[56:59], a[48:51], v[200:203], v[56:59]\n"
    "ds_read_b64_tr_b16 a[34:35], v236 offset:12288\n"
    "buffer_load_dwordx4 v239, s[48:51], s88 offen offset:1024 nt lds\n"
    "s_waitcnt lgkmcnt(5)\n"
    "v_mfma_f32_16x16x32_bf16 v[60:63], a[52:55], v[192:195], v[60:63]\n"
    "v_cvt_pk_bf16_f32 v206, v180, v181\n"
    "v_cvt_pk_bf16_f32 v207, v182, v183\n"
    "v_mfma_f32_16x16x32_bf16 v[64:67], a[52:55], v[196:199], v[64:67]\n"
    "buffer_load_dwordx4 v240, s[48:51], s88 offen offset:2048 nt lds\n"
    "ds_read_b64_tr_b16 a[36:37], v237 offset:8192\n"
    "v_mfma_f32_16x16x32_bf16 v[68:71], a[52:55], v[200:203], v[68:71]\n"
    "v_exp_f32 v172, v172\n"
    "v_mfma_f32_16x16x32_bf16 v[72:75], a[56:59], v[192:195], v[72:75]\n"
    "v_exp_f32 v173, v173\n"
    "v_mfma_f32_16x16x32_bf16 v[76:79], a[56:59], v[196:199], v[76:79]\n"
    "buffer_load_dwordx4 v241, s[48:51], s88 offen offset:3072 nt lds\n"
    "ds_read_b64_tr_b16 a[38:39], v237 offset:12288\n"
    "v_mfma_f32_16x16x32_bf16 v[80:83], a[56:59], v[200:203], v[80:83]\n"
    "v_exp_f32 v174, v174\n"
    "v_mfma_f32_16x16x32_bf16 v[84:87], a[60:63], v[192:195], v[84:87]\n"
    "v_exp_f32 v175, v175\n"
    "v_mfma_f32_16x16x32_bf16 v[88:91], a[60:63], v[196:199], v[88:91]\n"
    "s_lshl_b32 s47, s74, 7\n"
    "ds_read_b64_tr_b16 a[40:41], v236 offset:8704\n"
    "v_mfma_f32_16x16x32_bf16 v[92:95], a[60:63], v[200:203], v[92:95]\n"
    "v_cvt_pk_bf16_f32 v208, v172, v173\n"
    "s_add_u32 s47, s47, s89\n"
    "v_mfma_f32_16x16x32_bf16 v[144:147], a[0:3], v[96:99], v[216:219]\n"
    "v_cvt_pk_bf16_f32 v209, v174, v175\n"
    "ds_read_b64_tr_b16 a[42:43], v236 offset:12800\n"
    "v_mfma_f32_16x16x32_bf16 v[148:151], a[0:3], v[100:103], v[220:223]\n"
    "v_exp_f32 v184, v184\n"
    "v_mfma_f32_16x16x32_bf16 v[152:155], a[0:3], v[104:107], v[224:227]\n"
    "s_mul_i32 s90, s47, 5120\n"
    "ds_read_b64_tr_b16 a[44:45], v237 offset:8704\n"
    "v_mfma_f32_16x16x32_bf16 v[156:159], a[4:7], v[96:99], v[216:219]\n"
    "v_exp_f32 v185, v185\n"
    "v_mfma_f32_16x16x32_bf16 v[160:163], a[4:7], v[100:103], v[220:223]\n"
    "v_exp_f32 v186, v186\n"
    "v_mfma_f32_16x16x32_bf16 v[164:167], a[4:7], v[104:107], v[224:227]\n"
    "s_lshl_b32 s47, s84, 2\n"
    "ds_read_b64_tr_b16 a[46:47], v237 offset:12800\n"
    "v_mfma_f32_16x16x32_bf16 v[144:147], a[8:11], v[108:111], v[144:147]\n"
    "v_exp_f32 v187, v187\n"
    "v_mfma_f32_16x16x32_bf16 v[148:151], a[8:11], v[112:115], v[148:151]\n"
    "v_cvt_pk_bf16_f32 v210, v184, v185\n"
    "s_add_u32 s47, s47, s93\n"
    "v_mfma_f32_16x16x32_bf16 v[152:155], a[8:11], v[116:119], v[152:155]\n"
    "v_cvt_pk_bf16_f32 v211, v186, v187\n"
    "ds_read_b64_tr_b16 a[48:49], v236 offset:9216\n"
    "v_mfma_f32_16x16x32_bf16 v[156:159], a[12:15], v[108:111], v[156:159]\n"
    "s_lshl_b32 s47, s47, 7\n"
    "ds_read_b64_tr_b16 a[50:51], v236 offset:13312\n"
    "v_mfma_f32_16x16x32_bf16 v[160:163], a[12:15], v[112:115], v[160:163]\n"
    "v_exp_f32 v176, v176\n"
    "v_mfma_f32_16x16x32_bf16 v[164:167], a[12:15], v[116:119], v[164:167]\n"
    "v_exp_f32 v177, v177\n"
    "v_mfma_f32_16x16x32_bf16 v[144:147], a[16:19], v[120:123], v[144:147]\n"
    "s_add_u32 s47, s47, s89\n"
    "ds_read_b64_tr_b16 a[52:53], v237 offset:9216\n"
    "v_mfma_f32_16x16x32_bf16 v[148:151], a[16:19], v[124:127], v[148:151]\n"
    "v_exp_f32 v178, v178\n"
    "v_mfma_f32_16x16x32_bf16 v[152:155], a[16:19], v[128:131], v[152:155]\n"
    "v_exp_f32 v179, v179\n"
    "s_waitcnt lgkmcnt(4)\n"
    "v_mfma_f32_16x16x32_bf16 v[156:159], a[20:23], v[120:123], v[156:159]\n"
    "s_mul_i32 s91, s47, 3072\n"
    "v_cvt_pk_bf16_f32 v212, v176, v177\n"
    "v_mfma_f32_16x16x32_bf16 v[160:163], a[20:23], v[124:127], v[160:163]\n"
    "ds_read_b64_tr_b16 a[54:55], v237 offset:13312\n"
    "v_cvt_pk_bf16_f32 v213, v178, v179\n"
    "v_mfma_f32_16x16x32_bf16 v[164:167], a[20:23], v[128:131], v[164:167]\n"
    "s_add_u32 s76, s90, 0\n"
    "ds_read_b64_tr_b16 a[56:57], v236 offset:9728\n"
    "v_mfma_f32_16x16x32_bf16 v[144:147], a[24:27], v[132:135], v[144:147]\n"
    "v_exp_f32 v188, v188\n"
    "v_mfma_f32_16x16x32_bf16 v[148:151], a[24:27], v[136:139], v[148:151]\n"
    "v_exp_f32 v189, v189\n"
    "v_mfma_f32_16x16x32_bf16 v[152:155], a[24:27], v[140:143], v[152:155]\n"
    "s_add_u32 s52, s91, 0\n"
    "ds_read_b64_tr_b16 a[58:59], v236 offset:13824\n"
    "v_mfma_f32_16x16x32_bf16 v[156:159], a[28:31], v[132:135], v[156:159]\n"
    "v_exp_f32 v190, v190\n"
    "v_mfma_f32_16x16x32_bf16 v[160:163], a[28:31], v[136:139], v[160:163]\n"
    "v_exp_f32 v191, v191\n"
    "v_mfma_f32_16x16x32_bf16 v[164:167], a[28:31], v[140:143], v[164:167]\n"
    "s_add_u32 s77, s90, 5120\n"
    "v_cvt_pk_bf16_f32 v214, v188, v189\n"
    "ds_read_b64_tr_b16 a[60:61], v237 offset:9728\n"
    "ds_read_b64_tr_b16 a[62:63], v237 offset:13824\n"
    "v_cvt_pk_bf16_f32 v215, v190, v191\n"
    "s_add_u32 s53, s91, 3072\n"
    "s_add_u32 s78, s90, 10240\n"
    "s_add_u32 s54, s91, 6144\n"
    "v_add_u32 v236, 32768, v236\n"
    "v_add_u32 v237, 32768, v237\n"
    "v_mfma_f32_16x16x32_bf16 a[64:67], a[76:79], v[204:207], a[64:67]\n"
    "ds_read_b128 a[0:3], v234 offset:8192\n"
    "v_mfma_f32_16x16x32_bf16 a[68:71], a[76:79], v[208:211], a[68:71]\n"
    "ds_read_b128 a[4:7], v234 offset:12288\n"
    "v_mfma_f32_16x16x32_bf16 a[72:75], a[76:79], v[212:215], a[72:75]\n"
    "v_exp_f32 v144, v144\n"
    "v_mfma_f32_16x16x32_bf16 v[0:3], a[32:35], v[204:207], v[0:3]\n"
    "ds_read_b128 a[8:11], v235 offset:8192\n"
    "v_mfma_f32_16x16x32_bf16 v[4:7], a[32:35], v[208:211], v[4:7]\n"
    "ds_read_b128 a[12:15], v235 offset:12288\n"
    "s_add_u32 s88, s87, 81920\n"
    "v_mfma_f32_16x16x32_bf16 v[8:11], a[32:35], v[212:215], v[8:11]\n"
    "v_exp_f32 v145, v145\n"
    "v_mfma_f32_16x16x32_bf16 v[12:15], a[36:39], v[204:207], v[12:15]\n"
    "ds_read_b128 a[16:19], v234 offset:10240\n"
    "s_add_u32 m0, s86, 131072\n"
    "v_mfma_f32_16x16x32_bf16 v[16:19], a[36:39], v[208:211], v[16:19]\n"
    "v_exp_f32 v146, v146\n"
    "v_mfma_f32_16x16x32_bf16 v[20:23], a[36:39], v[212:215], v[20:23]\n"
    "ds_read_b128 a[20:23], v234 offset:14336\n"
    "v_mfma_f32_16x16x32_bf16 v[24:27], a[40:43], v[204:207], v[24:27]\n"
    "ds_read_b128 a[24:27], v235 offset:10240\n"
    "buffer_load_dwordx4 v238, s[48:51], s88 offen offset:0 nt lds\n"
    "v_mfma_f32_16x16x32_bf16 v[28:31], a[40:43], v[208:211], v[28:31]\n"
    "v_exp_f32 v147, v147\n"
    "v_mfma_f32_16x16x32_bf16 v[32:35], a[40:43], v[212:215], v[32:35]\n"
    "ds_read_b128 a[28:31], v235 offset:14336\n"
    "v_cvt_pk_bf16_f32 v192, v144, v145\n"
    "s_waitcnt lgkmcnt(6)\n"
    "v_mfma_f32_16x16x32_bf16 v[36:39], a[44:47], v[204:207], v[36:39]\n"
    "v_cvt_pk_bf16_f32 v193, v146, v147\n"
    "ds_read_b64_tr_b16 a[32:33], v236 offset:0\n"
    "v_mfma_f32_16x16x32_bf16 v[40:43], a[44:47], v[208:211], v[40:43]\n"
    "v_exp_f32 v156, v156\n"
    "v_mfma_f32_16x16x32_bf16 v[44:47], a[44:47], v[212:215], v[44:47]\n"
    "v_exp_f32 v157, v157\n"
    "v_mfma_f32_16x16x32_bf16 v[48:51], a[48:51], v[204:207], v[48:51]\n"
    "v_exp_f32 v158, v158\n"
    "v_mfma_f32_16x16x32_bf16 v[52:55], a[48:51], v[208:211], v[52:55]\n"
    "v_exp_f32 v159, v159\n"
    "v_mfma_f32_16x16x32_bf16 v[56:59], a[48:51], v[212:215], v[56:59]\n"
    "ds_read_b64_tr_b16 a[34:35], v236 offset:4096\n"
    "v_cvt_pk_bf16_f32 v194, v156, v157\n"
    "v_mfma_f32_16x16x32_bf16 v[60:63], a[52:55], v[204:207], v[60:63]\n"
    "buffer_load_dwordx4 v239, s[48:51], s88 offen offset:1024 nt lds\n"
    "v_cvt_pk_bf16_f32 v195, v158, v159\n"
    "v_mfma_f32_16x16x32_bf16 v[64:67], a[52:55], v[208:211], v[64:67]\n"
    "ds_read_b64_tr_b16 a[36:37], v237 offset:0\n"
    "buffer_load_dwordx4 v240, s[48:51], s88 offen offset:2048 nt lds\n"
    "v_mfma_f32_16x16x32_bf16 v[68:71], a[52:55], v[212:215], v[68:71]\n"
    "v_exp_f32 v148, v148\n"
    "v_mfma_f32_16x16x32_bf16 v[72:75], a[56:59], v[204:207], v[72:75]\n"
    "v_exp_f32 v149, v149\n"
    "v_mfma_f32_16x16x32_bf16 v[76:79], a[56:59], v[208:211], v[76:79]\n"
    "ds_read_b64_tr_b16 a[38:39], v237 offset:4096\n"
    "buffer_load_dwordx4 v241, s[48:51], s88 offen offset:3072 nt lds\n"
    "v_mfma_f32_16x16x32_bf16 v[80:83], a[56:59], v[212:215], v[80:83]\n"
    "v_exp_f32 v150, v150\n"
    "v_mfma_f32_16x16x32_bf16 v[84:87], a[60:63], v[204:207], v[84:87]\n"
    "v_exp_f32 v151, v151\n"
    "v_mfma_f32_16x16x32_bf16 v[88:91], a[60:63], v[208:211], v[88:91]\n"
    "ds_read_b64_tr_b16 a[40:41], v236 offset:512\n"
    "v_cvt_pk_bf16_f32 v196, v148, v149\n"
    "v_mfma_f32_16x16x32_bf16 v[92:95], a[60:63], v[212:215], v[92:95]\n"
    "s_add_u32 s79, s90, 15360\n"
    "ds_read_b64_tr_b16 a[42:43], v236 offset:4608\n"
    "v_mfma_f32_16x16x32_bf16 v[168:171], a[0:3], v[96:99], v[216:219]\n"
    "v_cvt_pk_bf16_f32 v197, v150, v151\n"
    "s_add_u32 s55, s91, 9216\n"
    "v_mfma_f32_16x16x32_bf16 v[172:175], a[0:3], v[100:103], v[220:223]\n"
    "v_exp_f32 v160, v160\n"
    "v_mfma_f32_16x16x32_bf16 v[176:179], a[0:3], v[104:107], v[224:227]\n"
    "ds_read_b64_tr_b16 a[44:45], v237 offset:512\n"
    "s_sub_u32 s47, s85, s93\n"
    "v_mfma_f32_16x16x32_bf16 v[180:183], a[4:7], v[96:99], v[216:219]\n"
    "v_exp_f32 v161, v161\n"
    "v_mfma_f32_16x16x32_bf16 v[184:187], a[4:7], v[100:103], v[220:223]\n"
    "v_exp_f32 v162, v162\n"
    "v_mfma_f32_16x16x32_bf16 v[188:191], a[4:7], v[104:107], v[224:227]\n"
    "ds_read_b64_tr_b16 a[46:47], v237 offset:4608\n"
    "s_mul_i32 s46, s47, 393216\n"
    "s_waitcnt lgkmcnt(4)\n"
    "v_mfma_f32_16x16x32_bf16 v[168:171], a[8:11], v[108:111], v[168:171]\n"
    "v_exp_f32 v163, v163\n"
    "v_mfma_f32_16x16x32_bf16 v[172:175], a[8:11], v[112:115], v[172:175]\n"
    "v_cvt_pk_bf16_f32 v198, v160, v161\n"
    "ds_read_b64_tr_b16 a[48:49], v236 offset:1024\n"
    "v_mfma_f32_16x16x32_bf16 v[176:179], a[8:11], v[116:119], v[176:179]\n"
    "v_cvt_pk_bf16_f32 v199, v162, v163\n"
    "v_lshlrev_b32 v230, 4, v254\n"
    "v_mfma_f32_16x16x32_bf16 v[180:183], a[12:15], v[108:111], v[180:183]\n"
    "v_exp_f32 v152, v152\n"
    "v_mfma_f32_16x16x32_bf16 v[184:187], a[12:15], v[112:115], v[184:187]\n"
    "ds_read_b64_tr_b16 a[50:51], v236 offset:5120\n"
    "s_and_b32 s47, s74, 1\n"
    "v_mfma_f32_16x16x32_bf16 v[188:191], a[12:15], v[116:119], v[188:191]\n"
    "v_exp_f32 v153, v153\n"
    "v_mfma_f32_16x16x32_bf16 v[168:171], a[16:19], v[120:123], v[168:171]\n"
    "v_exp_f32 v154, v154\n"
    "v_mfma_f32_16x16x32_bf16 v[172:175], a[16:19], v[124:127], v[172:175]\n"
    "ds_read_b64_tr_b16 a[52:53], v237 offset:1024\n"
    "v_mfma_f32_16x16x32_bf16 v[176:179], a[16:19], v[128:131], v[176:179]\n"
    "v_exp_f32 v155, v155\n"
    "v_mfma_f32_16x16x32_bf16 v[180:183], a[20:23], v[120:123], v[180:183]\n"
    "v_cvt_pk_bf16_f32 v200, v152, v153\n"
    "ds_read_b64_tr_b16 a[54:55], v237 offset:5120\n"
    "v_mfma_f32_16x16x32_bf16 v[184:187], a[20:23], v[124:127], v[184:187]\n"
    "s_cmp_eq_u32 s47, 0\n"
    "s_mov_b32 s47, 67108864\n"
    "s_cselect_b32 s47, 4096, s47\n"
    "v_mfma_f32_16x16x32_bf16 v[188:191], a[20:23], v[128:131], v[188:191]\n"
    "v_cvt_pk_bf16_f32 v201, v154, v155\n"
    "ds_read_b64_tr_b16 a[56:57], v236 offset:1536\n"
    "v_mfma_f32_16x16x32_bf16 v[168:171], a[24:27], v[132:135], v[168:171]\n"
    "v_exp_f32 v164, v164\n"
    "v_mfma_f32_16x16x32_bf16 v[172:175], a[24:27], v[136:139], v[172:175]\n"
    "v_exp_f32 v165, v165\n"
    "v_mfma_f32_16x16x32_bf16 v[176:179], a[24:27], v[140:143], v[176:179]\n"
    "v_add_u32 v231, s47, v230\n"
    "ds_read_b64_tr_b16 a[58:59], v236 offset:5632\n"
    "v_mfma_f32_16x16x32_bf16 v[180:183], a[28:31], v[132:135], v[180:183]\n"
    "v_exp_f32 v166, v166\n"
    "v_mfma_f32_16x16x32_bf16 v[184:187], a[28:31], v[136:139], v[184:187]\n"
    "v_exp_f32 v167, v167\n"
    "v_mfma_f32_16x16x32_bf16 v[188:191], a[28:31], v[140:143], v[188:191]\n"
    "s_lshl_b32 s96, s66, 14\n"
    "v_cvt_pk_bf16_f32 v202, v164, v165\n"
    "ds_read_b64_tr_b16 a[60:61], v237 offset:1536\n"
    "ds_read_b64_tr_b16 a[62:63], v237 offset:5632\n"
    "v_cvt_pk_bf16_f32 v203, v166, v167\n"
    "s_add_u32 s96, s96, 65536\n"
    "s_mov_b64 s[60:61], s[18:19]\n"
    "s_and_b32 s61, s61, 65535\n"
    "s_waitcnt vmcnt(12)\n"
    "s_barrier\n"
    "v_add_u32 v234, 32768, v234\n"
    "v_add_u32 v235, 32768, v235\n"
    "v_mfma_f32_16x16x32_bf16 a[64:67], a[76:79], v[192:195], a[64:67]\n"
    "ds_read_b128 a[0:3], v234 offset:0\n"
    "v_mfma_f32_16x16x32_bf16 a[68:71], a[76:79], v[196:199], a[68:71]\n"
    "ds_read_b128 a[4:7], v234 offset:4096\n"
    "v_mfma_f32_16x16x32_bf16 a[72:75], a[76:79], v[200:203], a[72:75]\n"
    "v_exp_f32 v168, v168\n"
    "v_mfma_f32_16x16x32_bf16 v[0:3], a[32:35], v[192:195], v[0:3]\n"
    "ds_read_b128 a[8:11], v235 offset:0\n"
    "v_mfma_f32_16x16x32_bf16 v[4:7], a[32:35], v[196:199], v[4:7]\n"
    "ds_read_b128 a[12:15], v235 offset:4096\n"
    "s_add_u32 s88, s87, 86016\n"
    "v_mfma_f32_16x16x32_bf16 v[8:11], a[32:35], v[200:203], v[8:11]\n"
    "v_exp_f32 v169, v169\n"
    "v_mfma_f32_16x16x32_bf16 v[12:15], a[36:39], v[192:195], v[12:15]\n"
    "ds_read_b128 a[16:19], v234 offset:2048\n"
    "s_add_u32 m0, s86, 135168\n"
    "v_mfma_f32_16x16x32_bf16 v[16:19], a[36:39], v[196:199], v[16:19]\n"
    "v_exp_f32 v170, v170\n"
    "v_mfma_f32_16x16x32_bf16 v[20:23], a[36:39], v[200:203], v[20:23]\n"
    "ds_read_b128 a[20:23], v234 offset:6144\n"
    "buffer_load_dwordx4 v238, s[48:51], s88 offen offset:0 nt lds\n"
    "s_waitcnt lgkmcnt(6)\n"
    "v_mfma_f32_16x16x32_bf16 v[24:27], a[40:43], v[192:195], v[24:27]\n"
    "ds_read_b128 a[24:27], v235 offset:2048\n"
    "v_mfma_f32_16x16x32_bf16 v[28:31], a[40:43], v[196:199], v[28:31]\n"
    "v_exp_f32 v171, v171\n"
    "v_mfma_f32_16x16x32_bf16 v[32:35], a[40:43], v[200:203], v[32:35]\n"
    "ds_read_b128 a[28:31], v235 offset:6144\n"
    "v_cvt_pk_bf16_f32 v204, v168, v169\n"
    "v_mfma_f32_16x16x32_bf16 v[36:39], a[44:47], v[192:195], v[36:39]\n"
    "v_cvt_pk_bf16_f32 v205, v170, v171\n"
    "ds_read_b64_tr_b16 a[32:33], v236 offset:8192\n"
    "v_mfma_f32_16x16x32_bf16 v[40:43], a[44:47], v[196:199], v[40:43]\n"
    "v_exp_f32 v180, v180\n"
    "v_mfma_f32_16x16x32_bf16 v[44:47], a[44:47], v[200:203], v[44:47]\n"
    "v_exp_f32 v181, v181\n"
    "v_mfma_f32_16x16x32_bf16 v[48:51], a[48:51], v[192:195], v[48:51]\n"
    "v_exp_f32 v182, v182\n"
    "v_mfma_f32_16x16x32_bf16 v[52:55], a[48:51], v[196:199], v[52:55]\n"
    "v_exp_f32 v183, v183\n"
    "v_mfma_f32_16x16x32_bf16 v[56:59], a[48:51], v[200:203], v[56:59]\n"
    "ds_read_b64_tr_b16 a[34:35], v236 offset:12288\n"
    "buffer_load_dwordx4 v239, s[48:51], s88 offen offset:1024 nt lds\n"
    "v_mfma_f32_16x16x32_bf16 v[60:63], a[52:55], v[192:195], v[60:63]\n"
    "v_cvt_pk_bf16_f32 v206, v180, v181\n"
    "v_cvt_pk_bf16_f32 v207, v182, v183\n"
    "v_mfma_f32_16x16x32_bf16 v[64:67], a[52:55], v[196:199], v[64:67]\n"
    "buffer_load_dwordx4 v240, s[48:51], s88 offen offset:2048 nt lds\n"
    "ds_read_b64_tr_b16 a[36:37], v237 offset:8192\n"
    "v_mfma_f32_16x16x32_bf16 v[68:71], a[52:55], v[200:203], v[68:71]\n"
    "v_exp_f32 v172, v172\n"
    "v_mfma_f32_16x16x32_bf16 v[72:75], a[56:59], v[192:195], v[72:75]\n"
    "v_exp_f32 v173, v173\n"
    "v_mfma_f32_16x16x32_bf16 v[76:79], a[56:59], v[196:199], v[76:79]\n"
    "buffer_load_dwordx4 v241, s[48:51], s88 offen offset:3072 nt lds\n"
    "ds_read_b64_tr_b16 a[38:39], v237 offset:12288\n"
    "v_mfma_f32_16x16x32_bf16 v[80:83], a[56:59], v[200:203], v[80:83]\n"
    "v_exp_f32 v174, v174\n"
    "v_mfma_f32_16x16x32_bf16 v[84:87], a[60:63], v[192:195], v[84:87]\n"
    "v_exp_f32 v175, v175\n"
    "v_mfma_f32_16x16x32_bf16 v[88:91], a[60:63], v[196:199], v[88:91]\n"
    "s_mov_b32 s62, 1769472\n"
    "ds_read_b64_tr_b16 a[40:41], v236 offset:8704\n"
    "v_mfma_f32_16x16x32_bf16 v[92:95], a[60:63], v[200:203], v[92:95]\n"
    "v_cvt_pk_bf16_f32 v208, v172, v173\n"
    "s_mov_b32 s63, 131072\n"
    "s_waitcnt lgkmcnt(3)\n"
    "v_mfma_f32_16x16x32_bf16 v[144:147], a[0:3], v[96:99], v[216:219]\n"
    "v_cvt_pk_bf16_f32 v209, v174, v175\n"
    "ds_read_b64_tr_b16 a[42:43], v236 offset:12800\n"
    "v_mfma_f32_16x16x32_bf16 v[148:151], a[0:3], v[100:103], v[220:223]\n"
    "v_exp_f32 v184, v184\n"
    "v_mfma_f32_16x16x32_bf16 v[152:155], a[0:3], v[104:107], v[224:227]\n"
    "s_mul_i32 s47, s84, 192\n"
    "ds_read_b64_tr_b16 a[44:45], v237 offset:8704\n"
    "v_mfma_f32_16x16x32_bf16 v[156:159], a[4:7], v[96:99], v[216:219]\n"
    "v_exp_f32 v185, v185\n"
    "v_mfma_f32_16x16x32_bf16 v[160:163], a[4:7], v[100:103], v[220:223]\n"
    "v_exp_f32 v186, v186\n"
    "v_mfma_f32_16x16x32_bf16 v[164:167], a[4:7], v[104:107], v[224:227]\n"
    "s_mul_i32 s89, s85, 48\n"
    "ds_read_b64_tr_b16 a[46:47], v237 offset:12800\n"
    "v_mfma_f32_16x16x32_bf16 v[144:147], a[8:11], v[108:111], v[144:147]\n"
    "v_exp_f32 v187, v187\n"
    "v_mfma_f32_16x16x32_bf16 v[148:151], a[8:11], v[112:115], v[148:151]\n"
    "v_cvt_pk_bf16_f32 v210, v184, v185\n"
    "s_add_u32 s47, s47, s89\n"
    "v_mfma_f32_16x16x32_bf16 v[152:155], a[8:11], v[116:119], v[152:155]\n"
    "v_cvt_pk_bf16_f32 v211, v186, v187\n"
    "ds_read_b64_tr_b16 a[48:49], v236 offset:9216\n"
    "v_mfma_f32_16x16x32_bf16 v[156:159], a[12:15], v[108:111], v[156:159]\n"
    "s_mul_i32 s47, s47, 1152\n"
    "ds_read_b64_tr_b16 a[50:51], v236 offset:13312\n"
    "v_mfma_f32_16x16x32_bf16 v[160:163], a[12:15], v[112:115], v[160:163]\n"
    "v_exp_f32 v176, v176\n"
    "v_mfma_f32_16x16x32_bf16 v[164:167], a[12:15], v[116:119], v[164:167]\n"
    "v_exp_f32 v177, v177\n"
    "v_mfma_f32_16x16x32_bf16 v[144:147], a[16:19], v[120:123], v[144:147]\n"
    "s_lshr_b32 s89, s74, 1\n"
    "ds_read_b64_tr_b16 a[52:53], v237 offset:9216\n"
    "v_mfma_f32_16x16x32_bf16 v[148:151], a[16:19], v[124:127], v[148:151]\n"
    "v_exp_f32 v178, v178\n"
    "v_mfma_f32_16x16x32_bf16 v[152:155], a[16:19], v[128:131], v[152:155]\n"
    "v_exp_f32 v179, v179\n"
    "v_mfma_f32_16x16x32_bf16 v[156:159], a[20:23], v[120:123], v[156:159]\n"
    "s_mul_i32 s89, s89, 9\n"
    "v_cvt_pk_bf16_f32 v212, v176, v177\n"
    "v_mfma_f32_16x16x32_bf16 v[160:163], a[20:23], v[124:127], v[160:163]\n"
    "ds_read_b64_tr_b16 a[54:55], v237 offset:13312\n"
    "v_cvt_pk_bf16_f32 v213, v178, v179\n"
    "v_mfma_f32_16x16x32_bf16 v[164:167], a[20:23], v[128:131], v[164:167]\n"
    "s_and_b32 s90, s74, 1\n"
    "ds_read_b64_tr_b16 a[56:57], v236 offset:9728\n"
    "v_mfma_f32_16x16x32_bf16 v[144:147], a[24:27], v[132:135], v[144:147]\n"
    "v_exp_f32 v188, v188\n"
    "v_mfma_f32_16x16x32_bf16 v[148:151], a[24:27], v[136:139], v[148:151]\n"
    "v_exp_f32 v189, v189\n"
    "v_mfma_f32_16x16x32_bf16 v[152:155], a[24:27], v[140:143], v[152:155]\n"
    "s_mul_i32 s91, s90, 5\n"
    "ds_read_b64_tr_b16 a[58:59], v236 offset:13824\n"
    "v_mfma_f32_16x16x32_bf16 v[156:159], a[28:31], v[132:135], v[156:159]\n"
    "v_exp_f32 v190, v190\n"
    "v_mfma_f32_16x16x32_bf16 v[160:163], a[28:31], v[136:139], v[160:163]\n"
    "v_exp_f32 v191, v191\n"
    "v_mfma_f32_16x16x32_bf16 v[164:167], a[28:31], v[140:143], v[164:167]\n"
    "s_add_u32 s89, s89, s91\n"
    "v_cvt_pk_bf16_f32 v214, v188, v189\n"
    "ds_read_b64_tr_b16 a[60:61], v237 offset:9728\n"
    "ds_read_b64_tr_b16 a[62:63], v237 offset:13824\n"
    "v_cvt_pk_bf16_f32 v215, v190, v191\n"
    "s_lshl_b32 s89, s89, 5\n"
    "s_add_u32 s47, s47, s89\n"
    "s_add_u32 s89, s66, 0\n"
    "v_add_u32 v236, 32768, v236\n"
    "v_add_u32 v237, 32768, v237\n"
    "v_mfma_f32_16x16x32_bf16 a[64:67], a[76:79], v[204:207], a[64:67]\n"
    "ds_read_b128 a[0:3], v234 offset:8192\n"
    "v_mfma_f32_16x16x32_bf16 a[68:71], a[76:79], v[208:211], a[68:71]\n"
    "ds_read_b128 a[4:7], v234 offset:12288\n"
    "v_mfma_f32_16x16x32_bf16 a[72:75], a[76:79], v[212:215], a[72:75]\n"
    "v_exp_f32 v144, v144\n"
    "v_mfma_f32_16x16x32_bf16 v[0:3], a[32:35], v[204:207], v[0:3]\n"
    "ds_read_b128 a[8:11], v235 offset:8192\n"
    "v_mfma_f32_16x16x32_bf16 v[4:7], a[32:35], v[208:211], v[4:7]\n"
    "ds_read_b128 a[12:15], v235 offset:12288\n"
    "s_add_u32 s88, s87, 98304\n"
    "v_mfma_f32_16x16x32_bf16 v[8:11], a[32:35], v[212:215], v[8:11]\n"
    "v_exp_f32 v145, v145\n"
    "s_waitcnt lgkmcnt(6)\n"
    "v_mfma_f32_16x16x32_bf16 v[12:15], a[36:39], v[204:207], v[12:15]\n"
    "ds_read_b128 a[16:19], v234 offset:10240\n"
    "s_add_u32 m0, s86, 0\n"
    "v_mfma_f32_16x16x32_bf16 v[16:19], a[36:39], v[208:211], v[16:19]\n"
    "v_exp_f32 v146, v146\n"
    "v_mfma_f32_16x16x32_bf16 v[20:23], a[36:39], v[212:215], v[20:23]\n"
    "ds_read_b128 a[20:23], v234 offset:14336\n"
    "buffer_load_dwordx4 v238, s[48:51], s88 offen offset:0 nt lds\n"
    "v_mfma_f32_16x16x32_bf16 v[24:27], a[40:43], v[204:207], v[24:27]\n"
    "ds_read_b128 a[24:27], v235 offset:10240\n"
    "v_mfma_f32_16x16x32_bf16 v[28:31], a[40:43], v[208:211], v[28:31]\n"
    "v_exp_f32 v147, v147\n"
    "v_mfma_f32_16x16x32_bf16 v[32:35], a[40:43], v[212:215], v[32:35]\n"
    "ds_read_b128 a[28:31], v235 offset:14336\n"
    "v_cvt_pk_bf16_f32 v192, v144, v145\n"
    "v_mfma_f32_16x16x32_bf16 v[36:39], a[44:47], v[204:207], v[36:39]\n"
    "v_cvt_pk_bf16_f32 v193, v146, v147\n"
    "ds_read_b64_tr_b16 a[32:33], v236 offset:0\n"
    "v_mfma_f32_16x16x32_bf16 v[40:43], a[44:47], v[208:211], v[40:43]\n"
    "v_exp_f32 v156, v156\n"
    "v_mfma_f32_16x16x32_bf16 v[44:47], a[44:47], v[212:215], v[44:47]\n"
    "v_exp_f32 v157, v157\n"
    "v_mfma_f32_16x16x32_bf16 v[48:51], a[48:51], v[204:207], v[48:51]\n"
    "v_exp_f32 v158, v158\n"
    "v_mfma_f32_16x16x32_bf16 v[52:55], a[48:51], v[208:211], v[52:55]\n"
    "v_exp_f32 v159, v159\n"
    "v_mfma_f32_16x16x32_bf16 v[56:59], a[48:51], v[212:215], v[56:59]\n"
    "ds_read_b64_tr_b16 a[34:35], v236 offset:4096\n"
    "v_cvt_pk_bf16_f32 v194, v156, v157\n"
    "v_mfma_f32_16x16x32_bf16 v[60:63], a[52:55], v[204:207], v[60:63]\n"
    "buffer_load_dwordx4 v239, s[48:51], s88 offen offset:1024 nt lds\n"
    "v_cvt_pk_bf16_f32 v195, v158, v159\n"
    "v_mfma_f32_16x16x32_bf16 v[64:67], a[52:55], v[208:211], v[64:67]\n"
    "ds_read_b64_tr_b16 a[36:37], v237 offset:0\n"
    "buffer_load_dwordx4 v240, s[48:51], s88 offen offset:2048 nt lds\n"
    "v_mfma_f32_16x16x32_bf16 v[68:71], a[52:55], v[212:215], v[68:71]\n"
    "v_exp_f32 v148, v148\n"
    "v_mfma_f32_16x16x32_bf16 v[72:75], a[56:59], v[204:207], v[72:75]\n"
    "v_exp_f32 v149, v149\n"
    "v_mfma_f32_16x16x32_bf16 v[76:79], a[56:59], v[208:211], v[76:79]\n"
    "ds_read_b64_tr_b16 a[38:39], v237 offset:4096\n"
    "buffer_load_dwordx4 v241, s[48:51], s88 offen offset:3072 nt lds\n"
    "v_mfma_f32_16x16x32_bf16 v[80:83], a[56:59], v[212:215], v[80:83]\n"
    "v_exp_f32 v150, v150\n"
    "s_waitcnt lgkmcnt(3)\n"
    "v_mfma_f32_16x16x32_bf16 v[84:87], a[60:63], v[204:207], v[84:87]\n"
    "v_exp_f32 v151, v151\n"
    "v_mfma_f32_16x16x32_bf16 v[88:91], a[60:63], v[208:211], v[88:91]\n"
    "ds_read_b64_tr_b16 a[40:41], v236 offset:512\n"
    "s_mul_i32 s91, s89, 21846\n"
    "v_mfma_f32_16x16x32_bf16 v[92:95], a[60:63], v[212:215], v[92:95]\n"
    "v_cvt_pk_bf16_f32 v196, v148, v149\n"
    "ds_read_b64_tr_b16 a[42:43], v236 offset:4608\n"
    "v_mfma_f32_16x16x32_bf16 v[168:171], a[0:3], v[96:99], v[216:219]\n"
    "v_cvt_pk_bf16_f32 v197, v150, v151\n"
    "s_lshr_b32 s91, s91, 16\n"
    "v_mfma_f32_16x16x32_bf16 v[172:175], a[0:3], v[100:103], v[220:223]\n"
    "v_exp_f32 v160, v160\n"
    "v_mfma_f32_16x16x32_bf16 v[176:179], a[0:3], v[104:107], v[224:227]\n"
    "ds_read_b64_tr_b16 a[44:45], v237 offset:512\n"
    "s_mul_i32 s92, s91, 3\n"
    "v_mfma_f32_16x16x32_bf16 v[180:183], a[4:7], v[96:99], v[216:219]\n"
    "v_exp_f32 v161, v161\n"
    "v_mfma_f32_16x16x32_bf16 v[184:187], a[4:7], v[100:103], v[220:223]\n"
    "v_exp_f32 v162, v162\n"
    "v_mfma_f32_16x16x32_bf16 v[188:191], a[4:7], v[104:107], v[224:227]\n"
    "ds_read_b64_tr_b16 a[46:47], v237 offset:4608\n"
    "s_sub_u32 s92, s89, s92\n"
    "v_mfma_f32_16x16x32_bf16 v[168:171], a[8:11], v[108:111], v[168:171]\n"
    "v_exp_f32 v163, v163\n"
    "v_mfma_f32_16x16x32_bf16 v[172:175], a[8:11], v[112:115], v[172:175]\n"
    "v_cvt_pk_bf16_f32 v198, v160, v161\n"
    "ds_read_b64_tr_b16 a[48:49], v236 offset:1024\n"
    "v_mfma_f32_16x16x32_bf16 v[176:179], a[8:11], v[116:119], v[176:179]\n"
    "v_cvt_pk_bf16_f32 v199, v162, v163\n"
    "s_mul_i32 s92, s92, 18432\n"
    "v_mfma_f32_16x16x32_bf16 v[180:183], a[12:15], v[108:111], v[180:183]\n"
    "v_exp_f32 v152, v152\n"
    "v_mfma_f32_16x16x32_bf16 v[184:187], a[12:15], v[112:115], v[184:187]\n"
    "ds_read_b64_tr_b16 a[50:51], v236 offset:5120\n"
    "s_lshl_b32 s93, s91, 5\n"
    "v_mfma_f32_16x16x32_bf16 v[188:191], a[12:15], v[116:119], v[188:191]\n"
    "v_exp_f32 v153, v153\n"
    "v_mfma_f32_16x16x32_bf16 v[168:171], a[16:19], v[120:123], v[168:171]\n"
    "v_exp_f32 v154, v154\n"
    "v_mfma_f32_16x16x32_bf16 v[172:175], a[16:19], v[124:127], v[172:175]\n"
    "ds_read_b64_tr_b16 a[52:53], v237 offset:1024\n"
    "s_add_u32 s92, s92, s93\n"
    "v_mfma_f32_16x16x32_bf16 v[176:179], a[16:19], v[128:131], v[176:179]\n"
    "v_exp_f32 v155, v155\n"
    "v_mfma_f32_16x16x32_bf16 v[180:183], a[20:23], v[120:123], v[180:183]\n"
    "v_cvt_pk_bf16_f32 v200, v152, v153\n"
    "ds_read_b64_tr_b16 a[54:55], v237 offset:5120\n"
    "v_mfma_f32_16x16x32_bf16 v[184:187], a[20:23], v[124:127], v[184:187]\n"
    "v_cvt_pk_bf16_f32 v201, v154, v155\n"
    "s_add_u32 s40, s92, s47\n"
    "v_mfma_f32_16x16x32_bf16 v[188:191], a[20:23], v[128:131], v[188:191]\n"
    "v_exp_f32 v164, v164\n"
    "v_mfma_f32_16x16x32_bf16 v[168:171], a[24:27], v[132:135], v[168:171]\n"
    "ds_read_b64_tr_b16 a[56:57], v236 offset:1536\n"
    "s_add_u32 s91, s91, s90\n"
    "v_mfma_f32_16x16x32_bf16 v[172:175], a[24:27], v[136:139], v[172:175]\n"
    "v_exp_f32 v165, v165\n"
    "v_mfma_f32_16x16x32_bf16 v[176:179], a[24:27], v[140:143], v[176:179]\n"
    "v_exp_f32 v166, v166\n"
    "v_mfma_f32_16x16x32_bf16 v[180:183], a[28:31], v[132:135], v[180:183]\n"
    "ds_read_b64_tr_b16 a[58:59], v236 offset:5632\n"
    "v_mfma_f32_16x16x32_bf16 v[184:187], a[28:31], v[136:139], v[184:187]\n"
    "v_exp_f32 v167, v167\n"
    "v_mfma_f32_16x16x32_bf16 v[188:191], a[28:31], v[140:143], v[188:191]\n"
    "s_cmp_ge_u32 s91, 5\n"
    "s_cselect_b32 s40, 0x4000000, s40\n"
    "ds_read_b64_tr_b16 a[60:61], v237 offset:1536\n"
    "ds_read_b64_tr_b16 a[62:63], v237 offset:5632\n"
    "v_cvt_pk_bf16_f32 v202, v164, v165\n"
    "v_cvt_pk_bf16_f32 v203, v166, v167\n"
    "s_add_u32 s89, s66, 4\n"
    "s_mul_i32 s91, s89, 21846\n"
    "s_lshr_b32 s91, s91, 16\n"
    "s_waitcnt vmcnt(12)\n"
    "s_barrier\n"
    "v_add_u32 v234, 32768, v234\n"
    "v_add_u32 v235, 32768, v235\n"
    "v_mfma_f32_16x16x32_bf16 a[64:67], a[76:79], v[192:195], a[64:67]\n"
    "ds_read_b128 a[0:3], v234 offset:0\n"
    "v_mfma_f32_16x16x32_bf16 a[68:71], a[76:79], v[196:199], a[68:71]\n"
    "ds_read_b128 a[4:7], v234 offset:4096\n"
    "v_mfma_f32_16x16x32_bf16 a[72:75], a[76:79], v[200:203], a[72:75]\n"
    "v_exp_f32 v168, v168\n"
    "s_waitcnt lgkmcnt(5)\n"
    "v_mfma_f32_16x16x32_bf16 v[0:3], a[32:35], v[192:195], v[0:3]\n"
    "ds_read_b128 a[8:11], v235 offset:0\n"
    "v_mfma_f32_16x16x32_bf16 v[4:7], a[32:35], v[196:199], v[4:7]\n"
    "ds_read_b128 a[12:15], v235 offset:4096\n"
    "s_add_u32 s88, s87, 102400\n"
    "v_mfma_f32_16x16x32_bf16 v[8:11], a[32:35], v[200:203], v[8:11]\n"
    "v_exp_f32 v169, v169\n"
    "v_mfma_f32_16x16x32_bf16 v[12:15], a[36:39], v[192:195], v[12:15]\n"
    "ds_read_b128 a[16:19], v234 offset:2048\n"
    "s_add_u32 m0, s86, 4096\n"
    "v_mfma_f32_16x16x32_bf16 v[16:19], a[36:39], v[196:199], v[16:19]\n"
    "v_exp_f32 v170, v170\n"
    "v_mfma_f32_16x16x32_bf16 v[20:23], a[36:39], v[200:203], v[20:23]\n"
    "ds_read_b128 a[20:23], v234 offset:6144\n"
    "buffer_load_dwordx4 v238, s[48:51], s88 offen offset:0 nt lds\n"
    "v_mfma_f32_16x16x32_bf16 v[24:27], a[40:43], v[192:195], v[24:27]\n"
    "ds_read_b128 a[24:27], v235 offset:2048\n"
    "v_mfma_f32_16x16x32_bf16 v[28:31], a[40:43], v[196:199], v[28:31]\n"
    "v_exp_f32 v171, v171\n"
    "v_mfma_f32_16x16x32_bf16 v[32:35], a[40:43], v[200:203], v[32:35]\n"
    "ds_read_b128 a[28:31], v235 offset:6144\n"
    "v_cvt_pk_bf16_f32 v204, v168, v169\n"
    "v_mfma_f32_16x16x32_bf16 v[36:39], a[44:47], v[192:195], v[36:39]\n"
    "v_cvt_pk_bf16_f32 v205, v170, v171\n"
    "ds_read_b64_tr_b16 a[32:33], v236 offset:8192\n"
    "v_mfma_f32_16x16x32_bf16 v[40:43], a[44:47], v[196:199], v[40:43]\n"
    "v_exp_f32 v180, v180\n"
    "v_mfma_f32_16x16x32_bf16 v[44:47], a[44:47], v[200:203], v[44:47]\n"
    "v_exp_f32 v181, v181\n"
    "v_mfma_f32_16x16x32_bf16 v[48:51], a[48:51], v[192:195], v[48:51]\n"
    "v_exp_f32 v182, v182\n"
    "v_mfma_f32_16x16x32_bf16 v[52:55], a[48:51], v[196:199], v[52:55]\n"
    "v_exp_f32 v183, v183\n"
    "v_mfma_f32_16x16x32_bf16 v[56:59], a[48:51], v[200:203], v[56:59]\n"
    "ds_read_b64_tr_b16 a[34:35], v236 offset:12288\n"
    "buffer_load_dwordx4 v239, s[48:51], s88 offen offset:1024 nt lds\n"
    "v_mfma_f32_16x16x32_bf16 v[60:63], a[52:55], v[192:195], v[60:63]\n"
    "v_cvt_pk_bf16_f32 v206, v180, v181\n"
    "v_cvt_pk_bf16_f32 v207, v182, v183\n"
    "v_mfma_f32_16x16x32_bf16 v[64:67], a[52:55], v[196:199], v[64:67]\n"
    "buffer_load_dwordx4 v240, s[48:51], s88 offen offset:2048 nt lds\n"
    "ds_read_b64_tr_b16 a[36:37], v237 offset:8192\n"
    "v_mfma_f32_16x16x32_bf16 v[68:71], a[52:55], v[200:203], v[68:71]\n"
    "v_exp_f32 v172, v172\n"
    "s_waitcnt lgkmcnt(4)\n"
    "v_mfma_f32_16x16x32_bf16 v[72:75], a[56:59], v[192:195], v[72:75]\n"
    "v_exp_f32 v173, v173\n"
    "v_mfma_f32_16x16x32_bf16 v[76:79], a[56:59], v[196:199], v[76:79]\n"
    "buffer_load_dwordx4 v241, s[48:51], s88 offen offset:3072 nt lds\n"
    "ds_read_b64_tr_b16 a[38:39], v237 offset:12288\n"
    "v_mfma_f32_16x16x32_bf16 v[80:83], a[56:59], v[200:203], v[80:83]\n"
    "v_exp_f32 v174, v174\n"
    "v_mfma_f32_16x16x32_bf16 v[84:87], a[60:63], v[192:195], v[84:87]\n"
    "v_exp_f32 v175, v175\n"
    "v_mfma_f32_16x16x32_bf16 v[88:91], a[60:63], v[196:199], v[88:91]\n"
    "s_mul_i32 s92, s91, 3\n"
    "ds_read_b64_tr_b16 a[40:41], v236 offset:8704\n"
    "v_mfma_f32_16x16x32_bf16 v[92:95], a[60:63], v[200:203], v[92:95]\n"
    "v_cvt_pk_bf16_f32 v208, v172, v173\n"
    "s_sub_u32 s92, s89, s92\n"
    "v_mfma_f32_16x16x32_bf16 v[144:147], a[0:3], v[96:99], v[216:219]\n"
    "v_cvt_pk_bf16_f32 v209, v174, v175\n"
    "ds_read_b64_tr_b16 a[42:43], v236 offset:12800\n"
    "v_mfma_f32_16x16x32_bf16 v[148:151], a[0:3], v[100:103], v[220:223]\n"
    "v_exp_f32 v184, v184\n"
    "v_mfma_f32_16x16x32_bf16 v[152:155], a[0:3], v[104:107], v[224:227]\n"
    "s_mul_i32 s92, s92, 18432\n"
    "ds_read_b64_tr_b16 a[44:45], v237 offset:8704\n"
    "v_mfma_f32_16x16x32_bf16 v[156:159], a[4:7], v[96:99], v[216:219]\n"
    "v_exp_f32 v185, v185\n"
    "v_mfma_f32_16x16x32_bf16 v[160:163], a[4:7], v[100:103], v[220:223]\n"
    "v_exp_f32 v186, v186\n"
    "v_mfma_f32_16x16x32_bf16 v[164:167], a[4:7], v[104:107], v[224:227]\n"
    "s_lshl_b32 s93, s91, 5\n"
    "ds_read_b64_tr_b16 a[46:47], v237 offset:12800\n"
    "v_mfma_f32_16x16x32_bf16 v[144:147], a[8:11], v[108:111], v[144:147]\n"
    "v_exp_f32 v187, v187\n"
    "v_mfma_f32_16x16x32_bf16 v[148:151], a[8:11], v[112:115], v[148:151]\n"
    "v_cvt_pk_bf16_f32 v210, v184, v185\n"
    "s_add_u32 s92, s92, s93\n"
    "v_mfma_f32_16x16x32_bf16 v[152:155], a[8:11], v[116:119], v[152:155]\n"
    "v_cvt_pk_bf16_f32 v211, v186, v187\n"
    "ds_read_b64_tr_b16 a[48:49], v236 offset:9216\n"
    "v_mfma_f32_16x16x32_bf16 v[156:159], a[12:15], v[108:111], v[156:159]\n"
    "s_add_u32 s41, s92, s47\n"
    "ds_read_b64_tr_b16 a[50:51], v236 offset:13312\n"
    "v_mfma_f32_16x16x32_bf16 v[160:163], a[12:15], v[112:115], v[160:163]\n"
    "v_exp_f32 v176, v176\n"
    "v_mfma_f32_16x16x32_bf16 v[164:167], a[12:15], v[116:119], v[164:167]\n"
    "v_exp_f32 v177, v177\n"
    "v_mfma_f32_16x16x32_bf16 v[144:147], a[16:19], v[120:123], v[144:147]\n"
    "s_add_u32 s91, s91, s90\n"
    "ds_read_b64_tr_b16 a[52:53], v237 offset:9216\n"
    "v_mfma_f32_16x16x32_bf16 v[148:151], a[16:19], v[124:127], v[148:151]\n"
    "v_exp_f32 v178, v178\n"
    "v_mfma_f32_16x16x32_bf16 v[152:155], a[16:19], v[128:131], v[152:155]\n"
    "v_exp_f32 v179, v179\n"
    "v_mfma_f32_16x16x32_bf16 v[156:159], a[20:23], v[120:123], v[156:159]\n"
    "s_cmp_ge_u32 s91, 5\n"
    "s_cselect_b32 s41, 0x4000000, s41\n"
    "v_mfma_f32_16x16x32_bf16 v[160:163], a[20:23], v[124:127], v[160:163]\n"
    "v_cvt_pk_bf16_f32 v212, v176, v177\n"
    "ds_read_b64_tr_b16 a[54:55], v237 offset:13312\n"
    "v_mfma_f32_16x16x32_bf16 v[164:167], a[20:23], v[128:131], v[164:167]\n"
    "v_cvt_pk_bf16_f32 v213, v178, v179\n"
    "s_add_u32 s89, s66, 8\n"
    "v_mfma_f32_16x16x32_bf16 v[144:147], a[24:27], v[132:135], v[144:147]\n"
    "v_exp_f32 v188, v188\n"
    "v_mfma_f32_16x16x32_bf16 v[148:151], a[24:27], v[136:139], v[148:151]\n"
    "ds_read_b64_tr_b16 a[56:57], v236 offset:9728\n"
    "s_mul_i32 s91, s89, 21846\n"
    "v_mfma_f32_16x16x32_bf16 v[152:155], a[24:27], v[140:143], v[152:155]\n"
    "v_exp_f32 v189, v189\n"
    "s_waitcnt lgkmcnt(3)\n"
    "v_mfma_f32_16x16x32_bf16 v[156:159], a[28:31], v[132:135], v[156:159]\n"
    "v_exp_f32 v190, v190\n"
    "v_mfma_f32_16x16x32_bf16 v[160:163], a[28:31], v[136:139], v[160:163]\n"
    "ds_read_b64_tr_b16 a[58:59], v236 offset:13824\n"
    "s_lshr_b32 s91, s91, 16\n"
    "v_mfma_f32_16x16x32_bf16 v[164:167], a[28:31], v[140:143], v[164:167]\n"
    "v_exp_f32 v191, v191\n"
    "ds_read_b64_tr_b16 a[60:61], v237 offset:9728\n"
    "ds_read_b64_tr_b16 a[62:63], v237 offset:13824\n"
    "v_cvt_pk_bf16_f32 v214, v188, v189\n"
    "v_cvt_pk_bf16_f32 v215, v190, v191\n"
    "s_mul_i32 s92, s91, 3\n"
    "s_sub_u32 s92, s89, s92\n"
    "v_lshlrev_b32 v232, 3, v254\n"
    "v_add_u32 v232, 6144, v232\n"
    "global_load_dwordx2 v[244:245], v232, s[64:65] offset:0 sc1\n"
    "global_load_dwordx2 v[246:247], v232, s[64:65] offset:512 sc1\n"
    "global_load_dwordx2 v[248:249], v232, s[64:65] offset:1024 sc1\n"
    "global_load_dwordx2 v[250:251], v232, s[64:65] offset:1536 sc1\n"
    "v_add_u32 v236, 32768, v236\n"
    "v_add_u32 v237, 32768, v237\n"
    "v_mfma_f32_16x16x32_bf16 a[64:67], a[76:79], v[204:207], a[64:67]\n"
    "ds_read_b128 a[0:3], v234 offset:8192\n"
    "v_mfma_f32_16x16x32_bf16 a[68:71], a[76:79], v[208:211], a[68:71]\n"
    "ds_read_b128 a[4:7], v234 offset:12288\n"
    "v_mfma_f32_16x16x32_bf16 a[72:75], a[76:79], v[212:215], a[72:75]\n"
    "v_exp_f32 v144, v144\n"
    "v_mfma_f32_16x16x32_bf16 v[0:3], a[32:35], v[204:207], v[0:3]\n"
    "ds_read_b128 a[8:11], v235 offset:8192\n"
    "v_mfma_f32_16x16x32_bf16 v[4:7], a[32:35], v[208:211], v[4:7]\n"
    "ds_read_b128 a[12:15], v235 offset:12288\n"
    "s_add_u32 s88, s87, 114688\n"
    "v_mfma_f32_16x16x32_bf16 v[8:11], a[32:35], v[212:215], v[8:11]\n"
    "v_exp_f32 v145, v145\n"
    "v_mfma_f32_16x16x32_bf16 v[12:15], a[36:39], v[204:207], v[12:15]\n"
    "ds_read_b128 a[16:19], v234 offset:10240\n"
    "s_add_u32 m0, s86, 32768\n"
    "v_mfma_f32_16x16x32_bf16 v[16:19], a[36:39], v[208:211], v[16:19]\n"
    "v_exp_f32 v146, v146\n"
    "v_mfma_f32_16x16x32_bf16 v[20:23], a[36:39], v[212:215], v[20:23]\n"
    "ds_read_b128 a[20:23], v234 offset:14336\n"
    "buffer_load_dwordx4 v238, s[48:51], s88 offen offset:0 nt lds\n"
    "v_mfma_f32_16x16x32_bf16 v[24:27], a[40:43], v[204:207], v[24:27]\n"
    "ds_read_b128 a[24:27], v235 offset:10240\n"
    "v_mfma_f32_16x16x32_bf16 v[28:31], a[40:43], v[208:211], v[28:31]\n"
    "v_exp_f32 v147, v147\n"
    "v_mfma_f32_16x16x32_bf16 v[32:35], a[40:43], v[212:215], v[32:35]\n"
    "ds_read_b128 a[28:31], v235 offset:14336\n"
    "v_cvt_pk_bf16_f32 v192, v144, v145\n"
    "v_mfma_f32_16x16x32_bf16 v[36:39], a[44:47], v[204:207], v[36:39]\n"
    "v_cvt_pk_bf16_f32 v193, v146, v147\n"
    "ds_read_b64_tr_b16 a[32:33], v236 offset:0\n"
    "v_mfma_f32_16x16x32_bf16 v[40:43], a[44:47], v[208:211], v[40:43]\n"
    "v_exp_f32 v156, v156\n"
    "v_mfma_f32_16x16x32_bf16 v[44:47], a[44:47], v[212:215], v[44:47]\n"
    "v_exp_f32 v157, v157\n"
    "v_mfma_f32_16x16x32_bf16 v[48:51], a[48:51], v[204:207], v[48:51]\n"
    "v_exp_f32 v158, v158\n"
    "v_mfma_f32_16x16x32_bf16 v[52:55], a[48:51], v[208:211], v[52:55]\n"
    "v_exp_f32 v159, v159\n"
    "v_mfma_f32_16x16x32_bf16 v[56:59], a[48:51], v[212:215], v[56:59]\n"
    "ds_read_b64_tr_b16 a[34:35], v236 offset:4096\n"
    "v_cvt_pk_bf16_f32 v194, v156, v157\n"
    "s_waitcnt lgkmcnt(5)\n"
    "v_mfma_f32_16x16x32_bf16 v[60:63], a[52:55], v[204:207], v[60:63]\n"
    "buffer_load_dwordx4 v239, s[48:51], s88 offen offset:1024 nt lds\n"
    "v_cvt_pk_bf16_f32 v195, v158, v159\n"
    "v_mfma_f32_16x16x32_bf16 v[64:67], a[52:55], v[208:211], v[64:67]\n"
    "ds_read_b64_tr_b16 a[36:37], v237 offset:0\n"
    "buffer_load_dwordx4 v240, s[48:51], s88 offen offset:2048 nt lds\n"
    "v_mfma_f32_16x16x32_bf16 v[68:71], a[52:55], v[212:215], v[68:71]\n"
    "v_exp_f32 v148, v148\n"
    "v_mfma_f32_16x16x32_bf16 v[72:75], a[56:59], v[204:207], v[72:75]\n"
    "v_exp_f32 v149, v149\n"
    "v_mfma_f32_16x16x32_bf16 v[76:79], a[56:59], v[208:211], v[76:79]\n"
    "ds_read_b64_tr_b16 a[38:39], v237 offset:4096\n"
    "buffer_load_dwordx4 v241, s[48:51], s88 offen offset:3072 nt lds\n"
    "v_mfma_f32_16x16x32_bf16 v[80:83], a[56:59], v[212:215], v[80:83]\n"
    "v_exp_f32 v150, v150\n"
    "v_mfma_f32_16x16x32_bf16 v[84:87], a[60:63], v[204:207], v[84:87]\n"
    "v_exp_f32 v151, v151\n"
    "v_mfma_f32_16x16x32_bf16 v[88:91], a[60:63], v[208:211], v[88:91]\n"
    "ds_read_b64_tr_b16 a[40:41], v236 offset:512\n"
    "s_mul_i32 s92, s92, 18432\n"
    "v_mfma_f32_16x16x32_bf16 v[92:95], a[60:63], v[212:215], v[92:95]\n"
    "v_cvt_pk_bf16_f32 v196, v148, v149\n"
    "ds_read_b64_tr_b16 a[42:43], v236 offset:4608\n"
    "v_mfma_f32_16x16x32_bf16 v[168:171], a[0:3], v[96:99], v[216:219]\n"
    "v_cvt_pk_bf16_f32 v197, v150, v151\n"
    "s_lshl_b32 s93, s91, 5\n"
    "v_mfma_f32_16x16x32_bf16 v[172:175], a[0:3], v[100:103], v[220:223]\n"
    "v_exp_f32 v160, v160\n"
    "v_mfma_f32_16x16x32_bf16 v[176:179], a[0:3], v[104:107], v[224:227]\n"
    "ds_read_b64_tr_b16 a[44:45], v237 offset:512\n"
    "s_add_u32 s92, s92, s93\n"
    "v_mfma_f32_16x16x32_bf16 v[180:183], a[4:7], v[96:99], v[216:219]\n"
    "v_exp_f32 v161, v161\n"
    "v_mfma_f32_16x16x32_bf16 v[184:187], a[4:7], v[100:103], v[220:223]\n"
    "v_exp_f32 v162, v162\n"
    "v_mfma_f32_16x16x32_bf16 v[188:191], a[4:7], v[104:107], v[224:227]\n"
    "ds_read_b64_tr_b16 a[46:47], v237 offset:4608\n"
    "s_add_u32 s42, s92, s47\n"
    "v_mfma_f32_16x16x32_bf16 v[168:171], a[8:11], v[108:111], v[168:171]\n"
    "v_exp_f32 v163, v163\n"
    "v_mfma_f32_16x16x32_bf16 v[172:175], a[8:11], v[112:115], v[172:175]\n"
    "v_cvt_pk_bf16_f32 v198, v160, v161\n"
    "ds_read_b64_tr_b16 a[48:49], v236 offset:1024\n"
    "v_mfma_f32_16x16x32_bf16 v[176:179], a[8:11], v[116:119], v[176:179]\n"
    "v_cvt_pk_bf16_f32 v199, v162, v163\n"
    "s_add_u32 s91, s91, s90\n"
    "v_mfma_f32_16x16x32_bf16 v[180:183], a[12:15], v[108:111], v[180:183]\n"
    "v_exp_f32 v152, v152\n"
    "v_mfma_f32_16x16x32_bf16 v[184:187], a[12:15], v[112:115], v[184:187]\n"
    "ds_read_b64_tr_b16 a[50:51], v236 offset:5120\n"
    "v_mfma_f32_16x16x32_bf16 v[188:191], a[12:15], v[116:119], v[188:191]\n"
    "v_exp_f32 v153, v153\n"
    "v_mfma_f32_16x16x32_bf16 v[168:171], a[16:19], v[120:123], v[168:171]\n"
    "s_cmp_ge_u32 s91, 5\n"
    "s_cselect_b32 s42, 0x4000000, s42\n"
    "v_mfma_f32_16x16x32_bf16 v[172:175], a[16:19], v[124:127], v[172:175]\n"
    "v_exp_f32 v154, v154\n"
    "v_mfma_f32_16x16x32_bf16 v[176:179], a[16:19], v[128:131], v[176:179]\n"
    "v_exp_f32 v155, v155\n"
    "s_waitcnt lgkmcnt(3)\n"
    "v_mfma_f32_16x16x32_bf16 v[180:183], a[20:23], v[120:123], v[180:183]\n"
    "ds_read_b64_tr_b16 a[52:53], v237 offset:1024\n"
    "s_add_u32 s89, s66, 12\n"
    "v_mfma_f32_16x16x32_bf16 v[184:187], a[20:23], v[124:127], v[184:187]\n"
    "v_cvt_pk_bf16_f32 v200, v152, v153\n"
    "ds_read_b64_tr_b16 a[54:55], v237 offset:5120\n"
    "v_mfma_f32_16x16x32_bf16 v[188:191], a[20:23], v[128:131], v[188:191]\n"
    "v_cvt_pk_bf16_f32 v201, v154, v155\n"
    "s_mul_i32 s91, s89, 21846\n"
    "v_mfma_f32_16x16x32_bf16 v[168:171], a[24:27], v[132:135], v[168:171]\n"
    "v_exp_f32 v164, v164\n"
    "v_mfma_f32_16x16x32_bf16 v[172:175], a[24:27], v[136:139], v[172:175]\n"
    "ds_read_b64_tr_b16 a[56:57], v236 offset:1536\n"
    "s_lshr_b32 s91, s91, 16\n"
    "v_mfma_f32_16x16x32_bf16 v[176:179], a[24:27], v[140:143], v[176:179]\n"
    "v_exp_f32 v165, v165\n"
    "v_mfma_f32_16x16x32_bf16 v[180:183], a[28:31], v[132:135], v[180:183]\n"
    "v_exp_f32 v166, v166\n"
    "v_mfma_f32_16x16x32_bf16 v[184:187], a[28:31], v[136:139], v[184:187]\n"
    "ds_read_b64_tr_b16 a[58:59], v236 offset:5632\n"
    "s_mul_i32 s92, s91, 3\n"
    "v_mfma_f32_16x16x32_bf16 v[188:191], a[28:31], v[140:143], v[188:191]\n"
    "v_exp_f32 v167, v167\n"
    "ds_read_b64_tr_b16 a[60:61], v237 offset:1536\n"
    "ds_read_b64_tr_b16 a[62:63], v237 offset:5632\n"
    "v_cvt_pk_bf16_f32 v202, v164, v165\n"
    "v_cvt_pk_bf16_f32 v203, v166, v167\n"
    "s_sub_u32 s92, s89, s92\n"
    "s_mul_i32 s92, s92, 18432\n"
    "s_lshl_b32 s93, s91, 5\n"
    "s_waitcnt vmcnt(16)\n"
    "s_barrier\n"
    "s_waitcnt vmcnt(4)\n"
    "v_cmp_eq_u64 vcc, v[244:245], s[94:95]\n"
    "s_nop 3\n"
    "s_mov_b64 s[38:39], vcc\n"
    "v_cmp_eq_u64 vcc, v[246:247], s[94:95]\n"
    "s_nop 3\n"
    "s_and_b64 s[38:39], s[38:39], vcc\n"
    "v_cmp_eq_u64 vcc, v[248:249], s[94:95]\n"
    "s_nop 3\n"
    "s_and_b64 s[38:39], s[38:39], vcc\n"
    "v_cmp_eq_u64 vcc, v[250:251], s[94:95]\n"
    "s_nop 3\n"
    "s_and_b64 s[38:39], s[38:39], vcc\n"
    "s_cmp_eq_u64 s[38:39], -1\n"
    "s_cbranch_scc0 L_SPIN2\n"
    "L_SPIN2_OK:\n"
    "v_add_u32 v234, 32768, v234\n"
    "v_add_u32 v235, 32768, v235\n"
    "v_mfma_f32_16x16x32_bf16 a[64:67], a[76:79], v[192:195], a[64:67]\n"
    "ds_read_b128 a[0:3], v234 offset:0\n"
    "v_mfma_f32_16x16x32_bf16 a[68:71], a[76:79], v[196:199], a[68:71]\n"
    "ds_read_b128 a[4:7], v234 offset:4096\n"
    "v_mfma_f32_16x16x32_bf16 a[72:75], a[76:79], v[200:203], a[72:75]\n"
    "v_exp_f32 v168, v168\n"
    "v_mfma_f32_16x16x32_bf16 v[0:3], a[32:35], v[192:195], v[0:3]\n"
    "ds_read_b128 a[8:11], v235 offset:0\n"
    "v_mfma_f32_16x16x32_bf16 v[4:7], a[32:35], v[196:199], v[4:7]\n"
    "ds_read_b128 a[12:15], v235 offset:4096\n"
    "s_add_u32 s88, s87, 118784\n"
    "v_mfma_f32_16x16x32_bf16 v[8:11], a[32:35], v[200:203], v[8:11]\n"
    "v_exp_f32 v169, v169\n"
    "v_mfma_f32_16x16x32_bf16 v[12:15], a[36:39], v[192:195], v[12:15]\n"
    "ds_read_b128 a[16:19], v234 offset:2048\n"
    "s_add_u32 m0, s86, 36864\n"
    "v_mfma_f32_16x16x32_bf16 v[16:19], a[36:39], v[196:199], v[16:19]\n"
    "v_exp_f32 v170, v170\n"
    "v_mfma_f32_16x16x32_bf16 v[20:23], a[36:39], v[200:203], v[20:23]\n"
    "ds_read_b128 a[20:23], v234 offset:6144\n"
    "buffer_load_dwordx4 v238, s[48:51], s88 offen offset:0 nt lds\n"
    "v_mfma_f32_16x16x32_bf16 v[24:27], a[40:43], v[192:195], v[24:27]\n"
    "ds_read_b128 a[24:27], v235 offset:2048\n"
    "v_mfma_f32_16x16x32_bf16 v[28:31], a[40:43], v[196:199], v[28:31]\n"
    "v_exp_f32 v171, v171\n"
    "v_mfma_f32_16x16x32_bf16 v[32:35], a[40:43], v[200:203], v[32:35]\n"
    "ds_read_b128 a[28:31], v235 offset:6144\n"
    "v_cvt_pk_bf16_f32 v204, v168, v169\n"
    "s_waitcnt lgkmcnt(6)\n"
    "v_mfma_f32_16x16x32_bf16 v[36:39], a[44:47], v[192:195], v[36:39]\n"
    "v_cvt_pk_bf16_f32 v205, v170, v171\n"
    "ds_read_b64_tr_b16 a[32:33], v236 offset:8192\n"
    "v_mfma_f32_16x16x32_bf16 v[40:43], a[44:47], v[196:199], v[40:43]\n"
    "v_exp_f32 v180, v180\n"
    "v_mfma_f32_16x16x32_bf16 v[44:47], a[44:47], v[200:203], v[44:47]\n"
    "v_exp_f32 v181, v181\n"
    "v_mfma_f32_16x16x32_bf16 v[48:51], a[48:51], v[192:195], v[48:51]\n"
    "v_exp_f32 v182, v182\n"
    "v_mfma_f32_16x16x32_bf16 v[52:55], a[48:51], v[196:199], v[52:55]\n"
    "v_exp_f32 v183, v183\n"
    "v_mfma_f32_16x16x32_bf16 v[56:59], a[48:51], v[200:203], v[56:59]\n"
    "ds_read_b64_tr_b16 a[34:35], v236 offset:12288\n"
    "v_cvt_pk_bf16_f32 v206, v180, v181\n"
    "v_mfma_f32_16x16x32_bf16 v[60:63], a[52:55], v[192:195], v[60:63]\n"
    "buffer_load_dwordx4 v239, s[48:51], s88 offen offset:1024 nt lds\n"
    "v_cvt_pk_bf16_f32 v207, v182, v183\n"
    "v_mfma_f32_16x16x32_bf16 v[64:67], a[52:55], v[196:199], v[64:67]\n"
    "ds_read_b64_tr_b16 a[36:37], v237 offset:8192\n"
    "buffer_load_dwordx4 v240, s[48:51], s88 offen offset:2048 nt lds\n"
    "v_mfma_f32_16x16x32_bf16 v[68:71], a[52:55], v[200:203], v[68:71]\n"
    "v_exp_f32 v172, v172\n"
    "v_mfma_f32_16x16x32_bf16 v[72:75], a[56:59], v[192:195], v[72:75]\n"
    "v_exp_f32 v173, v173\n"
    "v_mfma_f32_16x16x32_bf16 v[76:79], a[56:59], v[196:199], v[76:79]\n"
    "ds_read_b64_tr_b16 a[38:39], v237 offset:12288\n"
    "buffer_load_dwordx4 v241, s[48:51], s88 offen offset:3072 nt lds\n"
    "v_mfma_f32_16x16x32_bf16 v[80:83], a[56:59], v[200:203], v[80:83]\n"
    "v_exp_f32 v174, v174\n"
    "v_mfma_f32_16x16x32_bf16 v[84:87], a[60:63], v[192:195], v[84:87]\n"
    "v_exp_f32 v175, v175\n"
    "v_mfma_f32_16x16x32_bf16 v[88:91], a[60:63], v[196:199], v[88:91]\n"
    "ds_read_b64_tr_b16 a[40:41], v236 offset:8704\n"
    "s_add_u32 s92, s92, s93\n"
    "v_mfma_f32_16x16x32_bf16 v[92:95], a[60:63], v[200:203], v[92:95]\n"
    "v_cvt_pk_bf16_f32 v208, v172, v173\n"
    "ds_read_b64_tr_b16 a[42:43], v236 offset:12800\n"
    "v_mfma_f32_16x16x32_bf16 v[144:147], a[0:3], v[96:99], v[216:219]\n"
    "v_cvt_pk_bf16_f32 v209, v174, v175\n"
    "s_add_u32 s43, s92, s47\n"
    "v_mfma_f32_16x16x32_bf16 v[148:151], a[0:3], v[100:103], v[220:223]\n"
    "v_exp_f32 v184, v184\n"
    "v_mfma_f32_16x16x32_bf16 v[152:155], a[0:3], v[104:107], v[224:227]\n"
    "ds_read_b64_tr_b16 a[44:45], v237 offset:8704\n"
    "s_add_u32 s91, s91, s90\n"
    "v_mfma_f32_16x16x32_bf16 v[156:159], a[4:7], v[96:99], v[216:219]\n"
    "v_exp_f32 v185, v185\n"
    "v_mfma_f32_16x16x32_bf16 v[160:163], a[4:7], v[100:103], v[220:223]\n"
    "v_exp_f32 v186, v186\n"
    "v_mfma_f32_16x16x32_bf16 v[164:167], a[4:7], v[104:107], v[224:227]\n"
    "ds_read_b64_tr_b16 a[46:47], v237 offset:12800\n"
    "s_waitcnt lgkmcnt(4)\n"
    "v_mfma_f32_16x16x32_bf16 v[144:147], a[8:11], v[108:111], v[144:147]\n"
    "v_exp_f32 v187, v187\n"
    "v_mfma_f32_16x16x32_bf16 v[148:151], a[8:11], v[112:115], v[148:151]\n"
    "s_cmp_ge_u32 s91, 5\n"
    "s_cselect_b32 s43, 0x4000000, s43\n"
    "v_mfma_f32_16x16x32_bf16 v[152:155], a[8:11], v[116:119], v[152:155]\n"
    "v_cvt_pk_bf16_f32 v210, v184, v185\n"
    "v_cvt_pk_bf16_f32 v211, v186, v187\n"
    "v_mfma_f32_16x16x32_bf16 v[156:159], a[12:15], v[108:111], v[156:159]\n"
    "ds_read_b64_tr_b16 a[48:49], v236 offset:9216\n"
    "s_lshl_b32 s47, s84, 5\n"
    "v_mfma_f32_16x16x32_bf16 v[160:163], a[12:15], v[112:115], v[160:163]\n"
    "v_exp_f32 v176, v176\n"
    "v_mfma_f32_16x16x32_bf16 v[164:167], a[12:15], v[116:119], v[164:167]\n"
    "v_exp_f32 v177, v177\n"
    "v_mfma_f32_16x16x32_bf16 v[144:147], a[16:19], v[120:123], v[144:147]\n"
    "ds_read_b64_tr_b16 a[50:51], v236 offset:13312\n"
    "s_lshl_b32 s89, s75, 4\n"
    "v_mfma_f32_16x16x32_bf16 v[148:151], a[16:19], v[124:127], v[148:151]\n"
    "v_exp_f32 v178, v178\n"
    "v_mfma_f32_16x16x32_bf16 v[152:155], a[16:19], v[128:131], v[152:155]\n"
    "v_exp_f32 v179, v179\n"
    "v_mfma_f32_16x16x32_bf16 v[156:159], a[20:23], v[120:123], v[156:159]\n"
    "ds_read_b64_tr_b16 a[52:53], v237 offset:9216\n"
    "s_add_u32 s47, s47, s89\n"
    "v_mfma_f32_16x16x32_bf16 v[160:163], a[20:23], v[124:127], v[160:163]\n"
    "v_cvt_pk_bf16_f32 v212, v176, v177\n"
    "ds_read_b64_tr_b16 a[54:55], v237 offset:13312\n"
    "v_mfma_f32_16x16x32_bf16 v[164:167], a[20:23], v[128:131], v[164:167]\n"
    "v_cvt_pk_bf16_f32 v213, v178, v179\n"
    "s_lshl_b32 s89, s66, 2\n"
    "v_mfma_f32_16x16x32_bf16 v[144:147], a[24:27], v[132:135], v[144:147]\n"
    "v_exp_f32 v188, v188\n"
    "v_mfma_f32_16x16x32_bf16 v[148:151], a[24:27], v[136:139], v[148:151]\n"
    "ds_read_b64_tr_b16 a[56:57], v236 offset:9728\n"
    "s_add_u32 s47, s47, s89\n"
    "v_mfma_f32_16x16x32_bf16 v[152:155], a[24:27], v[140:143], v[152:155]\n"
    "v_exp_f32 v189, v189\n"
    "v_mfma_f32_16x16x32_bf16 v[156:159], a[28:31], v[132:135], v[156:159]\n"
    "v_exp_f32 v190, v190\n"
    "v_mfma_f32_16x16x32_bf16 v[160:163], a[28:31], v[136:139], v[160:163]\n"
    "ds_read_b64_tr_b16 a[58:59], v236 offset:13824\n"
    "s_lshl_b32 s47, s47, 3\n"
    "v_mfma_f32_16x16x32_bf16 v[164:167], a[28:31], v[140:143], v[164:167]\n"
    "v_exp_f32 v191, v191\n"
    "ds_read_b64_tr_b16 a[60:61], v237 offset:9728\n"
    "ds_read_b64_tr_b16 a[62:63], v237 offset:13824\n"
    "v_cvt_pk_bf16_f32 v214, v188, v189\n"
    "v_cvt_pk_bf16_f32 v215, v190, v191\n"
    "s_add_u32 s44, s47, 4096\n"
    "s_lshl_b32 s47, s67, 5\n"
    "s_add_u32 s47, s47, s68\n"
    "v_add_u32 v236, 32768, v236\n"
    "v_add_u32 v237, 32768, v237\n"
    "v_mfma_f32_16x16x32_bf16 a[64:67], a[76:79], v[204:207], a[64:67]\n"
    "ds_read_b128 a[0:3], v234 offset:8192\n"
    "v_mfma_f32_16x16x32_bf16 a[68:71], a[76:79], v[208:211], a[68:71]\n"
    "ds_read_b128 a[4:7], v234 offset:12288\n"
    "v_mfma_f32_16x16x32_bf16 a[72:75], a[76:79], v[212:215], a[72:75]\n"
    "v_exp_f32 v144, v144\n"
    "v_mfma_f32_16x16x32_bf16 v[0:3], a[32:35], v[204:207], v[0:3]\n"
    "ds_read_b128 a[8:11], v235 offset:8192\n"
    "v_mfma_f32_16x16x32_bf16 v[4:7], a[32:35], v[208:211], v[4:7]\n"
    "ds_read_b128 a[12:15], v235 offset:12288\n"
    "s_lshl_b32 s47, s47, 3\n"
    "v_mfma_f32_16x16x32_bf16 v[8:11], a[32:35], v[212:215], v[8:11]\n"
    "v_exp_f32 v145, v145\n"
    "v_mfma_f32_16x16x32_bf16 v[12:15], a[36:39], v[204:207], v[12:15]\n"
    "ds_read_b128 a[16:19], v234 offset:10240\n"
    "v_mfma_f32_16x16x32_bf16 v[16:19], a[36:39], v[208:211], v[16:19]\n"
    "v_exp_f32 v146, v146\n"
    "v_mfma_f32_16x16x32_bf16 v[20:23], a[36:39], v[212:215], v[20:23]\n"
    "ds_read_b128 a[20:23], v234 offset:14336\n"
    "s_add_u32 s45, s47, 4096\n"
    "s_waitcnt lgkmcnt(6)\n"
    "v_mfma_f32_16x16x32_bf16 v[24:27], a[40:43], v[204:207], v[24:27]\n"
    "ds_read_b128 a[24:27], v235 offset:10240\n"
    "v_mfma_f32_16x16x32_bf16 v[28:31], a[40:43], v[208:211], v[28:31]\n"
    "v_exp_f32 v147, v147\n"
    "v_mfma_f32_16x16x32_bf16 v[32:35], a[40:43], v[212:215], v[32:35]\n"
    "ds_read_b128 a[28:31], v235 offset:14336\n"
    "v_cvt_pk_bf16_f32 v192, v144, v145\n"
    "v_mfma_f32_16x16x32_bf16 v[36:39], a[44:47], v[204:207], v[36:39]\n"
    "v_cvt_pk_bf16_f32 v193, v146, v147\n"
    "ds_read_b64_tr_b16 a[32:33], v236 offset:0\n"
    "v_mfma_f32_16x16x32_bf16 v[40:43], a[44:47], v[208:211], v[40:43]\n"
    "v_exp_f32 v156, v156\n"
    "v_mfma_f32_16x16x32_bf16 v[44:47], a[44:47], v[212:215], v[44:47]\n"
    "v_exp_f32 v157, v157\n"
    "v_mfma_f32_16x16x32_bf16 v[48:51], a[48:51], v[204:207], v[48:51]\n"
    "v_exp_f32 v158, v158\n"
    "v_mfma_f32_16x16x32_bf16 v[52:55], a[48:51], v[208:211], v[52:55]\n"
    "v_exp_f32 v159, v159\n"
    "v_mfma_f32_16x16x32_bf16 v[56:59], a[48:51], v[212:215], v[56:59]\n"
    "ds_read_b64_tr_b16 a[34:35], v236 offset:4096\n"
    "v_cvt_pk_bf16_f32 v194, v156, v157\n"
    "v_mfma_f32_16x16x32_bf16 v[60:63], a[52:55], v[204:207], v[60:63]\n"
    "v_cvt_pk_bf16_f32 v195, v158, v159\n"
    "ds_read_b64_tr_b16 a[36:37], v237 offset:0\n"
    "v_mfma_f32_16x16x32_bf16 v[64:67], a[52:55], v[208:211], v[64:67]\n"
    "s_mov_b64 s[56:57], s[20:21]\n"
    "ds_read_b64_tr_b16 a[38:39], v237 offset:4096\n"
    "v_mfma_f32_16x16x32_bf16 v[68:71], a[52:55], v[212:215], v[68:71]\n"
    "v_exp_f32 v148, v148\n"
    "v_mfma_f32_16x16x32_bf16 v[72:75], a[56:59], v[204:207], v[72:75]\n"
    "v_exp_f32 v149, v149\n"
    "v_mfma_f32_16x16x32_bf16 v[76:79], a[56:59], v[208:211], v[76:79]\n"
    "v_exp_f32 v150, v150\n"
    "v_mfma_f32_16x16x32_bf16 v[80:83], a[56:59], v[212:215], v[80:83]\n"
    "s_add_u32 s56, s56, 1048576\n"
    "s_addc_u32 s57, s57, 0\n"
    "v_mfma_f32_16x16x32_bf16 v[84:87], a[60:63], v[204:207], v[84:87]\n"
    "v_exp_f32 v151, v151\n"
    "v_mfma_f32_16x16x32_bf16 v[88:91], a[60:63], v[208:211], v[88:91]\n"
    "ds_read_b64_tr_b16 a[40:41], v236 offset:512\n"
    "v_cvt_pk_bf16_f32 v196, v148, v149\n"
    "v_mfma_f32_16x16x32_bf16 v[92:95], a[60:63], v[212:215], v[92:95]\n"
    "ds_read_b64_tr_b16 a[42:43], v236 offset:4608\n"
    "v_cvt_pk_bf16_f32 v197, v150, v151\n"
    "s_waitcnt lgkmcnt(4)\n"
    "v_mfma_f32_16x16x32_bf16 v[168:171], a[0:3], v[96:99], v[216:219]\n"
    "s_and_b32 s57, s57, 65535\n"
    "ds_read_b64_tr_b16 a[44:45], v237 offset:512\n"
    "v_mfma_f32_16x16x32_bf16 v[172:175], a[0:3], v[100:103], v[220:223]\n"
    "v_exp_f32 v160, v160\n"
    "v_mfma_f32_16x16x32_bf16 v[176:179], a[0:3], v[104:107], v[224:227]\n"
    "v_exp_f32 v161, v161\n"
    "v_mfma_f32_16x16x32_bf16 v[180:183], a[4:7], v[96:99], v[216:219]\n"
    "v_exp_f32 v162, v162\n"
    "v_mfma_f32_16x16x32_bf16 v[184:187], a[4:7], v[100:103], v[220:223]\n"
    "s_mov_b32 s58, 12582912\n"
    "ds_read_b64_tr_b16 a[46:47], v237 offset:4608\n"
    "v_mfma_f32_16x16x32_bf16 v[188:191], a[4:7], v[104:107], v[224:227]\n"
    "v_exp_f32 v163, v163\n"
    "v_mfma_f32_16x16x32_bf16 v[168:171], a[8:11], v[108:111], v[168:171]\n"
    "v_cvt_pk_bf16_f32 v198, v160, v161\n"
    "ds_read_b64_tr_b16 a[48:49], v236 offset:1024\n"
    "v_mfma_f32_16x16x32_bf16 v[172:175], a[8:11], v[112:115], v[172:175]\n"
    "s_mov_b32 s59, 131072\n"
    "v_cvt_pk_bf16_f32 v199, v162, v163\n"
    "v_mfma_f32_16x16x32_bf16 v[176:179], a[8:11], v[116:119], v[176:179]\n"
    "v_exp_f32 v152, v152\n"
    "v_mfma_f32_16x16x32_bf16 v[180:183], a[12:15], v[108:111], v[180:183]\n"
    "ds_read_b64_tr_b16 a[50:51], v236 offset:5120\n"
    "s_lshl_b32 s47, s67, 2\n"
    "v_mfma_f32_16x16x32_bf16 v[184:187], a[12:15], v[112:115], v[184:187]\n"
    "v_exp_f32 v153, v153\n"
    "v_mfma_f32_16x16x32_bf16 v[188:191], a[12:15], v[116:119], v[188:191]\n"
    "v_exp_f32 v154, v154\n"
    "v_mfma_f32_16x16x32_bf16 v[168:171], a[16:19], v[120:123], v[168:171]\n"
    "ds_read_b64_tr_b16 a[52:53], v237 offset:1024\n"
    "s_add_u32 s47, s47, s66\n"
    "v_mfma_f32_16x16x32_bf16 v[172:175], a[16:19], v[124:127], v[172:175]\n"
    "v_exp_f32 v155, v155\n"
    "v_mfma_f32_16x16x32_bf16 v[176:179], a[16:19], v[128:131], v[176:179]\n"
    "v_cvt_pk_bf16_f32 v200, v152, v153\n"
    "ds_read_b64_tr_b16 a[54:55], v237 offset:5120\n"
    "v_mfma_f32_16x16x32_bf16 v[180:183], a[20:23], v[120:123], v[180:183]\n"
    "v_cvt_pk_bf16_f32 v201, v154, v155\n"
    "ds_read_b64_tr_b16 a[56:57], v236 offset:1536\n"
    "v_mfma_f32_16x16x32_bf16 v[184:187], a[20:23], v[124:127], v[184:187]\n"
    "v_exp_f32 v164, v164\n"
    "v_mfma_f32_16x16x32_bf16 v[188:191], a[20:23], v[128:131], v[188:191]\n"
    "s_lshl_b32 s47, s47, 7\n"
    "ds_read_b64_tr_b16 a[58:59], v236 offset:5632\n"
    "v_mfma_f32_16x16x32_bf16 v[168:171], a[24:27], v[132:135], v[168:171]\n"
    "v_exp_f32 v165, v165\n"
    "v_mfma_f32_16x16x32_bf16 v[172:175], a[24:27], v[136:139], v[172:175]\n"
    "v_exp_f32 v166, v166\n"
    "v_mfma_f32_16x16x32_bf16 v[176:179], a[24:27], v[140:143], v[176:179]\n"
    "v_exp_f32 v167, v167\n"
    "v_mfma_f32_16x16x32_bf16 v[180:183], a[28:31], v[132:135], v[180:183]\n"
    "s_lshl_b32 s89, s68, 2\n"
    "ds_read_b64_tr_b16 a[60:61], v237 offset:1536\n"
    "v_mfma_f32_16x16x32_bf16 v[184:187], a[28:31], v[136:139], v[184:187]\n"
    "v_cvt_pk_bf16_f32 v202, v164, v165\n"
    "ds_read_b64_tr_b16 a[62:63], v237 offset:5632\n"
    "v_mfma_f32_16x16x32_bf16 v[188:191], a[28:31], v[140:143], v[188:191]\n"
    "v_cvt_pk_bf16_f32 v203, v166, v167\n"
    "s_add_u32 s47, s47, s89\n"
    "s_mul_i32 s98, s47, 3072\n"
    "s_barrier\n"
    "v_subrev_u32 v234, 131072, v234\n"
    "v_subrev_u32 v235, 131072, v235\n"
    "v_mfma_f32_16x16x32_bf16 a[64:67], a[76:79], v[192:195], a[64:67]\n"
    "ds_read_b128 a[0:3], v234 offset:0\n"
    "v_mfma_f32_16x16x32_bf16 a[68:71], a[76:79], v[196:199], a[68:71]\n"
    "ds_read_b128 a[4:7], v234 offset:4096\n"
    "v_mfma_f32_16x16x32_bf16 a[72:75], a[76:79], v[200:203], a[72:75]\n"
    "v_exp_f32 v168, v168\n"
    "v_mfma_f32_16x16x32_bf16 v[0:3], a[32:35], v[192:195], v[0:3]\n"
    "ds_read_b128 a[8:11], v235 offset:0\n"
    "v_mfma_f32_16x16x32_bf16 v[4:7], a[32:35], v[196:199], v[4:7]\n"
    "ds_read_b128 a[12:15], v235 offset:4096\n"
    "v_mfma_f32_16x16x32_bf16 v[8:11], a[32:35], v[200:203], v[8:11]\n"
    "v_exp_f32 v169, v169\n"
    "s_waitcnt lgkmcnt(6)\n"
    "v_mfma_f32_16x16x32_bf16 v[12:15], a[36:39], v[192:195], v[12:15]\n"
    "ds_read_b128 a[16:19], v234 offset:2048\n"
    "v_mfma_f32_16x16x32_bf16 v[16:19], a[36:39], v[196:199], v[16:19]\n"
    "v_exp_f32 v170, v170\n"
    "v_mfma_f32_16x16x32_bf16 v[20:23], a[36:39], v[200:203], v[20:23]\n"
    "ds_read_b128 a[20:23], v234 offset:6144\n"
    "v_mfma_f32_16x16x32_bf16 v[24:27], a[40:43], v[192:195], v[24:27]\n"
    "ds_read_b128 a[24:27], v235 offset:2048\n"
    "v_mfma_f32_16x16x32_bf16 v[28:31], a[40:43], v[196:199], v[28:31]\n"
    "v_exp_f32 v171, v171\n"
    "v_mfma_f32_16x16x32_bf16 v[32:35], a[40:43], v[200:203], v[32:35]\n"
    "ds_read_b128 a[28:31], v235 offset:6144\n"
    "v_cvt_pk_bf16_f32 v204, v168, v169\n"
    "v_mfma_f32_16x16x32_bf16 v[36:39], a[44:47], v[192:195], v[36:39]\n"
    "v_cvt_pk_bf16_f32 v205, v170, v171\n"
    "ds_read_b64_tr_b16 a[32:33], v236 offset:8192\n"
    "v_mfma_f32_16x16x32_bf16 v[40:43], a[44:47], v[196:199], v[40:43]\n"
    "v_exp_f32 v180, v180\n"
    "v_mfma_f32_16x16x32_bf16 v[44:47], a[44:47], v[200:203], v[44:47]\n"
    "v_exp_f32 v181, v181\n"
    "v_mfma_f32_16x16x32_bf16 v[48:51], a[48:51], v[192:195], v[48:51]\n"
    "v_exp_f32 v182, v182\n"
    "v_mfma_f32_16x16x32_bf16 v[52:55], a[48:51], v[196:199], v[52:55]\n"
    "v_exp_f32 v183, v183\n"
    "v_mfma_f32_16x16x32_bf16 v[56:59], a[48:51], v[200:203], v[56:59]\n"
    "ds_read_b64_tr_b16 a[34:35], v236 offset:12288\n"
    "v_cvt_pk_bf16_f32 v206, v180, v181\n"
    "v_mfma_f32_16x16x32_bf16 v[60:63], a[52:55], v[192:195], v[60:63]\n"
    "v_cvt_pk_bf16_f32 v207, v182, v183\n"
    "ds_read_b64_tr_b16 a[36:37], v237 offset:8192\n"
    "v_mfma_f32_16x16x32_bf16 v[64:67], a[52:55], v[196:199], v[64:67]\n"
    "v_exp_f32 v172, v172\n"
    "v_mfma_f32_16x16x32_bf16 v[68:71], a[52:55], v[200:203], v[68:71]\n"
    "ds_read_b64_tr_b16 a[38:39], v237 offset:12288\n"
    "v_mfma_f32_16x16x32_bf16 v[72:75], a[56:59], v[192:195], v[72:75]\n"
    "v_exp_f32 v173, v173\n"
    "v_mfma_f32_16x16x32_bf16 v[76:79], a[56:59], v[196:199], v[76:79]\n"
    "v_exp_f32 v174, v174\n"
    "v_mfma_f32_16x16x32_bf16 v[80:83], a[56:59], v[200:203], v[80:83]\n"
    "ds_read_b64_tr_b16 a[40:41], v236 offset:8704\n"
    "s_waitcnt lgkmcnt(4)\n"
    "v_mfma_f32_16x16x32_bf16 v[84:87], a[60:63], v[192:195], v[84:87]\n"
    "v_exp_f32 v175, v175\n"
    "v_mfma_f32_16x16x32_bf16 v[88:91], a[60:63], v[196:199], v[88:91]\n"
    "v_cvt_pk_bf16_f32 v208, v172, v173\n"
    "ds_read_b64_tr_b16 a[42:43], v236 offset:12800\n"
    "v_mfma_f32_16x16x32_bf16 v[92:95], a[60:63], v[200:203], v[92:95]\n"
    "v_cvt_pk_bf16_f32 v209, v174, v175\n"
    "ds_read_b64_tr_b16 a[44:45], v237 offset:8704\n"
    "v_mfma_f32_16x16x32_bf16 v[144:147], a[0:3], v[96:99], v[216:219]\n"
    "v_exp_f32 v184, v184\n"
    "v_mfma_f32_16x16x32_bf16 v[148:151], a[0:3], v[100:103], v[220:223]\n"
    "v_exp_f32 v185, v185\n"
    "v_mfma_f32_16x16x32_bf16 v[152:155], a[0:3], v[104:107], v[224:227]\n"
    "ds_read_b64_tr_b16 a[46:47], v237 offset:12800\n"
    "v_mfma_f32_16x16x32_bf16 v[156:159], a[4:7], v[96:99], v[216:219]\n"
    "v_exp_f32 v186, v186\n"
    "v_mfma_f32_16x16x32_bf16 v[160:163], a[4:7], v[100:103], v[220:223]\n"
    "v_exp_f32 v187, v187\n"
    "v_mfma_f32_16x16x32_bf16 v[164:167], a[4:7], v[104:107], v[224:227]\n"
    "ds_read_b64_tr_b16 a[48:49], v236 offset:9216\n"
    "v_cvt_pk_bf16_f32 v210, v184, v185\n"
    "v_mfma_f32_16x16x32_bf16 v[144:147], a[8:11], v[108:111], v[144:147]\n"
    "v_cvt_pk_bf16_f32 v211, v186, v187\n"
    "ds_read_b64_tr_b16 a[50:51], v236 offset:13312\n"
    "v_mfma_f32_16x16x32_bf16 v[148:151], a[8:11], v[112:115], v[148:151]\n"
    "v_exp_f32 v176, v176\n"
    "v_mfma_f32_16x16x32_bf16 v[152:155], a[8:11], v[116:119], v[152:155]\n"
    "v_exp_f32 v177, v177\n"
    "v_mfma_f32_16x16x32_bf16 v[156:159], a[12:15], v[108:111], v[156:159]\n"
    "ds_read_b64_tr_b16 a[52:53], v237 offset:9216\n"
    "v_mfma_f32_16x16x32_bf16 v[160:163], a[12:15], v[112:115], v[160:163]\n"
    "v_exp_f32 v178, v178\n"
    "v_mfma_f32_16x16x32_bf16 v[164:167], a[12:15], v[116:119], v[164:167]\n"
    "v_exp_f32 v179, v179\n"
    "v_mfma_f32_16x16x32_bf16 v[144:147], a[16:19], v[120:123], v[144:147]\n"
    "ds_read_b64_tr_b16 a[54:55], v237 offset:13312\n"
    "v_cvt_pk_bf16_f32 v212, v176, v177\n"
    "v_mfma_f32_16x16x32_bf16 v[148:151], a[16:19], v[124:127], v[148:151]\n"
    "ds_read_b64_tr_b16 a[56:57], v236 offset:9728\n"
    "v_cvt_pk_bf16_f32 v213, v178, v179\n"
    "v_mfma_f32_16x16x32_bf16 v[152:155], a[16:19], v[128:131], v[152:155]\n"
    "v_exp_f32 v188, v188\n"
    "v_mfma_f32_16x16x32_bf16 v[156:159], a[20:23], v[120:123], v[156:159]\n"
    "ds_read_b64_tr_b16 a[58:59], v236 offset:13824\n"
    "v_mfma_f32_16x16x32_bf16 v[160:163], a[20:23], v[124:127], v[160:163]\n"
    "v_exp_f32 v189, v189\n"
    "v_mfma_f32_16x16x32_bf16 v[164:167], a[20:23], v[128:131], v[164:167]\n"
    "v_exp_f32 v190, v190\n"
    "v_mfma_f32_16x16x32_bf16 v[144:147], a[24:27], v[132:135], v[144:147]\n"
    "ds_read_b64_tr_b16 a[60:61], v237 offset:9728\n"
    "v_mfma_f32_16x16x32_bf16 v[148:151], a[24:27], v[136:139], v[148:151]\n"
    "v_exp_f32 v191, v191\n"
    "v_mfma_f32_16x16x32_bf16 v[152:155], a[24:27], v[140:143], v[152:155]\n"
    "v_cvt_pk_bf16_f32 v214, v188, v189\n"
    "ds_read_b64_tr_b16 a[62:63], v237 offset:13824\n"
    "v_mfma_f32_16x16x32_bf16 v[156:159], a[28:31], v[132:135], v[156:159]\n"
    "v_cvt_pk_bf16_f32 v215, v190, v191\n"
    "v_mfma_f32_16x16x32_bf16 v[160:163], a[28:31], v[136:139], v[160:163]\n"
    "v_mfma_f32_16x16x32_bf16 v[164:167], a[28:31], v[140:143], v[164:167]\n"
    "v_subrev_u32 v236, 131072, v236\n"
    "v_subrev_u32 v237, 131072, v237\n"
    "v_mfma_f32_16x16x32_bf16 a[64:67], a[76:79], v[204:207], a[64:67]\n"
    "ds_read_b128 a[0:3], v234 offset:8192\n"
    "v_mfma_f32_16x16x32_bf16 a[68:71], a[76:79], v[208:211], a[68:71]\n"
    "ds_read_b128 a[4:7], v234 offset:12288\n"
    "v_mfma_f32_16x16x32_bf16 a[72:75], a[76:79], v[212:215], a[72:75]\n"
    "v_exp_f32 v144, v144\n"
    "s_waitcnt lgkmcnt(4)\n"
    "v_mfma_f32_16x16x32_bf16 v[0:3], a[32:35], v[204:207], v[0:3]\n"
    "ds_read_b128 a[8:11], v235 offset:8192\n"
    "v_mfma_f32_16x16x32_bf16 v[4:7], a[32:35], v[208:211], v[4:7]\n"
    "ds_read_b128 a[12:15], v235 offset:12288\n"
    "s_add_u32 s92, s76, 0\n"
    "v_mfma_f32_16x16x32_bf16 v[8:11], a[32:35], v[212:215], v[8:11]\n"
    "v_exp_f32 v145, v145\n"
    "v_mfma_f32_16x16x32_bf16 v[12:15], a[36:39], v[204:207], v[12:15]\n"
    "ds_read_b128 a[16:19], v234 offset:10240\n"
    "v_mfma_f32_16x16x32_bf16 v[16:19], a[36:39], v[208:211], v[16:19]\n"
    "v_exp_f32 v146, v146\n"
    "v_mfma_f32_16x16x32_bf16 v[20:23], a[36:39], v[212:215], v[20:23]\n"
    "ds_read_b128 a[20:23], v234 offset:14336\n"
    "buffer_load_dwordx4 a[96:99], v230, s[80:83], s92 offen offset:0 sc1\n"
    "v_mfma_f32_16x16x32_bf16 v[24:27], a[40:43], v[204:207], v[24:27]\n"
    "ds_read_b128 a[24:27], v235 offset:10240\n"
    "v_mfma_f32_16x16x32_bf16 v[28:31], a[40:43], v[208:211], v[28:31]\n"
    "v_exp_f32 v147, v147\n"
    "v_mfma_f32_16x16x32_bf16 v[32:35], a[40:43], v[212:215], v[32:35]\n"
    "ds_read_b128 a[28:31], v235 offset:14336\n"
    "v_cvt_pk_bf16_f32 v192, v144, v145\n"
    "v_mfma_f32_16x16x32_bf16 v[36:39], a[44:47], v[204:207], v[36:39]\n"
    "v_cvt_pk_bf16_f32 v193, v146, v147\n"
    "ds_read_b64_tr_b16 a[32:33], v236 offset:0\n"
    "v_mfma_f32_16x16x32_bf16 v[40:43], a[44:47], v[208:211], v[40:43]\n"
    "v_exp_f32 v156, v156\n"
    "v_mfma_f32_16x16x32_bf16 v[44:47], a[44:47], v[212:215], v[44:47]\n"
    "v_exp_f32 v157, v157\n"
    "v_mfma_f32_16x16x32_bf16 v[48:51], a[48:51], v[204:207], v[48:51]\n"
    "v_exp_f32 v158, v158\n"
    "v_mfma_f32_16x16x32_bf16 v[52:55], a[48:51], v[208:211], v[52:55]\n"
    "v_exp_f32 v159, v159\n"
    "v_mfma_f32_16x16x32_bf16 v[56:59], a[48:51], v[212:215], v[56:59]\n"
    "ds_read_b64_tr_b16 a[34:35], v236 offset:4096\n"
    "v_cvt_pk_bf16_f32 v194, v156, v157\n"
    "v_mfma_f32_16x16x32_bf16 v[60:63], a[52:55], v[204:207], v[60:63]\n"
    "v_cvt_pk_bf16_f32 v195, v158, v159\n"
    "ds_read_b64_tr_b16 a[36:37], v237 offset:0\n"
    "v_mfma_f32_16x16x32_bf16 v[64:67], a[52:55], v[208:211], v[64:67]\n"
    "v_exp_f32 v148, v148\n"
    "v_mfma_f32_16x16x32_bf16 v[68:71], a[52:55], v[212:215], v[68:71]\n"
    "buffer_load_dwordx4 a[100:103], v230, s[80:83], s92 offen offset:1024 sc1\n"
    "ds_read_b64_tr_b16 a[38:39], v237 offset:4096\n"
    "v_mfma_f32_16x16x32_bf16 v[72:75], a[56:59], v[204:207], v[72:75]\n"
    "v_exp_f32 v149, v149\n"
    "v_mfma_f32_16x16x32_bf16 v[76:79], a[56:59], v[208:211], v[76:79]\n"
    "v_exp_f32 v150, v150\n"
    "v_mfma_f32_16x16x32_bf16 v[80:83], a[56:59], v[212:215], v[80:83]\n"
    "ds_read_b64_tr_b16 a[40:41], v236 offset:512\n"
    "buffer_load_dwordx4 a[104:107], v230, s[80:83], s92 offen offset:2048 sc1\n"
    "s_waitcnt lgkmcnt(4)\n"
    "v_mfma_f32_16x16x32_bf16 v[84:87], a[60:63], v[204:207], v[84:87]\n"
    "v_exp_f32 v151, v151\n"
    "v_mfma_f32_16x16x32_bf16 v[88:91], a[60:63], v[208:211], v[88:91]\n"
    "v_cvt_pk_bf16_f32 v196, v148, v149\n"
    "ds_read_b64_tr_b16 a[42:43], v236 offset:4608\n"
    "v_mfma_f32_16x16x32_bf16 v[92:95], a[60:63], v[212:215], v[92:95]\n"
    "v_cvt_pk_bf16_f32 v197, v150, v151\n"
    "buffer_load_dwordx4 a[108:111], v230, s[80:83], s92 offen offset:3072 sc1\n"
    "v_mfma_f32_16x16x32_bf16 v[168:171], a[0:3], v[96:99], v[216:219]\n"
    "v_exp_f32 v160, v160\n"
    "v_mfma_f32_16x16x32_bf16 v[172:175], a[0:3], v[100:103], v[220:223]\n"
    "ds_read_b64_tr_b16 a[44:45], v237 offset:512\n"
    "buffer_load_dwordx4 a[112:115], v231, s[80:83], s92 offen offset:0 sc1\n"
    "v_mfma_f32_16x16x32_bf16 v[176:179], a[0:3], v[104:107], v[224:227]\n"
    "v_exp_f32 v161, v161\n"
    "v_mfma_f32_16x16x32_bf16 v[180:183], a[4:7], v[96:99], v[216:219]\n"
    "v_exp_f32 v162, v162\n"
    "v_mfma_f32_16x16x32_bf16 v[184:187], a[4:7], v[100:103], v[220:223]\n"
    "ds_read_b64_tr_b16 a[46:47], v237 offset:4608\n"
    "s_add_u32 s92, s77, 0\n"
    "v_mfma_f32_16x16x32_bf16 v[188:191], a[4:7], v[104:107], v[224:227]\n"
    "v_exp_f32 v163, v163\n"
    "v_mfma_f32_16x16x32_bf16 v[168:171], a[8:11], v[108:111], v[168:171]\n"
    "v_cvt_pk_bf16_f32 v198, v160, v161\n"
    "ds_read_b64_tr_b16 a[48:49], v236 offset:1024\n"
    "v_mfma_f32_16x16x32_bf16 v[172:175], a[8:11], v[112:115], v[172:175]\n"
    "v_cvt_pk_bf16_f32 v199, v162, v163\n"
    "ds_read_b64_tr_b16 a[50:51], v236 offset:5120\n"
    "v_mfma_f32_16x16x32_bf16 v[176:179], a[8:11], v[116:119], v[176:179]\n"
    "v_exp_f32 v152, v152\n"
    "v_mfma_f32_16x16x32_bf16 v[180:183], a[12:15], v[108:111], v[180:183]\n"
    "v_exp_f32 v153, v153\n"
    "v_mfma_f32_16x16x32_bf16 v[184:187], a[12:15], v[112:115], v[184:187]\n"
    "ds_read_b64_tr_b16 a[52:53], v237 offset:1024\n"
    "buffer_load_dwordx4 a[116:119], v230, s[80:83], s92 offen offset:0 sc1\n"
    "v_mfma_f32_16x16x32_bf16 v[188:191], a[12:15], v[116:119], v[188:191]\n"
    "v_exp_f32 v154, v154\n"
    "v_mfma_f32_16x16x32_bf16 v[168:171], a[16:19], v[120:123], v[168:171]\n"
    "v_exp_f32 v155, v155\n"
    "v_mfma_f32_16x16x32_bf16 v[172:175], a[16:19], v[124:127], v[172:175]\n"
    "ds_read_b64_tr_b16 a[54:55], v237 offset:5120\n"
    "v_cvt_pk_bf16_f32 v200, v152, v153\n"
    "v_mfma_f32_16x16x32_bf16 v[176:179], a[16:19], v[128:131], v[176:179]\n"
    "v_cvt_pk_bf16_f32 v201, v154, v155\n"
    "buffer_load_dwordx4 a[120:123], v230, s[80:83], s92 offen offset:1024 sc1\n"
    "v_mfma_f32_16x16x32_bf16 v[180:183], a[20:23], v[120:123], v[180:183]\n"
    "ds_read_b64_tr_b16 a[56:57], v236 offset:1536\n"
    "buffer_load_dwordx4 a[124:127], v230, s[80:83], s92 offen offset:2048 sc1\n"
    "v_mfma_f32_16x16x32_bf16 v[184:187], a[20:23], v[124:127], v[184:187]\n"
    "v_exp_f32 v164, v164\n"
    "v_mfma_f32_16x16x32_bf16 v[188:191], a[20:23], v[128:131], v[188:191]\n"
    "v_exp_f32 v165, v165\n"
    "v_mfma_f32_16x16x32_bf16 v[168:171], a[24:27], v[132:135], v[168:171]\n"
    "ds_read_b64_tr_b16 a[58:59], v236 offset:5632\n"
    "buffer_load_dwordx4 a[128:131], v230, s[80:83], s92 offen offset:3072 sc1\n"
    "v_mfma_f32_16x16x32_bf16 v[172:175], a[24:27], v[136:139], v[172:175]\n"
    "v_exp_f32 v166, v166\n"
    "v_mfma_f32_16x16x32_bf16 v[176:179], a[24:27], v[140:143], v[176:179]\n"
    "v_exp_f32 v167, v167\n"
    "v_mfma_f32_16x16x32_bf16 v[180:183], a[28:31], v[132:135], v[180:183]\n"
    "ds_read_b64_tr_b16 a[60:61], v237 offset:1536\n"
    "v_cvt_pk_bf16_f32 v202, v164, v165\n"
    "v_mfma_f32_16x16x32_bf16 v[184:187], a[28:31], v[136:139], v[184:187]\n"
    "ds_read_b64_tr_b16 a[62:63], v237 offset:5632\n"
    "v_cvt_pk_bf16_f32 v203, v166, v167\n"
    "v_mfma_f32_16x16x32_bf16 v[188:191], a[28:31], v[140:143], v[188:191]\n"
    "buffer_load_dwordx4 a[132:135], v231, s[80:83], s92 offen offset:0 sc1\n"
    "s_waitcnt vmcnt(10)\n"
    "s_barrier\n"
    "v_add_u32 v234, 32768, v234\n"
    "v_add_u32 v235, 32768, v235\n"
    "v_mfma_f32_16x16x32_bf16 a[64:67], a[76:79], v[192:195], a[64:67]\n"
    "ds_read_b128 a[0:3], v234 offset:0\n"
    "v_mfma_f32_16x16x32_bf16 a[68:71], a[76:79], v[196:199], a[68:71]\n"
    "ds_read_b128 a[4:7], v234 offset:4096\n"
    "v_mfma_f32_16x16x32_bf16 a[72:75], a[76:79], v[200:203], a[72:75]\n"
    "v_exp_f32 v168, v168\n"
    "s_waitcnt lgkmcnt(4)\n"
    "v_mfma_f32_16x16x32_bf16 v[0:3], a[32:35], v[192:195], v[0:3]\n"
    "ds_read_b128 a[8:11], v235 offset:0\n"
    "v_mfma_f32_16x16x32_bf16 v[4:7], a[32:35], v[196:199], v[4:7]\n"
    "ds_read_b128 a[12:15], v235 offset:4096\n"
    "s_add_u32 s92, s78, 0\n"
    "v_mfma_f32_16x16x32_bf16 v[8:11], a[32:35], v[200:203], v[8:11]\n"
    "v_exp_f32 v169, v169\n"
    "v_mfma_f32_16x16x32_bf16 v[12:15], a[36:39], v[192:195], v[12:15]\n"
    "ds_read_b128 a[16:19], v234 offset:2048\n"
    "buffer_load_dwordx4 a[136:139], v230, s[80:83], s92 offen offset:0 sc1\n"
    "v_mfma_f32_16x16x32_bf16 v[16:19], a[36:39], v[196:199], v[16:19]\n"
    "v_exp_f32 v170, v170\n"
    "v_mfma_f32_16x16x32_bf16 v[20:23], a[36:39], v[200:203], v[20:23]\n"
    "ds_read_b128 a[20:23], v234 offset:6144\n"
    "buffer_load_dwordx4 a[140:143], v230, s[80:83], s92 offen offset:1024 sc1\n"
    "v_mfma_f32_16x16x32_bf16 v[24:27], a[40:43], v[192:195], v[24:27]\n"
    "ds_read_b128 a[24:27], v235 offset:2048\n"
    "v_mfma_f32_16x16x32_bf16 v[28:31], a[40:43], v[196:199], v[28:31]\n"
    "v_exp_f32 v171, v171\n"
    "v_mfma_f32_16x16x32_bf16 v[32:35], a[40:43], v[200:203], v[32:35]\n"
    "ds_read_b128 a[28:31], v235 offset:6144\n"
    "v_cvt_pk_bf16_f32 v204, v168, v169\n"
    "v_mfma_f32_16x16x32_bf16 v[36:39], a[44:47], v[192:195], v[36:39]\n"
    "v_cvt_pk_bf16_f32 v205, v170, v171\n"
    "ds_read_b64_tr_b16 a[32:33], v236 offset:8192\n"
    "v_mfma_f32_16x16x32_bf16 v[40:43], a[44:47], v[196:199], v[40:43]\n"
    "v_exp_f32 v180, v180\n"
    "v_mfma_f32_16x16x32_bf16 v[44:47], a[44:47], v[200:203], v[44:47]\n"
    "v_exp_f32 v181, v181\n"
    "v_mfma_f32_16x16x32_bf16 v[48:51], a[48:51], v[192:195], v[48:51]\n"
    "v_exp_f32 v182, v182\n"
    "v_mfma_f32_16x16x32_bf16 v[52:55], a[48:51], v[196:199], v[52:55]\n"
    "v_exp_f32 v183, v183\n"
    "v_mfma_f32_16x16x32_bf16 v[56:59], a[48:51], v[200:203], v[56:59]\n"
    "ds_read_b64_tr_b16 a[34:35], v236 offset:12288\n"
    "buffer_load_dwordx4 a[144:147], v230, s[80:83], s92 offen offset:2048 sc1\n"
    "v_mfma_f32_16x16x32_bf16 v[60:63], a[52:55], v[192:195], v[60:63]\n"
    "v_cvt_pk_bf16_f32 v206, v180, v181\n"
    "v_cvt_pk_bf16_f32 v207, v182, v183\n"
    "v_mfma_f32_16x16x32_bf16 v[64:67], a[52:55], v[196:199], v[64:67]\n"
    "buffer_load_dwordx4 a[148:151], v230, s[80:83], s92 offen offset:3072 sc1\n"
    "ds_read_b64_tr_b16 a[36:37], v237 offset:8192\n"
    "v_mfma_f32_16x16x32_bf16 v[68:71], a[52:55], v[200:203], v[68:71]\n"
    "v_exp_f32 v172, v172\n"
    "v_mfma_f32_16x16x32_bf16 v[72:75], a[56:59], v[192:195], v[72:75]\n"
    "v_exp_f32 v173, v173\n"
    "v_mfma_f32_16x16x32_bf16 v[76:79], a[56:59], v[196:199], v[76:79]\n"
    "buffer_load_dwordx4 a[152:155], v231, s[80:83], s92 offen offset:0 sc1\n"
    "ds_read_b64_tr_b16 a[38:39], v237 offset:12288\n"
    "v_mfma_f32_16x16x32_bf16 v[80:83], a[56:59], v[200:203], v[80:83]\n"
    "v_exp_f32 v174, v174\n"
    "s_waitcnt lgkmcnt(3)\n"
    "v_mfma_f32_16x16x32_bf16 v[84:87], a[60:63], v[192:195], v[84:87]\n"
    "v_exp_f32 v175, v175\n"
    "v_mfma_f32_16x16x32_bf16 v[88:91], a[60:63], v[196:199], v[88:91]\n"
    "s_add_u32 s92, s79, 0\n"
    "ds_read_b64_tr_b16 a[40:41], v236 offset:8704\n"
    "v_mfma_f32_16x16x32_bf16 v[92:95], a[60:63], v[200:203], v[92:95]\n"
    "v_cvt_pk_bf16_f32 v208, v172, v173\n"
    "buffer_load_dwordx4 a[156:159], v230, s[80:83], s92 offen offset:0 sc1\n"
    "v_mfma_f32_16x16x32_bf16 v[144:147], a[0:3], v[96:99], v[216:219]\n"
    "v_cvt_pk_bf16_f32 v209, v174, v175\n"
    "ds_read_b64_tr_b16 a[42:43], v236 offset:12800\n"
    "v_mfma_f32_16x16x32_bf16 v[148:151], a[0:3], v[100:103], v[220:223]\n"
    "v_exp_f32 v184, v184\n"
    "v_mfma_f32_16x16x32_bf16 v[152:155], a[0:3], v[104:107], v[224:227]\n"
    "buffer_load_dwordx4 a[160:163], v230, s[80:83], s92 offen offset:1024 sc1\n"
    "ds_read_b64_tr_b16 a[44:45], v237 offset:8704\n"
    "v_mfma_f32_16x16x32_bf16 v[156:159], a[4:7], v[96:99], v[216:219]\n"
    "v_exp_f32 v185, v185\n"
    "v_mfma_f32_16x16x32_bf16 v[160:163], a[4:7], v[100:103], v[220:223]\n"
    "v_exp_f32 v186, v186\n"
    "v_mfma_f32_16x16x32_bf16 v[164:167], a[4:7], v[104:107], v[224:227]\n"
    "buffer_load_dwordx4 a[164:167], v230, s[80:83], s92 offen offset:2048 sc1\n"
    "ds_read_b64_tr_b16 a[46:47], v237 offset:12800\n"
    "v_mfma_f32_16x16x32_bf16 v[144:147], a[8:11], v[108:111], v[144:147]\n"
    "v_exp_f32 v187, v187\n"
    "v_mfma_f32_16x16x32_bf16 v[148:151], a[8:11], v[112:115], v[148:151]\n"
    "v_cvt_pk_bf16_f32 v210, v184, v185\n"
    "buffer_load_dwordx4 a[168:171], v230, s[80:83], s92 offen offset:3072 sc1\n"
    "v_mfma_f32_16x16x32_bf16 v[152:155], a[8:11], v[116:119], v[152:155]\n"
    "v_cvt_pk_bf16_f32 v211, v186, v187\n"
    "ds_read_b64_tr_b16 a[48:49], v236 offset:9216\n"
    "v_mfma_f32_16x16x32_bf16 v[156:159], a[12:15], v[108:111], v[156:159]\n"
    "buffer_load_dwordx4 a[172:175], v231, s[80:83], s92 offen offset:0 sc1\n"
    "ds_read_b64_tr_b16 a[50:51], v236 offset:13312\n"
    "v_mfma_f32_16x16x32_bf16 v[160:163], a[12:15], v[112:115], v[160:163]\n"
    "v_exp_f32 v176, v176\n"
    "v_mfma_f32_16x16x32_bf16 v[164:167], a[12:15], v[116:119], v[164:167]\n"
    "v_exp_f32 v177, v177\n"
    "v_mfma_f32_16x16x32_bf16 v[144:147], a[16:19], v[120:123], v[144:147]\n"
    "s_add_u32 s92, s76, 40960\n"
    "ds_read_b64_tr_b16 a[52:53], v237 offset:9216\n"
    "v_mfma_f32_16x16x32_bf16 v[148:151], a[16:19], v[124:127], v[148:151]\n"
    "v_exp_f32 v178, v178\n"
    "v_mfma_f32_16x16x32_bf16 v[152:155], a[16:19], v[128:131], v[152:155]\n"
    "v_exp_f32 v179, v179\n"
    "v_mfma_f32_16x16x32_bf16 v[156:159], a[20:23], v[120:123], v[156:159]\n"
    "s_add_u32 m0, s96, 0\n"
    "v_cvt_pk_bf16_f32 v212, v176, v177\n"
    "v_mfma_f32_16x16x32_bf16 v[160:163], a[20:23], v[124:127], v[160:163]\n"
    "ds_read_b64_tr_b16 a[54:55], v237 offset:13312\n"
    "v_cvt_pk_bf16_f32 v213, v178, v179\n"
    "v_mfma_f32_16x16x32_bf16 v[164:167], a[20:23], v[128:131], v[164:167]\n"
    "buffer_load_dwordx4 v230, s[80:83], s92 offen offset:0 sc1 lds\n"
    "ds_read_b64_tr_b16 a[56:57], v236 offset:9728\n"
    "v_mfma_f32_16x16x32_bf16 v[144:147], a[24:27], v[132:135], v[144:147]\n"
    "v_exp_f32 v188, v188\n"
    "v_mfma_f32_16x16x32_bf16 v[148:151], a[24:27], v[136:139], v[148:151]\n"
    "v_exp_f32 v189, v189\n"
    "v_mfma_f32_16x16x32_bf16 v[152:155], a[24:27], v[140:143], v[152:155]\n"
    "buffer_load_dwordx4 v230, s[80:83], s92 offen offset:1024 sc1 lds\n"
    "ds_read_b64_tr_b16 a[58:59], v236 offset:13824\n"
    "v_mfma_f32_16x16x32_bf16 v[156:159], a[28:31], v[132:135], v[156:159]\n"
    "v_exp_f32 v190, v190\n"
    "v_mfma_f32_16x16x32_bf16 v[160:163], a[28:31], v[136:139], v[160:163]\n"
    "v_exp_f32 v191, v191\n"
    "v_mfma_f32_16x16x32_bf16 v[164:167], a[28:31], v[140:143], v[164:167]\n"
    "buffer_load_dwordx4 v230, s[80:83], s92 offen offset:2048 sc1 lds\n"
    "v_cvt_pk_bf16_f32 v214, v188, v189\n"
    "ds_read_b64_tr_b16 a[60:61], v237 offset:9728\n"
    "ds_read_b64_tr_b16 a[62:63], v237 offset:13824\n"
    "v_cvt_pk_bf16_f32 v215, v190, v191\n"
    "buffer_load_dwordx4 v230, s[80:83], s92 offen offset:3072 sc1 lds\n"
    "s_add_u32 m0, s96, 4096\n"
    "s_nop 0\n"
    "buffer_load_dwordx4 v231, s[80:83], s92 offen offset:0 sc1 lds\n"
    "v_add_u32 v236, 32768, v236\n"
    "v_add_u32 v237, 32768, v237\n"
    "v_mfma_f32_16x16x32_bf16 a[64:67], a[76:79], v[204:207], a[64:67]\n"
    "ds_read_b128 a[0:3], v234 offset:8192\n"
    "v_mfma_f32_16x16x32_bf16 a[68:71], a[76:79], v[208:211], a[68:71]\n"
    "ds_read_b128 a[4:7], v234 offset:12288\n"
    "v_mfma_f32_16x16x32_bf16 a[72:75], a[76:79], v[212:215], a[72:75]\n"
    "v_exp_f32 v144, v144\n"
    "s_waitcnt lgkmcnt(5)\n"
    "v_mfma_f32_16x16x32_bf16 v[0:3], a[32:35], v[204:207], v[0:3]\n"
    "ds_read_b128 a[8:11], v235 offset:8192\n"
    "v_mfma_f32_16x16x32_bf16 v[4:7], a[32:35], v[208:211], v[4:7]\n"
    "ds_read_b128 a[12:15], v235 offset:12288\n"
    "s_add_u32 s92, s76, 20480\n"
    "v_mfma_f32_16x16x32_bf16 v[8:11], a[32:35], v[212:215], v[8:11]\n"
    "v_exp_f32 v145, v145\n"
    "v_mfma_f32_16x16x32_bf16 v[12:15], a[36:39], v[204:207], v[12:15]\n"
    "ds_read_b128 a[16:19], v234 offset:10240\n"
    "buffer_load_dwordx4 a[176:179], v230, s[80:83], s92 offen offset:0 sc1\n"
    "v_mfma_f32_16x16x32_bf16 v[16:19], a[36:39], v[208:211], v[16:19]\n"
    "v_exp_f32 v146, v146\n"
    "v_mfma_f32_16x16x32_bf16 v[20:23], a[36:39], v[212:215], v[20:23]\n"
    "ds_read_b128 a[20:23], v234 offset:14336\n"
    "buffer_load_dwordx4 a[180:183], v230, s[80:83], s92 offen offset:1024 sc1\n"
    "v_mfma_f32_16x16x32_bf16 v[24:27], a[40:43], v[204:207], v[24:27]\n"
    "ds_read_b128 a[24:27], v235 offset:10240\n"
    "v_mfma_f32_16x16x32_bf16 v[28:31], a[40:43], v[208:211], v[28:31]\n"
    "v_exp_f32 v147, v147\n"
    "v_mfma_f32_16x16x32_bf16 v[32:35], a[40:43], v[212:215], v[32:35]\n"
    "ds_read_b128 a[28:31], v235 offset:14336\n"
    "v_cvt_pk_bf16_f32 v192, v144, v145\n"
    "v_mfma_f32_16x16x32_bf16 v[36:39], a[44:47], v[204:207], v[36:39]\n"
    "v_cvt_pk_bf16_f32 v193, v146, v147\n"
    "ds_read_b64_tr_b16 a[32:33], v236 offset:0\n"
    "v_mfma_f32_16x16x32_bf16 v[40:43], a[44:47], v[208:211], v[40:43]\n"
    "v_exp_f32 v156, v156\n"
    "v_mfma_f32_16x16x32_bf16 v[44:47], a[44:47], v[212:215], v[44:47]\n"
    "v_exp_f32 v157, v157\n"
    "v_mfma_f32_16x16x32_bf16 v[48:51], a[48:51], v[204:207], v[48:51]\n"
    "v_exp_f32 v158, v158\n"
    "v_mfma_f32_16x16x32_bf16 v[52:55], a[48:51], v[208:211], v[52:55]\n"
    "v_exp_f32 v159, v159\n"
    "v_mfma_f32_16x16x32_bf16 v[56:59], a[48:51], v[212:215], v[56:59]\n"
    "ds_read_b64_tr_b16 a[34:35], v236 offset:4096\n"
    "buffer_load_dwordx4 a[184:187], v230, s[80:83], s92 offen offset:2048 sc1\n"
    "v_mfma_f32_16x16x32_bf16 v[60:63], a[52:55], v[204:207], v[60:63]\n"
    "v_cvt_pk_bf16_f32 v194, v156, v157\n"
    "v_cvt_pk_bf16_f32 v195, v158, v159\n"
    "v_mfma_f32_16x16x32_bf16 v[64:67], a[52:55], v[208:211], v[64:67]\n"
    "buffer_load_dwordx4 a[188:191], v230, s[80:83], s92 offen offset:3072 sc1\n"
    "ds_read_b64_tr_b16 a[36:37], v237 offset:0\n"
    "v_mfma_f32_16x16x32_bf16 v[68:71], a[52:55], v[212:215], v[68:71]\n"
    "v_exp_f32 v148, v148\n"
    "s_waitcnt lgkmcnt(4)\n"
    "v_mfma_f32_16x16x32_bf16 v[72:75], a[56:59], v[204:207], v[72:75]\n"
    "v_exp_f32 v149, v149\n"
    "v_mfma_f32_16x16x32_bf16 v[76:79], a[56:59], v[208:211], v[76:79]\n"
    "buffer_load_dwordx4 a[192:195], v231, s[80:83], s92 offen offset:0 sc1\n"
    "ds_read_b64_tr_b16 a[38:39], v237 offset:4096\n"
    "v_mfma_f32_16x16x32_bf16 v[80:83], a[56:59], v[212:215], v[80:83]\n"
    "v_exp_f32 v150, v150\n"
    "v_mfma_f32_16x16x32_bf16 v[84:87], a[60:63], v[204:207], v[84:87]\n"
    "v_exp_f32 v151, v151\n"
    "v_mfma_f32_16x16x32_bf16 v[88:91], a[60:63], v[208:211], v[88:91]\n"
    "s_add_u32 s92, s77, 20480\n"
    "ds_read_b64_tr_b16 a[40:41], v236 offset:512\n"
    "v_mfma_f32_16x16x32_bf16 v[92:95], a[60:63], v[212:215], v[92:95]\n"
    "v_cvt_pk_bf16_f32 v196, v148, v149\n"
    "buffer_load_dwordx4 a[196:199], v230, s[80:83], s92 offen offset:0 sc1\n"
    "v_mfma_f32_16x16x32_bf16 v[168:171], a[0:3], v[96:99], v[216:219]\n"
    "v_cvt_pk_bf16_f32 v197, v150, v151\n"
    "ds_read_b64_tr_b16 a[42:43], v236 offset:4608\n"
    "v_mfma_f32_16x16x32_bf16 v[172:175], a[0:3], v[100:103], v[220:223]\n"
    "v_exp_f32 v160, v160\n"
    "v_mfma_f32_16x16x32_bf16 v[176:179], a[0:3], v[104:107], v[224:227]\n"
    "buffer_load_dwordx4 a[200:203], v230, s[80:83], s92 offen offset:1024 sc1\n"
    "ds_read_b64_tr_b16 a[44:45], v237 offset:512\n"
    "v_mfma_f32_16x16x32_bf16 v[180:183], a[4:7], v[96:99], v[216:219]\n"
    "v_exp_f32 v161, v161\n"
    "v_mfma_f32_16x16x32_bf16 v[184:187], a[4:7], v[100:103], v[220:223]\n"
    "v_exp_f32 v162, v162\n"
    "v_mfma_f32_16x16x32_bf16 v[188:191], a[4:7], v[104:107], v[224:227]\n"
    "buffer_load_dwordx4 a[204:207], v230, s[80:83], s92 offen offset:2048 sc1\n"
    "ds_read_b64_tr_b16 a[46:47], v237 offset:4608\n"
    "v_mfma_f32_16x16x32_bf16 v[168:171], a[8:11], v[108:111], v[168:171]\n"
    "v_exp_f32 v163, v163\n"
    "v_mfma_f32_16x16x32_bf16 v[172:175], a[8:11], v[112:115], v[172:175]\n"
    "v_cvt_pk_bf16_f32 v198, v160, v161\n"
    "buffer_load_dwordx4 a[208:211], v230, s[80:83], s92 offen offset:3072 sc1\n"
    "v_mfma_f32_16x16x32_bf16 v[176:179], a[8:11], v[116:119], v[176:179]\n"
    "v_cvt_pk_bf16_f32 v199, v162, v163\n"
    "ds_read_b64_tr_b16 a[48:49], v236 offset:1024\n"
    "v_mfma_f32_16x16x32_bf16 v[180:183], a[12:15], v[108:111], v[180:183]\n"
    "buffer_load_dwordx4 a[212:215], v231, s[80:83], s92 offen offset:0 sc1\n"
    "ds_read_b64_tr_b16 a[50:51], v236 offset:5120\n"
    "v_mfma_f32_16x16x32_bf16 v[184:187], a[12:15], v[112:115], v[184:187]\n"
    "v_exp_f32 v152, v152\n"
    "v_mfma_f32_16x16x32_bf16 v[188:191], a[12:15], v[116:119], v[188:191]\n"
    "v_exp_f32 v153, v153\n"
    "v_mfma_f32_16x16x32_bf16 v[168:171], a[16:19], v[120:123], v[168:171]\n"
    "s_add_u32 s92, s77, 40960\n"
    "ds_read_b64_tr_b16 a[52:53], v237 offset:1024\n"
    "v_mfma_f32_16x16x32_bf16 v[172:175], a[16:19], v[124:127], v[172:175]\n"
    "v_exp_f32 v154, v154\n"
    "v_mfma_f32_16x16x32_bf16 v[176:179], a[16:19], v[128:131], v[176:179]\n"
    "v_exp_f32 v155, v155\n"
    "v_mfma_f32_16x16x32_bf16 v[180:183], a[20:23], v[120:123], v[180:183]\n"
    "s_add_u32 m0, s96, 5120\n"
    "v_cvt_pk_bf16_f32 v200, v152, v153\n"
    "v_mfma_f32_16x16x32_bf16 v[184:187], a[20:23], v[124:127], v[184:187]\n"
    "ds_read_b64_tr_b16 a[54:55], v237 offset:5120\n"
    "v_cvt_pk_bf16_f32 v201, v154, v155\n"
    "v_mfma_f32_16x16x32_bf16 v[188:191], a[20:23], v[128:131], v[188:191]\n"
    "buffer_load_dwordx4 v230, s[80:83], s92 offen offset:0 sc1 lds\n"
    "ds_read_b64_tr_b16 a[56:57], v236 offset:1536\n"
    "v_mfma_f32_16x16x32_bf16 v[168:171], a[24:27], v[132:135], v[168:171]\n"
    "v_exp_f32 v164, v164\n"
    "v_mfma_f32_16x16x32_bf16 v[172:175], a[24:27], v[136:139], v[172:175]\n"
    "v_exp_f32 v165, v165\n"
    "v_mfma_f32_16x16x32_bf16 v[176:179], a[24:27], v[140:143], v[176:179]\n"
    "buffer_load_dwordx4 v230, s[80:83], s92 offen offset:1024 sc1 lds\n"
    "ds_read_b64_tr_b16 a[58:59], v236 offset:5632\n"
    "s_waitcnt lgkmcnt(4)\n"
    "v_mfma_f32_16x16x32_bf16 v[180:183], a[28:31], v[132:135], v[180:183]\n"
    "v_exp_f32 v166, v166\n"
    "v_mfma_f32_16x16x32_bf16 v[184:187], a[28:31], v[136:139], v[184:187]\n"
    "v_exp_f32 v167, v167\n"
    "v_mfma_f32_16x16x32_bf16 v[188:191], a[28:31], v[140:143], v[188:191]\n"
    "buffer_load_dwordx4 v230, s[80:83], s92 offen offset:2048 sc1 lds\n"
    "v_cvt_pk_bf16_f32 v202, v164, v165\n"
    "ds_read_b64_tr_b16 a[60:61], v237 offset:1536\n"
    "ds_read_b64_tr_b16 a[62:63], v237 offset:5632\n"
    "v_cvt_pk_bf16_f32 v203, v166, v167\n"
    "buffer_load_dwordx4 v230, s[80:83], s92 offen offset:3072 sc1 lds\n"
    "s_add_u32 m0, s96, 9216\n"
    "s_nop 0\n"
    "buffer_load_dwordx4 v231, s[80:83], s92 offen offset:0 sc1 lds\n"
    "v_mfma_f32_16x16x32_bf16 a[64:67], a[76:79], v[192:195], a[64:67]\n"
    "v_mfma_f32_16x16x32_bf16 a[68:71], a[76:79], v[196:199], a[68:71]\n"
    "v_exp_f32 v168, v168\n"
    "v_mfma_f32_16x16x32_bf16 a[72:75], a[76:79], v[200:203], a[72:75]\n"
    "v_exp_f32 v169, v169\n"
    "v_mfma_f32_16x16x32_bf16 v[0:3], a[32:35], v[192:195], v[0:3]\n"
    "v_exp_f32 v170, v170\n"
    "v_mfma_f32_16x16x32_bf16 v[4:7], a[32:35], v[196:199], v[4:7]\n"
    "v_exp_f32 v171, v171\n"
    "v_mfma_f32_16x16x32_bf16 v[8:11], a[32:35], v[200:203], v[8:11]\n"
    "v_cvt_pk_bf16_f32 v204, v168, v169\n"
    "v_cvt_pk_bf16_f32 v205, v170, v171\n"
    "v_mfma_f32_16x16x32_bf16 v[12:15], a[36:39], v[192:195], v[12:15]\n"
    "s_add_u32 s92, s78, 20480\n"
    "buffer_load_dwordx4 a[216:219], v230, s[80:83], s92 offen offset:0 sc1\n"
    "v_mfma_f32_16x16x32_bf16 v[16:19], a[36:39], v[196:199], v[16:19]\n"
    "v_exp_f32 v180, v180\n"
    "v_mfma_f32_16x16x32_bf16 v[20:23], a[36:39], v[200:203], v[20:23]\n"
    "v_exp_f32 v181, v181\n"
    "v_mfma_f32_16x16x32_bf16 v[24:27], a[40:43], v[192:195], v[24:27]\n"
    "v_exp_f32 v182, v182\n"
    "v_mfma_f32_16x16x32_bf16 v[28:31], a[40:43], v[196:199], v[28:31]\n"
    "v_exp_f32 v183, v183\n"
    "v_mfma_f32_16x16x32_bf16 v[32:35], a[40:43], v[200:203], v[32:35]\n"
    "v_cvt_pk_bf16_f32 v206, v180, v181\n"
    "buffer_load_dwordx4 a[220:223], v230, s[80:83], s92 offen offset:1024 sc1\n"
    "v_mfma_f32_16x16x32_bf16 v[36:39], a[44:47], v[192:195], v[36:39]\n"
    "v_cvt_pk_bf16_f32 v207, v182, v183\n"
    "buffer_load_dwordx4 a[224:227], v230, s[80:83], s92 offen offset:2048 sc1\n"
    "v_mfma_f32_16x16x32_bf16 v[40:43], a[44:47], v[196:199], v[40:43]\n"
    "v_exp_f32 v172, v172\n"
    "v_mfma_f32_16x16x32_bf16 v[44:47], a[44:47], v[200:203], v[44:47]\n"
    "ds_read_b64_tr_b16 a[32:33], v236 offset:8192\n"
    "buffer_load_dwordx4 a[228:231], v230, s[80:83], s92 offen offset:3072 sc1\n"
    "v_mfma_f32_16x16x32_bf16 v[48:51], a[48:51], v[192:195], v[48:51]\n"
    "v_exp_f32 v173, v173\n"
    "v_mfma_f32_16x16x32_bf16 v[52:55], a[48:51], v[196:199], v[52:55]\n"
    "v_exp_f32 v174, v174\n"
    "v_mfma_f32_16x16x32_bf16 v[56:59], a[48:51], v[200:203], v[56:59]\n"
    "v_exp_f32 v175, v175\n"
    "s_waitcnt lgkmcnt(1)\n"
    "v_mfma_f32_16x16x32_bf16 v[60:63], a[52:55], v[192:195], v[60:63]\n"
    "v_cvt_pk_bf16_f32 v208, v172, v173\n"
    "ds_read_b64_tr_b16 a[34:35], v236 offset:12288\n"
    "v_mfma_f32_16x16x32_bf16 v[64:67], a[52:55], v[196:199], v[64:67]\n"
    "v_cvt_pk_bf16_f32 v209, v174, v175\n"
    "buffer_load_dwordx4 a[232:235], v231, s[80:83], s92 offen offset:0 sc1\n"
    "v_mfma_f32_16x16x32_bf16 v[68:71], a[52:55], v[200:203], v[68:71]\n"
    "v_exp_f32 v184, v184\n"
    "v_mfma_f32_16x16x32_bf16 v[72:75], a[56:59], v[192:195], v[72:75]\n"
    "v_exp_f32 v185, v185\n"
    "v_mfma_f32_16x16x32_bf16 v[76:79], a[56:59], v[196:199], v[76:79]\n"
    "ds_read_b64_tr_b16 a[36:37], v237 offset:8192\n"
    "s_add_u32 s92, s79, 20480\n"
    "v_mfma_f32_16x16x32_bf16 v[80:83], a[56:59], v[200:203], v[80:83]\n"
    "v_exp_f32 v186, v186\n"
    "v_mfma_f32_16x16x32_bf16 v[84:87], a[60:63], v[192:195], v[84:87]\n"
    "v_exp_f32 v187, v187\n"
    "v_mfma_f32_16x16x32_bf16 v[88:91], a[60:63], v[196:199], v[88:91]\n"
    "v_cvt_pk_bf16_f32 v210, v184, v185\n"
    "ds_read_b64_tr_b16 a[38:39], v237 offset:12288\n"
    "v_mfma_f32_16x16x32_bf16 v[92:95], a[60:63], v[200:203], v[92:95]\n"
    "v_cvt_pk_bf16_f32 v211, v186, v187\n"
    "buffer_load_dwordx4 a[236:239], v230, s[80:83], s92 offen offset:0 sc1\n"
    "ds_read_b64_tr_b16 a[40:41], v236 offset:8704\n"
    "ds_read_b64_tr_b16 a[42:43], v236 offset:12800\n"
    "ds_read_b64_tr_b16 a[44:45], v237 offset:8704\n"
    "ds_read_b64_tr_b16 a[46:47], v237 offset:12800\n"
    "ds_read_b64_tr_b16 a[48:49], v236 offset:9216\n"
    "ds_read_b64_tr_b16 a[50:51], v236 offset:13312\n"
    "ds_read_b64_tr_b16 a[52:53], v237 offset:9216\n"
    "ds_read_b64_tr_b16 a[54:55], v237 offset:13312\n"
    "ds_read_b64_tr_b16 a[56:57], v236 offset:9728\n"
    "ds_read_b64_tr_b16 a[58:59], v236 offset:13824\n"
    "ds_read_b64_tr_b16 a[60:61], v237 offset:9728\n"
    "ds_read_b64_tr_b16 a[62:63], v237 offset:13824\n"
    "v_exp_f32 v176, v176\n"
    "v_exp_f32 v177, v177\n"
    "v_exp_f32 v178, v178\n"
    "v_exp_f32 v179, v179\n"
    "v_cvt_pk_bf16_f32 v212, v176, v177\n"
    "v_cvt_pk_bf16_f32 v213, v178, v179\n"
    "v_exp_f32 v188, v188\n"
    "v_exp_f32 v189, v189\n"
    "v_exp_f32 v190, v190\n"
    "v_exp_f32 v191, v191\n"
    "v_cvt_pk_bf16_f32 v214, v188, v189\n"
    "v_cvt_pk_bf16_f32 v215, v190, v191\n"
    "buffer_load_dwordx4 a[240:243], v230, s[80:83], s92 offen offset:1024 sc1\n"
    "buffer_load_dwordx4 a[244:247], v230, s[80:83], s92 offen offset:2048 sc1\n"
    "buffer_load_dwordx4 a[248:251], v230, s[80:83], s92 offen offset:3072 sc1\n"
    "buffer_load_dwordx4 a[252:255], v231, s[80:83], s92 offen offset:0 sc1\n"
    "s_add_u32 s92, s78, 40960\n"
    "s_add_u32 m0, s96, 10240\n"
    "s_nop 0\n"
    "buffer_load_dwordx4 v230, s[80:83], s92 offen offset:0 sc1 lds\n"
    "buffer_load_dwordx4 v230, s[80:83], s92 offen offset:1024 sc1 lds\n"
    "buffer_load_dwordx4 v230, s[80:83], s92 offen offset:2048 sc1 lds\n"
    "buffer_load_dwordx4 v230, s[80:83], s92 offen offset:3072 sc1 lds\n"
    "s_add_u32 m0, s96, 14336\n"
    "s_nop 0\n"
    "buffer_load_dwordx4 v231, s[80:83], s92 offen offset:0 sc1 lds\n"
    "v_mfma_f32_16x16x32_bf16 a[64:67], a[76:79], v[204:207], a[64:67]\n"
    "v_mfma_f32_16x16x32_bf16 a[68:71], a[76:79], v[208:211], a[68:71]\n"
    "v_mfma_f32_16x16x32_bf16 a[72:75], a[76:79], v[212:215], a[72:75]\n"
    "s_waitcnt lgkmcnt(0)\n"
    "v_mfma_f32_16x16x32_bf16 v[0:3], a[32:35], v[204:207], v[0:3]\n"
    "v_mfma_f32_16x16x32_bf16 v[4:7], a[32:35], v[208:211], v[4:7]\n"
    "v_mfma_f32_16x16x32_bf16 v[8:11], a[32:35], v[212:215], v[8:11]\n"
    "v_mfma_f32_16x16x32_bf16 v[12:15], a[36:39], v[204:207], v[12:15]\n"
    "v_mfma_f32_16x16x32_bf16 v[16:19], a[36:39], v[208:211], v[16:19]\n"
    "v_mfma_f32_16x16x32_bf16 v[20:23], a[36:39], v[212:215], v[20:23]\n"
    "v_accvgpr_read_b32 v244, a64\n"
    "v_accvgpr_read_b32 v246, a68\n"
    "v_accvgpr_read_b32 v248, a72\n"
    "s_mov_b32 s90, 1602224128\n"
    "v_cmp_lt_f32 s[38:39], v244, s90\n"
    "v_cmp_lt_f32 vcc, v246, s90\n"
    "s_nop 3\n"
    "s_and_b64 s[92:93], s[38:39], vcc\n"
    "v_cmp_lt_f32 vcc, v248, s90\n"
    "s_nop 3\n"
    "s_and_b64 s[92:93], s[92:93], vcc\n"
    "v_rcp_f32 v244, v244\n"
    "v_rcp_f32 v246, v246\n"
    "v_rcp_f32 v248, v248\n"
    "v_mov_b32 v245, v244\n"
    "v_mov_b32 v247, v246\n"
    "v_mov_b32 v249, v248\n"
    "v_mfma_f32_16x16x32_bf16 v[24:27], a[40:43], v[204:207], v[24:27]\n"
    "v_pk_mul_f32 v[0:1], v[0:1], v[244:245]\n"
    "v_pk_mul_f32 v[2:3], v[2:3], v[244:245]\n"
    "v_pk_mul_f32 v[12:13], v[12:13], v[244:245]\n"
    "v_pk_mul_f32 v[14:15], v[14:15], v[244:245]\n"
    "v_mfma_f32_16x16x32_bf16 v[28:31], a[40:43], v[208:211], v[28:31]\n"
    "v_cvt_pk_bf16_f32 v144, v0, v1\n"
    "v_cvt_pk_bf16_f32 v145, v2, v3\n"
    "v_cvt_pk_bf16_f32 v146, v12, v13\n"
    "v_cvt_pk_bf16_f32 v147, v14, v15\n"
    "v_mfma_f32_16x16x32_bf16 v[32:35], a[40:43], v[212:215], v[32:35]\n"
    "s_add_u32 s91, s98, 0\n"
    "buffer_store_dwordx4 v[144:147], v230, s[56:59], s91 offen offset:0 sc1\n"
    "v_pk_mul_f32 v[4:5], v[4:5], v[246:247]\n"
    "v_pk_mul_f32 v[6:7], v[6:7], v[246:247]\n"
    "v_mfma_f32_16x16x32_bf16 v[36:39], a[44:47], v[204:207], v[36:39]\n"
    "v_pk_mul_f32 v[16:17], v[16:17], v[246:247]\n"
    "v_pk_mul_f32 v[18:19], v[18:19], v[246:247]\n"
    "v_cvt_pk_bf16_f32 v148, v4, v5\n"
    "v_cvt_pk_bf16_f32 v149, v6, v7\n"
    "v_mfma_f32_16x16x32_bf16 v[40:43], a[44:47], v[208:211], v[40:43]\n"
    "v_cvt_pk_bf16_f32 v150, v16, v17\n"
    "v_cvt_pk_bf16_f32 v151, v18, v19\n"
    "s_add_u32 s91, s98, 1024\n"
    "buffer_store_dwordx4 v[148:151], v230, s[56:59], s91 offen offset:0 sc1\n"
    "v_mfma_f32_16x16x32_bf16 v[44:47], a[44:47], v[212:215], v[44:47]\n"
    "v_pk_mul_f32 v[8:9], v[8:9], v[248:249]\n"
    "v_pk_mul_f32 v[10:11], v[10:11], v[248:249]\n"
    "v_pk_mul_f32 v[20:21], v[20:21], v[248:249]\n"
    "v_pk_mul_f32 v[22:23], v[22:23], v[248:249]\n"
    "v_cvt_pk_bf16_f32 v152, v8, v9\n"
    "v_cvt_pk_bf16_f32 v153, v10, v11\n"
    "v_cvt_pk_bf16_f32 v154, v20, v21\n"
    "v_cvt_pk_bf16_f32 v155, v22, v23\n"
    "s_add_u32 s91, s98, 2048\n"
    "buffer_store_dwordx4 v[152:155], v230, s[56:59], s91 offen offset:0 sc1\n"
    "v_mfma_f32_16x16x32_bf16 v[48:51], a[48:51], v[204:207], v[48:51]\n"
    "v_pk_mul_f32 v[24:25], v[24:25], v[244:245]\n"
    "v_pk_mul_f32 v[26:27], v[26:27], v[244:245]\n"
    "v_pk_mul_f32 v[36:37], v[36:37], v[244:245]\n"
    "v_pk_mul_f32 v[38:39], v[38:39], v[244:245]\n"
    "v_mfma_f32_16x16x32_bf16 v[52:55], a[48:51], v[208:211], v[52:55]\n"
    "v_cvt_pk_bf16_f32 v156, v24, v25\n"
    "v_cvt_pk_bf16_f32 v157, v26, v27\n"
    "v_cvt_pk_bf16_f32 v158, v36, v37\n"
    "v_cvt_pk_bf16_f32 v159, v38, v39\n"
    "v_mfma_f32_16x16x32_bf16 v[56:59], a[48:51], v[212:215], v[56:59]\n"
    "s_add_u32 s91, s98, 3072\n"
    "buffer_store_dwordx4 v[156:159], v230, s[56:59], s91 offen offset:0 sc1\n"
    "v_pk_mul_f32 v[28:29], v[28:29], v[246:247]\n"
    "v_pk_mul_f32 v[30:31], v[30:31], v[246:247]\n"
    "v_mfma_f32_16x16x32_bf16 v[60:63], a[52:55], v[204:207], v[60:63]\n"
    "v_pk_mul_f32 v[40:41], v[40:41], v[246:247]\n"
    "v_pk_mul_f32 v[42:43], v[42:43], v[246:247]\n"
    "v_cvt_pk_bf16_f32 v160, v28, v29\n"
    "v_cvt_pk_bf16_f32 v161, v30, v31\n"
    "v_mfma_f32_16x16x32_bf16 v[64:67], a[52:55], v[208:211], v[64:67]\n"
    "v_cvt_pk_bf16_f32 v162, v40, v41\n"
    "v_cvt_pk_bf16_f32 v163, v42, v43\n"
    "s_add_u32 s91, s98, 4096\n"
    "buffer_store_dwordx4 v[160:163], v230, s[56:59], s91 offen offset:0 sc1\n"
    "v_mfma_f32_16x16x32_bf16 v[68:71], a[52:55], v[212:215], v[68:71]\n"
    "v_pk_mul_f32 v[32:33], v[32:33], v[248:249]\n"
    "v_pk_mul_f32 v[34:35], v[34:35], v[248:249]\n"
    "v_pk_mul_f32 v[44:45], v[44:45], v[248:249]\n"
    "v_pk_mul_f32 v[46:47], v[46:47], v[248:249]\n"
    "v_cvt_pk_bf16_f32 v164, v32, v33\n"
    "v_cvt_pk_bf16_f32 v165, v34, v35\n"
    "v_cvt_pk_bf16_f32 v166, v44, v45\n"
    "v_cvt_pk_bf16_f32 v167, v46, v47\n"
    "s_add_u32 s91, s98, 5120\n"
    "buffer_store_dwordx4 v[164:167], v230, s[56:59], s91 offen offset:0 sc1\n"
    "v_mfma_f32_16x16x32_bf16 v[72:75], a[56:59], v[204:207], v[72:75]\n"
    "v_pk_mul_f32 v[48:49], v[48:49], v[244:245]\n"
    "v_pk_mul_f32 v[50:51], v[50:51], v[244:245]\n"
    "v_pk_mul_f32 v[60:61], v[60:61], v[244:245]\n"
    "v_pk_mul_f32 v[62:63], v[62:63], v[244:245]\n"
    "v_mfma_f32_16x16x32_bf16 v[76:79], a[56:59], v[208:211], v[76:79]\n"
    "v_cvt_pk_bf16_f32 v168, v48, v49\n"
    "v_cvt_pk_bf16_f32 v169, v50, v51\n"
    "v_cvt_pk_bf16_f32 v170, v60, v61\n"
    "v_cvt_pk_bf16_f32 v171, v62, v63\n"
    "v_mfma_f32_16x16x32_bf16 v[80:83], a[56:59], v[212:215], v[80:83]\n"
    "s_add_u32 s91, s98, 6144\n"
    "buffer_store_dwordx4 v[168:171], v230, s[56:59], s91 offen offset:0 sc1\n"
    "v_pk_mul_f32 v[52:53], v[52:53], v[246:247]\n"
    "v_pk_mul_f32 v[54:55], v[54:55], v[246:247]\n"
    "v_mfma_f32_16x16x32_bf16 v[84:87], a[60:63], v[204:207], v[84:87]\n"
    "v_pk_mul_f32 v[64:65], v[64:65], v[246:247]\n"
    "v_pk_mul_f32 v[66:67], v[66:67], v[246:247]\n"
    "v_cvt_pk_bf16_f32 v172, v52, v53\n"
    "v_cvt_pk_bf16_f32 v173, v54, v55\n"
    "v_mfma_f32_16x16x32_bf16 v[88:91], a[60:63], v[208:211], v[88:91]\n"
    "v_cvt_pk_bf16_f32 v174, v64, v65\n"
    "v_cvt_pk_bf16_f32 v175, v66, v67\n"
    "s_add_u32 s91, s98, 7168\n"
    "buffer_store_dwordx4 v[172:175], v230, s[56:59], s91 offen offset:0 sc1\n"
    "v_mfma_f32_16x16x32_bf16 v[92:95], a[60:63], v[212:215], v[92:95]\n"
    "v_pk_mul_f32 v[56:57], v[56:57], v[248:249]\n"
    "v_pk_mul_f32 v[58:59], v[58:59], v[248:249]\n"
    "v_pk_mul_f32 v[68:69], v[68:69], v[248:249]\n"
    "v_pk_mul_f32 v[70:71], v[70:71], v[248:249]\n"
    "v_cvt_pk_bf16_f32 v176, v56, v57\n"
    "v_cvt_pk_bf16_f32 v177, v58, v59\n"
    "v_cvt_pk_bf16_f32 v178, v68, v69\n"
    "v_cvt_pk_bf16_f32 v179, v70, v71\n"
    "s_add_u32 s91, s98, 8192\n"
    "buffer_store_dwordx4 v[176:179], v230, s[56:59], s91 offen offset:0 sc1\n"
    "v_pk_mul_f32 v[72:73], v[72:73], v[244:245]\n"
    "v_pk_mul_f32 v[74:75], v[74:75], v[244:245]\n"
    "v_pk_mul_f32 v[84:85], v[84:85], v[244:245]\n"
    "v_pk_mul_f32 v[86:87], v[86:87], v[244:245]\n"
    "v_cvt_pk_bf16_f32 v180, v72, v73\n"
    "v_cvt_pk_bf16_f32 v181, v74, v75\n"
    "v_cvt_pk_bf16_f32 v182, v84, v85\n"
    "v_cvt_pk_bf16_f32 v183, v86, v87\n"
    "s_add_u32 s91, s98, 9216\n"
    "buffer_store_dwordx4 v[180:183], v230, s[56:59], s91 offen offset:0 sc1\n"
    "v_pk_mul_f32 v[76:77], v[76:77], v[246:247]\n"
    "v_pk_mul_f32 v[78:79], v[78:79], v[246:247]\n"
    "v_pk_mul_f32 v[88:89], v[88:89], v[246:247]\n"
    "v_pk_mul_f32 v[90:91], v[90:91], v[246:247]\n"
    "v_cvt_pk_bf16_f32 v184, v76, v77\n"
    "v_cvt_pk_bf16_f32 v185, v78, v79\n"
    "v_cvt_pk_bf16_f32 v186, v88, v89\n"
    "v_cvt_pk_bf16_f32 v187, v90, v91\n"
    "s_add_u32 s91, s98, 10240\n"
    "buffer_store_dwordx4 v[184:187], v230, s[56:59], s91 offen offset:0 sc1\n"
    "v_pk_mul_f32 v[80:81], v[80:81], v[248:249]\n"
    "v_pk_mul_f32 v[82:83], v[82:83], v[248:249]\n"
    "v_pk_mul_f32 v[92:93], v[92:93], v[248:249]\n"
    "v_pk_mul_f32 v[94:95], v[94:95], v[248:249]\n"
    "v_cvt_pk_bf16_f32 v188, v80, v81\n"
    "v_cvt_pk_bf16_f32 v189, v82, v83\n"
    "v_cvt_pk_bf16_f32 v190, v92, v93\n"
    "v_cvt_pk_bf16_f32 v191, v94, v95\n"
    "s_add_u32 s91, s98, 11264\n"
    "buffer_store_dwordx4 v[188:191], v230, s[56:59], s91 offen offset:0 sc1\n"
    "s_cmp_eq_u64 s[92:93], -1\n"
    "s_cselect_b32 s90, 0, 1\n"
    "v_mov_b32 v250, s90\n"
    "s_lshl_b32 s89, s66, 2\n"
    "s_add_u32 s89, s89, 80896\n"
    "v_mov_b32 v251, s89\n"
    "ds_write_b32 v251, v250 offset:0\n"
    "s_waitcnt vmcnt(0)\n"
    "s_waitcnt lgkmcnt(0)\n"
    "s_barrier\n"
    "v_mov_b32 v251, 80896\n"
    "ds_read_b128 v[224:227], v251 offset:0\n"
    "s_waitcnt lgkmcnt(0)\n"
    "v_or_b32 v224, v224, v225\n"
    "v_or_b32 v226, v226, v227\n"
    "v_or_b32 v224, v224, v226\n"
    "s_nop 0\n"
    "v_readfirstlane_b32 s90, v224\n"
    "s_nop 3\n"
    "s_cmp_lg_u32 s90, 0\n"
    "s_cbranch_scc1 L_FALLBACK\n"
    "L_PUBLISH:\n"
    "s_waitcnt vmcnt(0)\n"
    "s_mov_b32 s88, s45\n"
    "v_mov_b32 v244, s88\n"
    "v_mov_b32 v246, s94\n"
    "v_mov_b32 v247, s95\n"
    "s_cmp_eq_u32 s66, 0\n"
    "s_cselect_b64 exec, 1, 0\n"
    "global_store_dwordx2 v244, v[246:247], s[64:65] offset:0 sc1\n"
    "s_mov_b64 exec, -1\n"
    "v_add_u32 v228, s96, v230\n"
    "v_and_b32 v236, 3, v254\n"
    "v_lshlrev_b32 v236, 3, v236\n"
    "v_add_u32 v236, s44, v236\n"
    "s_add_u32 s92, s79, 40960\n"
    "buffer_load_dwordx4 a[0:3], v230, s[80:83], s92 offen offset:0 sc1\n"
    "buffer_load_dwordx4 a[4:7], v230, s[80:83], s92 offen offset:1024 sc1\n"
    "buffer_load_dwordx4 a[8:11], v230, s[80:83], s92 offen offset:2048 sc1\n"
    "buffer_load_dwordx4 a[12:15], v230, s[80:83], s92 offen offset:3072 sc1\n"
    "buffer_load_dwordx4 a[16:19], v231, s[80:83], s92 offen offset:0 sc1\n"
    "s_add_u32 s92, s76, 61440\n"
    "buffer_load_dwordx4 a[20:23], v230, s[80:83], s92 offen offset:0 sc1\n"
    "buffer_load_dwordx4 a[24:27], v230, s[80:83], s92 offen offset:1024 sc1\n"
    "buffer_load_dwordx4 a[28:31], v230, s[80:83], s92 offen offset:2048 sc1\n"
    "buffer_load_dwordx4 a[32:35], v230, s[80:83], s92 offen offset:3072 sc1\n"
    "buffer_load_dwordx4 a[36:39], v231, s[80:83], s92 offen offset:0 sc1\n"
    "s_add_u32 s92, s77, 61440\n"
    "buffer_load_dwordx4 a[40:43], v230, s[80:83], s92 offen offset:0 sc1\n"
    "buffer_load_dwordx4 a[44:47], v230, s[80:83], s92 offen offset:1024 sc1\n"
    "buffer_load_dwordx4 a[48:51], v230, s[80:83], s92 offen offset:2048 sc1\n"
    "buffer_load_dwordx4 a[52:55], v230, s[80:83], s92 offen offset:3072 sc1\n"
    "buffer_load_dwordx4 a[56:59], v231, s[80:83], s92 offen offset:0 sc1\n"
    "s_add_u32 s92, s78, 61440\n"
    "buffer_load_dwordx4 a[60:63], v230, s[80:83], s92 offen offset:0 sc1\n"
    "buffer_load_dwordx4 a[64:67], v230, s[80:83], s92 offen offset:1024 sc1\n"
    "buffer_load_dwordx4 a[68:71], v230, s[80:83], s92 offen offset:2048 sc1\n"
    "buffer_load_dwordx4 a[72:75], v230, s[80:83], s92 offen offset:3072 sc1\n"
    "buffer_load_dwordx4 a[76:79], v231, s[80:83], s92 offen offset:0 sc1\n"
    "s_add_u32 s92, s79, 61440\n"
    "buffer_load_dwordx4 a[80:83], v230, s[80:83], s92 offen offset:0 sc1\n"
    "buffer_load_dwordx4 a[84:87], v230, s[80:83], s92 offen offset:1024 sc1\n"
    "buffer_load_dwordx4 a[88:91], v230, s[80:83], s92 offen offset:2048 sc1\n"
    "buffer_load_dwordx4 a[92:95], v230, s[80:83], s92 offen offset:3072 sc1\n"
    "buffer_load_dwordx4 v[192:195], v231, s[80:83], s92 offen offset:0 sc1\n"
    "global_load_dwordx2 v[240:241], v236, s[64:65] offset:0 sc1\n"
    "ds_read_b128 v[132:135], v228 offset:0\n"
    "ds_read_b128 v[136:139], v228 offset:1024\n"
    "ds_read_b128 v[140:143], v228 offset:2048\n"
    "ds_read_b128 v[144:147], v228 offset:3072\n"
    "ds_read_b128 v[148:151], v228 offset:4096\n"
    "ds_read_b128 v[152:155], v228 offset:5120\n"
    "ds_read_b128 v[156:159], v228 offset:6144\n"
    "ds_read_b128 v[160:163], v228 offset:7168\n"
    "ds_read_b128 v[164:167], v228 offset:8192\n"
    "ds_read_b128 v[168:171], v228 offset:9216\n"
    "ds_read_b128 v[172:175], v228 offset:10240\n"
    "ds_read_b128 v[176:179], v228 offset:11264\n"
    "ds_read_b128 v[180:183], v228 offset:12288\n"
    "ds_read_b128 v[184:187], v228 offset:13312\n"
    "ds_read_b128 v[188:191], v228 offset:14336\n"
    "s_mov_b64 s[80:81], s[20:21]\n"
    "s_add_u32 s80, s80, 25165824\n"
    "s_addc_u32 s81, s81, 0\n"
    "s_and_b32 s81, s81, 65535\n"
    "s_mov_b32 s82, 3932160\n"
    "s_mov_b32 s83, 131072\n"
    "s_mul_i32 s47, s99, 15360\n"
    "s_lshl_b32 s88, s66, 10\n"
    "s_add_u32 s47, s47, s88\n"
    "s_xor_b32 s89, s99, 1\n"
    "s_mul_i32 s97, s89, 15360\n"
    "s_add_u32 s97, s97, s88\n"
    "s_lshl_b32 s88, s99, 2\n"
    "s_add_u32 s88, s88, s66\n"
    "s_lshl_b32 s88, s88, 3\n"
    "s_add_u32 s39, s88, 8192\n"
    "s_lshl_b32 s88, s89, 2\n"
    "s_add_u32 s88, s88, s66\n"
    "s_lshl_b32 s88, s88, 3\n"
    "s_add_u32 s74, s88, 8192\n"
    "s_waitcnt vmcnt(0)\n"
    "v_cmp_eq_u64 vcc, v[240:241], s[94:95]\n"
    "s_nop 3\n"
    "s_cmp_eq_u64 vcc, -1\n"
    "s_cbranch_scc0 L_POLL\n"
    "L_POLL_OK:\n"
    "v_lshlrev_b32 v238, 3, v254\n"
    "v_add_u32 v238, 4096, v238\n"
    "s_cmp_lg_u32 s99, 255\n"
    "s_cbranch_scc1 L_NOALL\n"
    "global_load_dwordx2 v[242:243], v238, s[64:65] offset:0 sc1\n"
    "global_load_dwordx2 v[244:245], v238, s[64:65] offset:512 sc1\n"
    "global_load_dwordx2 v[246:247], v238, s[64:65] offset:1024 sc1\n"
    "global_load_dwordx2 v[248:249], v238, s[64:65] offset:1536 sc1\n"
    "L_NOALL:\n"
    "s_add_u32 s91, s52, 0\n"
    "buffer_load_dwordx4 v[60:63], v230, s[56:59], s91 offen offset:0 sc1\n"
    "buffer_load_dwordx4 v[64:67], v230, s[56:59], s91 offen offset:1024 sc1\n"
    "buffer_load_dwordx4 v[68:71], v230, s[56:59], s91 offen offset:2048 sc1\n"
    "s_add_u32 s91, s53, 0\n"
    "buffer_load_dwordx4 v[72:75], v230, s[56:59], s91 offen offset:0 sc1\n"
    "buffer_load_dwordx4 v[76:79], v230, s[56:59], s91 offen offset:1024 sc1\n"
    "buffer_load_dwordx4 v[80:83], v230, s[56:59], s91 offen offset:2048 sc1\n"
    "s_add_u32 s91, s54, 0\n"
    "buffer_load_dwordx4 v[84:87], v230, s[56:59], s91 offen offset:0 sc1\n"
    "buffer_load_dwordx4 v[88:91], v230, s[56:59], s91 offen offset:1024 sc1\n"
    "buffer_load_dwordx4 v[92:95], v230, s[56:59], s91 offen offset:2048 sc1\n"
    "s_add_u32 s91, s55, 0\n"
    "buffer_load_dwordx4 v[96:99], v230, s[56:59], s91 offen offset:0 sc1\n"
    "buffer_load_dwordx4 v[100:103], v230, s[56:59], s91 offen offset:1024 sc1\n"
    "buffer_load_dwordx4 v[104:107], v230, s[56:59], s91 offen offset:2048 sc1\n"
    "s_add_u32 s91, s52, 12288\n"
    "buffer_load_dwordx4 v[108:111], v230, s[56:59], s91 offen offset:0 sc1\n"
    "buffer_load_dwordx4 v[112:115], v230, s[56:59], s91 offen offset:1024 sc1\n"
    "buffer_load_dwordx4 v[116:119], v230, s[56:59], s91 offen offset:2048 sc1\n"
    "s_add_u32 s91, s53, 12288\n"
    "buffer_load_dwordx4 v[120:123], v230, s[56:59], s91 offen offset:0 sc1\n"
    "buffer_load_dwordx4 v[124:127], v230, s[56:59], s91 offen offset:1024 sc1\n"
    "buffer_load_dwordx4 v[128:131], v230, s[56:59], s91 offen offset:2048 sc1\n"
    "s_lshl_b32 s88, s66, 14\n"
    "v_add_u32 v232, s88, v230\n"
    "s_lshl_b32 s88, s66, 10\n"
    "v_add_u32 v233, s88, v230\n"
    "s_waitcnt vmcnt(15)\n"
    "v_mfma_f32_16x16x32_bf16 v[0:3], a[96:99], v[60:63], 0\n"
    "v_mfma_f32_16x16x32_bf16 v[4:7], a[96:99], v[64:67], 0\n"
    "v_mfma_f32_16x16x32_bf16 v[8:11], a[96:99], v[68:71], 0\n"
    "v_mfma_f32_16x16x32_bf16 v[12:15], a[100:103], v[60:63], 0\n"
    "v_mfma_f32_16x16x32_bf16 v[16:19], a[100:103], v[64:67], 0\n"
    "v_mfma_f32_16x16x32_bf16 v[20:23], a[100:103], v[68:71], 0\n"
    "v_mfma_f32_16x16x32_bf16 v[24:27], a[104:107], v[60:63], 0\n"
    "v_mfma_f32_16x16x32_bf16 v[28:31], a[104:107], v[64:67], 0\n"
    "v_mfma_f32_16x16x32_bf16 v[32:35], a[104:107], v[68:71], 0\n"
    "v_mfma_f32_16x16x32_bf16 v[36:39], a[108:111], v[60:63], 0\n"
    "v_mfma_f32_16x16x32_bf16 v[40:43], a[108:111], v[64:67], 0\n"
    "v_mfma_f32_16x16x32_bf16 v[44:47], a[108:111], v[68:71], 0\n"
    "v_mfma_f32_16x16x32_bf16 v[48:51], a[112:115], v[60:63], 0\n"
    "v_mfma_f32_16x16x32_bf16 v[52:55], a[112:115], v[64:67], 0\n"
    "v_mfma_f32_16x16x32_bf16 v[56:59], a[112:115], v[68:71], 0\n"
    "s_waitcnt vmcnt(12)\n"
    "v_mfma_f32_16x16x32_bf16 v[0:3], a[116:119], v[72:75], v[0:3]\n"
    "s_add_u32 s91, s54, 12288\n"
    "v_mfma_f32_16x16x32_bf16 v[4:7], a[116:119], v[76:79], v[4:7]\n"
    "buffer_load_dwordx4 v[60:63], v230, s[56:59], s91 offen offset:0 sc1\n"
    "v_mfma_f32_16x16x32_bf16 v[8:11], a[116:119], v[80:83], v[8:11]\n"
    "buffer_load_dwordx4 v[64:67], v230, s[56:59], s91 offen offset:1024 sc1\n"
    "v_mfma_f32_16x16x32_bf16 v[12:15], a[120:123], v[72:75], v[12:15]\n"
    "buffer_load_dwordx4 v[68:71], v230, s[56:59], s91 offen offset:2048 sc1\n"
    "v_mfma_f32_16x16x32_bf16 v[16:19], a[120:123], v[76:79], v[16:19]\n"
    "v_mfma_f32_16x16x32_bf16 v[20:23], a[120:123], v[80:83], v[20:23]\n"
    "v_mfma_f32_16x16x32_bf16 v[24:27], a[124:127], v[72:75], v[24:27]\n"
    "v_mfma_f32_16x16x32_bf16 v[28:31], a[124:127], v[76:79], v[28:31]\n"
    "v_mfma_f32_16x16x32_bf16 v[32:35], a[124:127], v[80:83], v[32:35]\n"
    "v_mfma_f32_16x16x32_bf16 v[36:39], a[128:131], v[72:75], v[36:39]\n"
    "v_mfma_f32_16x16x32_bf16 v[40:43], a[128:131], v[76:79], v[40:43]\n"
    "v_mfma_f32_16x16x32_bf16 v[44:47], a[128:131], v[80:83], v[44:47]\n"
    "v_mfma_f32_16x16x32_bf16 v[48:51], a[132:135], v[72:75], v[48:51]\n"
    "v_mfma_f32_16x16x32_bf16 v[52:55], a[132:135], v[76:79], v[52:55]\n"
    "v_mfma_f32_16x16x32_bf16 v[56:59], a[132:135], v[80:83], v[56:59]\n"
    "s_waitcnt vmcnt(12)\n"
    "v_mfma_f32_16x16x32_bf16 v[0:3], a[136:139], v[84:87], v[0:3]\n"
    "s_add_u32 s91, s55, 12288\n"
    "v_mfma_f32_16x16x32_bf16 v[4:7], a[136:139], v[88:91], v[4:7]\n"
    "buffer_load_dwordx4 v[72:75], v230, s[56:59], s91 offen offset:0 sc1\n"
    "v_mfma_f32_16x16x32_bf16 v[8:11], a[136:139], v[92:95], v[8:11]\n"
    "buffer_load_dwordx4 v[76:79], v230, s[56:59], s91 offen offset:1024 sc1\n"
    "v_mfma_f32_16x16x32_bf16 v[12:15], a[140:143], v[84:87], v[12:15]\n"
    "buffer_load_dwordx4 v[80:83], v230, s[56:59], s91 offen offset:2048 sc1\n"
    "v_mfma_f32_16x16x32_bf16 v[16:19], a[140:143], v[88:91], v[16:19]\n"
    "v_mfma_f32_16x16x32_bf16 v[20:23], a[140:143], v[92:95], v[20:23]\n"
    "v_mfma_f32_16x16x32_bf16 v[24:27], a[144:147], v[84:87], v[24:27]\n"
    "v_mfma_f32_16x16x32_bf16 v[28:31], a[144:147], v[88:91], v[28:31]\n"
    "v_mfma_f32_16x16x32_bf16 v[32:35], a[144:147], v[92:95], v[32:35]\n"
    "v_mfma_f32_16x16x32_bf16 v[36:39], a[148:151], v[84:87], v[36:39]\n"
    "v_mfma_f32_16x16x32_bf16 v[40:43], a[148:151], v[88:91], v[40:43]\n"
    "v_mfma_f32_16x16x32_bf16 v[44:47], a[148:151], v[92:95], v[44:47]\n"
    "v_mfma_f32_16x16x32_bf16 v[48:51], a[152:155], v[84:87], v[48:51]\n"
    "v_mfma_f32_16x16x32_bf16 v[52:55], a[152:155], v[88:91], v[52:55]\n"
    "v_mfma_f32_16x16x32_bf16 v[56:59], a[152:155], v[92:95], v[56:59]\n"
    "s_waitcnt vmcnt(12)\n"
    "v_mfma_f32_16x16x32_bf16 v[0:3], a[156:159], v[96:99], v[0:3]\n"
    "s_add_u32 s91, s52, 24576\n"
    "v_mfma_f32_16x16x32_bf16 v[4:7], a[156:159], v[100:103], v[4:7]\n"
    "buffer_load_dwordx4 v[84:87], v230, s[56:59], s91 offen offset:0 sc1\n"
    "v_mfma_f32_16x16x32_bf16 v[8:11], a[156:159], v[104:107], v[8:11]\n"
    "buffer_load_dwordx4 v[88:91], v230, s[56:59], s91 offen offset:1024 sc1\n"
    "v_mfma_f32_16x16x32_bf16 v[12:15], a[160:163], v[96:99], v[12:15]\n"
    "buffer_load_dwordx4 v[92:95], v230, s[56:59], s91 offen offset:2048 sc1\n"
    "v_mfma_f32_16x16x32_bf16 v[16:19], a[160:163], v[100:103], v[16:19]\n"
    "v_mfma_f32_16x16x32_bf16 v[20:23], a[160:163], v[104:107], v[20:23]\n"
    "v_mfma_f32_16x16x32_bf16 v[24:27], a[164:167], v[96:99], v[24:27]\n"
    "v_mfma_f32_16x16x32_bf16 v[28:31], a[164:167], v[100:103], v[28:31]\n"
    "v_mfma_f32_16x16x32_bf16 v[32:35], a[164:167], v[104:107], v[32:35]\n"
    "v_mfma_f32_16x16x32_bf16 v[36:39], a[168:171], v[96:99], v[36:39]\n"
    "v_mfma_f32_16x16x32_bf16 v[40:43], a[168:171], v[100:103], v[40:43]\n"
    "v_mfma_f32_16x16x32_bf16 v[44:47], a[168:171], v[104:107], v[44:47]\n"
    "v_mfma_f32_16x16x32_bf16 v[48:51], a[172:175], v[96:99], v[48:51]\n"
    "v_mfma_f32_16x16x32_bf16 v[52:55], a[172:175], v[100:103], v[52:55]\n"
    "v_mfma_f32_16x16x32_bf16 v[56:59], a[172:175], v[104:107], v[56:59]\n"
    "s_waitcnt vmcnt(12)\n"
    "v_mfma_f32_16x16x32_bf16 v[0:3], a[176:179], v[108:111], v[0:3]\n"
    "s_add_u32 s91, s53, 24576\n"
    "v_mfma_f32_16x16x32_bf16 v[4:7], a[176:179], v[112:115], v[4:7]\n"
    "buffer_load_dwordx4 v[96:99], v230, s[56:59], s91 offen offset:0 sc1\n"
    "v_mfma_f32_16x16x32_bf16 v[8:11], a[176:179], v[116:119], v[8:11]\n"
    "buffer_load_dwordx4 v[100:103], v230, s[56:59], s91 offen offset:1024 sc1\n"
    "v_mfma_f32_16x16x32_bf16 v[12:15], a[180:183], v[108:111], v[12:15]\n"
    "buffer_load_dwordx4 v[104:107], v230, s[56:59], s91 offen offset:2048 sc1\n"
    "v_mfma_f32_16x16x32_bf16 v[16:19], a[180:183], v[112:115], v[16:19]\n"
    "v_mfma_f32_16x16x32_bf16 v[20:23], a[180:183], v[116:119], v[20:23]\n"
    "v_mfma_f32_16x16x32_bf16 v[24:27], a[184:187], v[108:111], v[24:27]\n"
    "v_mfma_f32_16x16x32_bf16 v[28:31], a[184:187], v[112:115], v[28:31]\n"
    "v_mfma_f32_16x16x32_bf16 v[32:35], a[184:187], v[116:119], v[32:35]\n"
    "v_mfma_f32_16x16x32_bf16 v[36:39], a[188:191], v[108:111], v[36:39]\n"
    "v_mfma_f32_16x16x32_bf16 v[40:43], a[188:191], v[112:115], v[40:43]\n"
    "v_mfma_f32_16x16x32_bf16 v[44:47], a[188:191], v[116:119], v[44:47]\n"
    "v_mfma_f32_16x16x32_bf16 v[48:51], a[192:195], v[108:111], v[48:51]\n"
    "v_mfma_f32_16x16x32_bf16 v[52:55], a[192:195], v[112:115], v[52:55]\n"
    "v_mfma_f32_16x16x32_bf16 v[56:59], a[192:195], v[116:119], v[56:59]\n"
    "s_waitcnt vmcnt(12)\n"
    "v_mfma_f32_16x16x32_bf16 v[0:3], a[196:199], v[120:123], v[0:3]\n"
    "s_add_u32 s91, s54, 24576\n"
    "v_mfma_f32_16x16x32_bf16 v[4:7], a[196:199], v[124:127], v[4:7]\n"
    "buffer_load_dwordx4 v[108:111], v230, s[56:59], s91 offen offset:0 sc1\n"
    "v_mfma_f32_16x16x32_bf16 v[8:11], a[196:199], v[128:131], v[8:11]\n"
    "buffer_load_dwordx4 v[112:115], v230, s[56:59], s91 offen offset:1024 sc1\n"
    "v_mfma_f32_16x16x32_bf16 v[12:15], a[200:203], v[120:123], v[12:15]\n"
    "buffer_load_dwordx4 v[116:119], v230, s[56:59], s91 offen offset:2048 sc1\n"
    "v_mfma_f32_16x16x32_bf16 v[16:19], a[200:203], v[124:127], v[16:19]\n"
    "v_mfma_f32_16x16x32_bf16 v[20:23], a[200:203], v[128:131], v[20:23]\n"
    "v_mfma_f32_16x16x32_bf16 v[24:27], a[204:207], v[120:123], v[24:27]\n"
    "v_mfma_f32_16x16x32_bf16 v[28:31], a[204:207], v[124:127], v[28:31]\n"
    "v_mfma_f32_16x16x32_bf16 v[32:35], a[204:207], v[128:131], v[32:35]\n"
    "v_mfma_f32_16x16x32_bf16 v[36:39], a[208:211], v[120:123], v[36:39]\n"
    "v_mfma_f32_16x16x32_bf16 v[40:43], a[208:211], v[124:127], v[40:43]\n"
    "v_mfma_f32_16x16x32_bf16 v[44:47], a[208:211], v[128:131], v[44:47]\n"
    "v_mfma_f32_16x16x32_bf16 v[48:51], a[212:215], v[120:123], v[48:51]\n"
    "v_mfma_f32_16x16x32_bf16 v[52:55], a[212:215], v[124:127], v[52:55]\n"
    "v_mfma_f32_16x16x32_bf16 v[56:59], a[212:215], v[128:131], v[56:59]\n"
    "s_waitcnt vmcnt(12)\n"
    "v_mfma_f32_16x16x32_bf16 v[0:3], a[216:219], v[60:63], v[0:3]\n"
    "s_add_u32 s91, s55, 24576\n"
    "v_mfma_f32_16x16x32_bf16 v[4:7], a[216:219], v[64:67], v[4:7]\n"
    "buffer_load_dwordx4 v[120:123], v230, s[56:59], s91 offen offset:0 sc1\n"
    "v_mfma_f32_16x16x32_bf16 v[8:11], a[216:219], v[68:71], v[8:11]\n"
    "buffer_load_dwordx4 v[124:127], v230, s[56:59], s91 offen offset:1024 sc1\n"
    "v_mfma_f32_16x16x32_bf16 v[12:15], a[220:223], v[60:63], v[12:15]\n"
    "buffer_load_dwordx4 v[128:131], v230, s[56:59], s91 offen offset:2048 sc1\n"
    "v_mfma_f32_16x16x32_bf16 v[16:19], a[220:223], v[64:67], v[16:19]\n"
    "v_mfma_f32_16x16x32_bf16 v[20:23], a[220:223], v[68:71], v[20:23]\n"
    "v_mfma_f32_16x16x32_bf16 v[24:27], a[224:227], v[60:63], v[24:27]\n"
    "v_mfma_f32_16x16x32_bf16 v[28:31], a[224:227], v[64:67], v[28:31]\n"
    "v_mfma_f32_16x16x32_bf16 v[32:35], a[224:227], v[68:71], v[32:35]\n"
    "v_mfma_f32_16x16x32_bf16 v[36:39], a[228:231], v[60:63], v[36:39]\n"
    "v_mfma_f32_16x16x32_bf16 v[40:43], a[228:231], v[64:67], v[40:43]\n"
    "v_mfma_f32_16x16x32_bf16 v[44:47], a[228:231], v[68:71], v[44:47]\n"
    "v_mfma_f32_16x16x32_bf16 v[48:51], a[232:235], v[60:63], v[48:51]\n"
    "v_mfma_f32_16x16x32_bf16 v[52:55], a[232:235], v[64:67], v[52:55]\n"
    "v_mfma_f32_16x16x32_bf16 v[56:59], a[232:235], v[68:71], v[56:59]\n"
    "s_waitcnt vmcnt(12)\n"
    "v_mfma_f32_16x16x32_bf16 v[0:3], a[236:239], v[72:75], v[0:3]\n"
    "s_add_u32 s91, s52, 36864\n"
    "v_mfma_f32_16x16x32_bf16 v[4:7], a[236:239], v[76:79], v[4:7]\n"
    "buffer_load_dwordx4 v[60:63], v230, s[56:59], s91 offen offset:0 sc1\n"
    "v_mfma_f32_16x16x32_bf16 v[8:11], a[236:239], v[80:83], v[8:11]\n"
    "buffer_load_dwordx4 v[64:67], v230, s[56:59], s91 offen offset:1024 sc1\n"
    "v_mfma_f32_16x16x32_bf16 v[12:15], a[240:243], v[72:75], v[12:15]\n"
    "buffer_load_dwordx4 v[68:71], v230, s[56:59], s91 offen offset:2048 sc1\n"
    "v_mfma_f32_16x16x32_bf16 v[16:19], a[240:243], v[76:79], v[16:19]\n"
    "v_mfma_f32_16x16x32_bf16 v[20:23], a[240:243], v[80:83], v[20:23]\n"
    "v_mfma_f32_16x16x32_bf16 v[24:27], a[244:247], v[72:75], v[24:27]\n"
    "v_mfma_f32_16x16x32_bf16 v[28:31], a[244:247], v[76:79], v[28:31]\n"
    "v_mfma_f32_16x16x32_bf16 v[32:35], a[244:247], v[80:83], v[32:35]\n"
    "v_mfma_f32_16x16x32_bf16 v[36:39], a[248:251], v[72:75], v[36:39]\n"
    "v_mfma_f32_16x16x32_bf16 v[40:43], a[248:251], v[76:79], v[40:43]\n"
    "v_mfma_f32_16x16x32_bf16 v[44:47], a[248:251], v[80:83], v[44:47]\n"
    "v_mfma_f32_16x16x32_bf16 v[48:51], a[252:255], v[72:75], v[48:51]\n"
    "v_mfma_f32_16x16x32_bf16 v[52:55], a[252:255], v[76:79], v[52:55]\n"
    "v_mfma_f32_16x16x32_bf16 v[56:59], a[252:255], v[80:83], v[56:59]\n"
    "s_waitcnt vmcnt(12)\n"
    "s_waitcnt lgkmcnt(0)\n"
    "v_mfma_f32_16x16x32_bf16 v[0:3], v[132:135], v[84:87], v[0:3]\n"
    "s_add_u32 s91, s53, 36864\n"
    "v_mfma_f32_16x16x32_bf16 v[4:7], v[132:135], v[88:91], v[4:7]\n"
    "buffer_load_dwordx4 v[72:75], v230, s[56:59], s91 offen offset:0 sc1\n"
    "v_mfma_f32_16x16x32_bf16 v[8:11], v[132:135], v[92:95], v[8:11]\n"
    "buffer_load_dwordx4 v[76:79], v230, s[56:59], s91 offen offset:1024 sc1\n"
    "v_mfma_f32_16x16x32_bf16 v[12:15], v[136:139], v[84:87], v[12:15]\n"
    "buffer_load_dwordx4 v[80:83], v230, s[56:59], s91 offen offset:2048 sc1\n"
    "v_mfma_f32_16x16x32_bf16 v[16:19], v[136:139], v[88:91], v[16:19]\n"
    "v_mfma_f32_16x16x32_bf16 v[20:23], v[136:139], v[92:95], v[20:23]\n"
    "v_mfma_f32_16x16x32_bf16 v[24:27], v[140:143], v[84:87], v[24:27]\n"
    "v_mfma_f32_16x16x32_bf16 v[28:31], v[140:143], v[88:91], v[28:31]\n"
    "v_mfma_f32_16x16x32_bf16 v[32:35], v[140:143], v[92:95], v[32:35]\n"
    "v_mfma_f32_16x16x32_bf16 v[36:39], v[144:147], v[84:87], v[36:39]\n"
    "v_mfma_f32_16x16x32_bf16 v[40:43], v[144:147], v[88:91], v[40:43]\n"
    "v_mfma_f32_16x16x32_bf16 v[44:47], v[144:147], v[92:95], v[44:47]\n"
    "v_mfma_f32_16x16x32_bf16 v[48:51], v[148:151], v[84:87], v[48:51]\n"
    "v_mfma_f32_16x16x32_bf16 v[52:55], v[148:151], v[88:91], v[52:55]\n"
    "v_mfma_f32_16x16x32_bf16 v[56:59], v[148:151], v[92:95], v[56:59]\n"
    "s_waitcnt vmcnt(12)\n"
    "v_mfma_f32_16x16x32_bf16 v[0:3], v[152:155], v[96:99], v[0:3]\n"
    "s_add_u32 s91, s54, 36864\n"
    "v_mfma_f32_16x16x32_bf16 v[4:7], v[152:155], v[100:103], v[4:7]\n"
    "buffer_load_dwordx4 v[84:87], v230, s[56:59], s91 offen offset:0 sc1\n"
    "v_mfma_f32_16x16x32_bf16 v[8:11], v[152:155], v[104:107], v[8:11]\n"
    "buffer_load_dwordx4 v[88:91], v230, s[56:59], s91 offen offset:1024 sc1\n"
    "v_mfma_f32_16x16x32_bf16 v[12:15], v[156:159], v[96:99], v[12:15]\n"
    "buffer_load_dwordx4 v[92:95], v230, s[56:59], s91 offen offset:2048 sc1\n"
    "v_mfma_f32_16x16x32_bf16 v[16:19], v[156:159], v[100:103], v[16:19]\n"
    "v_mfma_f32_16x16x32_bf16 v[20:23], v[156:159], v[104:107], v[20:23]\n"
    "v_mfma_f32_16x16x32_bf16 v[24:27], v[160:163], v[96:99], v[24:27]\n"
    "v_mfma_f32_16x16x32_bf16 v[28:31], v[160:163], v[100:103], v[28:31]\n"
    "v_mfma_f32_16x16x32_bf16 v[32:35], v[160:163], v[104:107], v[32:35]\n"
    "v_mfma_f32_16x16x32_bf16 v[36:39], v[164:167], v[96:99], v[36:39]\n"
    "v_mfma_f32_16x16x32_bf16 v[40:43], v[164:167], v[100:103], v[40:43]\n"
    "v_mfma_f32_16x16x32_bf16 v[44:47], v[164:167], v[104:107], v[44:47]\n"
    "v_mfma_f32_16x16x32_bf16 v[48:51], v[168:171], v[96:99], v[48:51]\n"
    "v_mfma_f32_16x16x32_bf16 v[52:55], v[168:171], v[100:103], v[52:55]\n"
    "v_mfma_f32_16x16x32_bf16 v[56:59], v[168:171], v[104:107], v[56:59]\n"
    "s_waitcnt vmcnt(12)\n"
    "v_mfma_f32_16x16x32_bf16 v[0:3], v[172:175], v[108:111], v[0:3]\n"
    "s_add_u32 s91, s55, 36864\n"
    "v_mfma_f32_16x16x32_bf16 v[4:7], v[172:175], v[112:115], v[4:7]\n"
    "buffer_load_dwordx4 v[96:99], v230, s[56:59], s91 offen offset:0 sc1\n"
    "v_mfma_f32_16x16x32_bf16 v[8:11], v[172:175], v[116:119], v[8:11]\n"
    "buffer_load_dwordx4 v[100:103], v230, s[56:59], s91 offen offset:1024 sc1\n"
    "v_mfma_f32_16x16x32_bf16 v[12:15], v[176:179], v[108:111], v[12:15]\n"
    "buffer_load_dwordx4 v[104:107], v230, s[56:59], s91 offen offset:2048 sc1\n"
    "v_mfma_f32_16x16x32_bf16 v[16:19], v[176:179], v[112:115], v[16:19]\n"
    "v_mfma_f32_16x16x32_bf16 v[20:23], v[176:179], v[116:119], v[20:23]\n"
    "v_mfma_f32_16x16x32_bf16 v[24:27], v[180:183], v[108:111], v[24:27]\n"
    "v_mfma_f32_16x16x32_bf16 v[28:31], v[180:183], v[112:115], v[28:31]\n"
    "v_mfma_f32_16x16x32_bf16 v[32:35], v[180:183], v[116:119], v[32:35]\n"
    "v_mfma_f32_16x16x32_bf16 v[36:39], v[184:187], v[108:111], v[36:39]\n"
    "v_mfma_f32_16x16x32_bf16 v[40:43], v[184:187], v[112:115], v[40:43]\n"
    "v_mfma_f32_16x16x32_bf16 v[44:47], v[184:187], v[116:119], v[44:47]\n"
    "v_mfma_f32_16x16x32_bf16 v[48:51], v[188:191], v[108:111], v[48:51]\n"
    "v_mfma_f32_16x16x32_bf16 v[52:55], v[188:191], v[112:115], v[52:55]\n"
    "v_mfma_f32_16x16x32_bf16 v[56:59], v[188:191], v[116:119], v[56:59]\n"
    "s_waitcnt vmcnt(12)\n"
    "v_mfma_f32_16x16x32_bf16 v[0:3], a[0:3], v[120:123], v[0:3]\n"
    "s_add_u32 s52, s52, s46\n"
    "s_add_u32 s53, s53, s46\n"
    "v_mfma_f32_16x16x32_bf16 v[4:7], a[0:3], v[124:127], v[4:7]\n"
    "s_add_u32 s54, s54, s46\n"
    "s_add_u32 s55, s55, s46\n"
    "v_mfma_f32_16x16x32_bf16 v[8:11], a[0:3], v[128:131], v[8:11]\n"
    "s_add_u32 s91, s52, 0\n"
    "v_mfma_f32_16x16x32_bf16 v[12:15], a[4:7], v[120:123], v[12:15]\n"
    "buffer_load_dwordx4 v[108:111], v230, s[56:59], s91 offen offset:0 sc1\n"
    "buffer_load_dwordx4 v[112:115], v230, s[56:59], s91 offen offset:1024 sc1\n"
    "v_mfma_f32_16x16x32_bf16 v[16:19], a[4:7], v[124:127], v[16:19]\n"
    "buffer_load_dwordx4 v[116:119], v230, s[56:59], s91 offen offset:2048 sc1\n"
    "v_mfma_f32_16x16x32_bf16 v[20:23], a[4:7], v[128:131], v[20:23]\n"
    "v_mfma_f32_16x16x32_bf16 v[24:27], a[8:11], v[120:123], v[24:27]\n"
    "v_mfma_f32_16x16x32_bf16 v[28:31], a[8:11], v[124:127], v[28:31]\n"
    "v_mfma_f32_16x16x32_bf16 v[32:35], a[8:11], v[128:131], v[32:35]\n"
    "v_mfma_f32_16x16x32_bf16 v[36:39], a[12:15], v[120:123], v[36:39]\n"
    "v_mfma_f32_16x16x32_bf16 v[40:43], a[12:15], v[124:127], v[40:43]\n"
    "v_mfma_f32_16x16x32_bf16 v[44:47], a[12:15], v[128:131], v[44:47]\n"
    "v_mfma_f32_16x16x32_bf16 v[48:51], a[16:19], v[120:123], v[48:51]\n"
    "v_mfma_f32_16x16x32_bf16 v[52:55], a[16:19], v[124:127], v[52:55]\n"
    "v_mfma_f32_16x16x32_bf16 v[56:59], a[16:19], v[128:131], v[56:59]\n"
    "s_waitcnt vmcnt(12)\n"
    "v_mfma_f32_16x16x32_bf16 v[0:3], a[20:23], v[60:63], v[0:3]\n"
    "s_add_u32 s91, s53, 0\n"
    "v_mfma_f32_16x16x32_bf16 v[4:7], a[20:23], v[64:67], v[4:7]\n"
    "buffer_load_dwordx4 v[120:123], v230, s[56:59], s91 offen offset:0 sc1\n"
    "v_mfma_f32_16x16x32_bf16 v[8:11], a[20:23], v[68:71], v[8:11]\n"
    "buffer_load_dwordx4 v[124:127], v230, s[56:59], s91 offen offset:1024 sc1\n"
    "v_mfma_f32_16x16x32_bf16 v[12:15], a[24:27], v[60:63], v[12:15]\n"
    "buffer_load_dwordx4 v[128:131], v230, s[56:59], s91 offen offset:2048 sc1\n"
    "v_mfma_f32_16x16x32_bf16 v[16:19], a[24:27], v[64:67], v[16:19]\n"
    "v_mfma_f32_16x16x32_bf16 v[20:23], a[24:27], v[68:71], v[20:23]\n"
    "v_mfma_f32_16x16x32_bf16 v[24:27], a[28:31], v[60:63], v[24:27]\n"
    "v_mfma_f32_16x16x32_bf16 v[28:31], a[28:31], v[64:67], v[28:31]\n"
    "v_mfma_f32_16x16x32_bf16 v[32:35], a[28:31], v[68:71], v[32:35]\n"
    "v_mfma_f32_16x16x32_bf16 v[36:39], a[32:35], v[60:63], v[36:39]\n"
    "v_mfma_f32_16x16x32_bf16 v[40:43], a[32:35], v[64:67], v[40:43]\n"
    "v_mfma_f32_16x16x32_bf16 v[44:47], a[32:35], v[68:71], v[44:47]\n"
    "v_mfma_f32_16x16x32_bf16 v[48:51], a[36:39], v[60:63], v[48:51]\n"
    "v_mfma_f32_16x16x32_bf16 v[52:55], a[36:39], v[64:67], v[52:55]\n"
    "v_mfma_f32_16x16x32_bf16 v[56:59], a[36:39], v[68:71], v[56:59]\n"
    "s_waitcnt vmcnt(12)\n"
    "v_mfma_f32_16x16x32_bf16 v[0:3], a[40:43], v[72:75], v[0:3]\n"
    "s_add_u32 s91, s54, 0\n"
    "v_mfma_f32_16x16x32_bf16 v[4:7], a[40:43], v[76:79], v[4:7]\n"
    "buffer_load_dwordx4 v[60:63], v230, s[56:59], s91 offen offset:0 sc1\n"
    "v_mfma_f32_16x16x32_bf16 v[8:11], a[40:43], v[80:83], v[8:11]\n"
    "buffer_load_dwordx4 v[64:67], v230, s[56:59], s91 offen offset:1024 sc1\n"
    "v_mfma_f32_16x16x32_bf16 v[12:15], a[44:47], v[72:75], v[12:15]\n"
    "buffer_load_dwordx4 v[68:71], v230, s[56:59], s91 offen offset:2048 sc1\n"
    "v_mfma_f32_16x16x32_bf16 v[16:19], a[44:47], v[76:79], v[16:19]\n"
    "v_mfma_f32_16x16x32_bf16 v[20:23], a[44:47], v[80:83], v[20:23]\n"
    "v_mfma_f32_16x16x32_bf16 v[24:27], a[48:51], v[72:75], v[24:27]\n"
    "v_mfma_f32_16x16x32_bf16 v[28:31], a[48:51], v[76:79], v[28:31]\n"
    "v_mfma_f32_16x16x32_bf16 v[32:35], a[48:51], v[80:83], v[32:35]\n"
    "v_mfma_f32_16x16x32_bf16 v[36:39], a[52:55], v[72:75], v[36:39]\n"
    "v_mfma_f32_16x16x32_bf16 v[40:43], a[52:55], v[76:79], v[40:43]\n"
    "v_mfma_f32_16x16x32_bf16 v[44:47], a[52:55], v[80:83], v[44:47]\n"
    "v_mfma_f32_16x16x32_bf16 v[48:51], a[56:59], v[72:75], v[48:51]\n"
    "v_mfma_f32_16x16x32_bf16 v[52:55], a[56:59], v[76:79], v[52:55]\n"
    "v_mfma_f32_16x16x32_bf16 v[56:59], a[56:59], v[80:83], v[56:59]\n"
    "s_waitcnt vmcnt(12)\n"
    "v_mfma_f32_16x16x32_bf16 v[0:3], a[60:63], v[84:87], v[0:3]\n"
    "s_add_u32 s91, s55, 0\n"
    "v_mfma_f32_16x16x32_bf16 v[4:7], a[60:63], v[88:91], v[4:7]\n"
    "buffer_load_dwordx4 v[72:75], v230, s[56:59], s91 offen offset:0 sc1\n"
    "v_mfma_f32_16x16x32_bf16 v[8:11], a[60:63], v[92:95], v[8:11]\n"
    "buffer_load_dwordx4 v[76:79], v230, s[56:59], s91 offen offset:1024 sc1\n"
    "v_mfma_f32_16x16x32_bf16 v[12:15], a[64:67], v[84:87], v[12:15]\n"
    "buffer_load_dwordx4 v[80:83], v230, s[56:59], s91 offen offset:2048 sc1\n"
    "v_mfma_f32_16x16x32_bf16 v[16:19], a[64:67], v[88:91], v[16:19]\n"
    "v_mfma_f32_16x16x32_bf16 v[20:23], a[64:67], v[92:95], v[20:23]\n"
    "v_mfma_f32_16x16x32_bf16 v[24:27], a[68:71], v[84:87], v[24:27]\n"
    "v_mfma_f32_16x16x32_bf16 v[28:31], a[68:71], v[88:91], v[28:31]\n"
    "v_mfma_f32_16x16x32_bf16 v[32:35], a[68:71], v[92:95], v[32:35]\n"
    "v_mfma_f32_16x16x32_bf16 v[36:39], a[72:75], v[84:87], v[36:39]\n"
    "v_mfma_f32_16x16x32_bf16 v[40:43], a[72:75], v[88:91], v[40:43]\n"
    "v_mfma_f32_16x16x32_bf16 v[44:47], a[72:75], v[92:95], v[44:47]\n"
    "v_mfma_f32_16x16x32_bf16 v[48:51], a[76:79], v[84:87], v[48:51]\n"
    "v_mfma_f32_16x16x32_bf16 v[52:55], a[76:79], v[88:91], v[52:55]\n"
    "v_mfma_f32_16x16x32_bf16 v[56:59], a[76:79], v[92:95], v[56:59]\n"
    "s_waitcnt vmcnt(12)\n"
    "v_mfma_f32_16x16x32_bf16 v[0:3], a[80:83], v[96:99], v[0:3]\n"
    "s_add_u32 s91, s52, 12288\n"
    "v_mfma_f32_16x16x32_bf16 v[4:7], a[80:83], v[100:103], v[4:7]\n"
    "buffer_load_dwordx4 v[84:87], v230, s[56:59], s91 offen offset:0 sc1\n"
    "v_mfma_f32_16x16x32_bf16 v[8:11], a[80:83], v[104:107], v[8:11]\n"
    "buffer_load_dwordx4 v[88:91], v230, s[56:59], s91 offen offset:1024 sc1\n"
    "v_mfma_f32_16x16x32_bf16 v[12:15], a[84:87], v[96:99], v[12:15]\n"
    "buffer_load_dwordx4 v[92:95], v230, s[56:59], s91 offen offset:2048 sc1\n"
    "v_mfma_f32_16x16x32_bf16 v[16:19], a[84:87], v[100:103], v[16:19]\n"
    "v_mfma_f32_16x16x32_bf16 v[20:23], a[84:87], v[104:107], v[20:23]\n"
    "v_mfma_f32_16x16x32_bf16 v[24:27], a[88:91], v[96:99], v[24:27]\n"
    "v_mfma_f32_16x16x32_bf16 v[28:31], a[88:91], v[100:103], v[28:31]\n"
    "v_mfma_f32_16x16x32_bf16 v[32:35], a[88:91], v[104:107], v[32:35]\n"
    "v_mfma_f32_16x16x32_bf16 v[36:39], a[92:95], v[96:99], v[36:39]\n"
    "v_mfma_f32_16x16x32_bf16 v[40:43], a[92:95], v[100:103], v[40:43]\n"
    "v_mfma_f32_16x16x32_bf16 v[44:47], a[92:95], v[104:107], v[44:47]\n"
    "v_mfma_f32_16x16x32_bf16 v[48:51], v[192:195], v[96:99], v[48:51]\n"
    "v_mfma_f32_16x16x32_bf16 v[52:55], v[192:195], v[100:103], v[52:55]\n"
    "v_mfma_f32_16x16x32_bf16 v[56:59], v[192:195], v[104:107], v[56:59]\n"
    "ds_write_b128 v232, v[0:3] offset:0\n"
    "ds_write_b128 v232, v[4:7] offset:1024\n"
    "s_waitcnt vmcnt(12)\n"
    "v_mfma_f32_16x16x32_bf16 v[0:3], a[96:99], v[108:111], 0\n"
    "ds_write_b128 v232, v[8:11] offset:2048\n"
    "s_add_u32 s91, s53, 12288\n"
    "v_mfma_f32_16x16x32_bf16 v[4:7], a[96:99], v[112:115], 0\n"
    "ds_write_b128 v232, v[12:15] offset:3072\n"
    "buffer_load_dwordx4 v[96:99], v230, s[56:59], s91 offen offset:0 sc1\n"
    "v_mfma_f32_16x16x32_bf16 v[8:11], a[96:99], v[116:119], 0\n"
    "ds_write_b128 v232, v[16:19] offset:4096\n"
    "buffer_load_dwordx4 v[100:103], v230, s[56:59], s91 offen offset:1024 sc1\n"
    "v_mfma_f32_16x16x32_bf16 v[12:15], a[100:103], v[108:111], 0\n"
    "ds_write_b128 v232, v[20:23] offset:5120\n"
    "buffer_load_dwordx4 v[104:107], v230, s[56:59], s91 offen offset:2048 sc1\n"
    "v_mfma_f32_16x16x32_bf16 v[16:19], a[100:103], v[112:115], 0\n"
    "ds_write_b128 v232, v[24:27] offset:6144\n"
    "v_mfma_f32_16x16x32_bf16 v[20:23], a[100:103], v[116:119], 0\n"
    "ds_write_b128 v232, v[28:31] offset:7168\n"
    "v_mfma_f32_16x16x32_bf16 v[24:27], a[104:107], v[108:111], 0\n"
    "ds_write_b128 v232, v[32:35] offset:8192\n"
    "v_mfma_f32_16x16x32_bf16 v[28:31], a[104:107], v[112:115], 0\n"
    "ds_write_b128 v232, v[36:39] offset:9216\n"
    "v_mfma_f32_16x16x32_bf16 v[32:35], a[104:107], v[116:119], 0\n"
    "ds_write_b128 v232, v[40:43] offset:10240\n"
    "v_mfma_f32_16x16x32_bf16 v[36:39], a[108:111], v[108:111], 0\n"
    "ds_write_b128 v232, v[44:47] offset:11264\n"
    "v_mfma_f32_16x16x32_bf16 v[40:43], a[108:111], v[112:115], 0\n"
    "ds_write_b128 v232, v[48:51] offset:12288\n"
    "v_mfma_f32_16x16x32_bf16 v[44:47], a[108:111], v[116:119], 0\n"
    "ds_write_b128 v232, v[52:55] offset:13312\n"
    "v_mfma_f32_16x16x32_bf16 v[48:51], a[112:115], v[108:111], 0\n"
    "ds_write_b128 v232, v[56:59] offset:14336\n"
    "v_mfma_f32_16x16x32_bf16 v[52:55], a[112:115], v[112:115], 0\n"
    "s_nop 0\n"
    "v_mfma_f32_16x16x32_bf16 v[56:59], a[112:115], v[116:119], 0\n"
    "s_waitcnt lgkmcnt(0)\n"
    "s_barrier\n"
    "s_waitcnt vmcnt(12)\n"
    "v_mfma_f32_16x16x32_bf16 v[0:3], a[116:119], v[120:123], v[0:3]\n"
    "s_add_u32 s91, s54, 12288\n"
    "v_mfma_f32_16x16x32_bf16 v[4:7], a[116:119], v[124:127], v[4:7]\n"
    "ds_read_b128 v[196:199], v233 offset:0\n"
    "buffer_load_dwordx4 v[108:111], v230, s[56:59], s91 offen offset:0 sc1\n"
    "v_mfma_f32_16x16x32_bf16 v[8:11], a[116:119], v[128:131], v[8:11]\n"
    "ds_read_b128 v[200:203], v233 offset:16384\n"
    "buffer_load_dwordx4 v[112:115], v230, s[56:59], s91 offen offset:1024 sc1\n"
    "v_mfma_f32_16x16x32_bf16 v[12:15], a[120:123], v[120:123], v[12:15]\n"
    "ds_read_b128 v[204:207], v233 offset:32768\n"
    "buffer_load_dwordx4 v[116:119], v230, s[56:59], s91 offen offset:2048 sc1\n"
    "v_mfma_f32_16x16x32_bf16 v[16:19], a[120:123], v[124:127], v[16:19]\n"
    "ds_read_b128 v[208:211], v233 offset:49152\n"
    "v_mfma_f32_16x16x32_bf16 v[20:23], a[120:123], v[128:131], v[20:23]\n"
    "v_mfma_f32_16x16x32_bf16 v[24:27], a[124:127], v[120:123], v[24:27]\n"
    "v_mfma_f32_16x16x32_bf16 v[28:31], a[124:127], v[124:127], v[28:31]\n"
    "v_mfma_f32_16x16x32_bf16 v[32:35], a[124:127], v[128:131], v[32:35]\n"
    "v_mfma_f32_16x16x32_bf16 v[36:39], a[128:131], v[120:123], v[36:39]\n"
    "v_mfma_f32_16x16x32_bf16 v[40:43], a[128:131], v[124:127], v[40:43]\n"
    "v_mfma_f32_16x16x32_bf16 v[44:47], a[128:131], v[128:131], v[44:47]\n"
    "v_mfma_f32_16x16x32_bf16 v[48:51], a[132:135], v[120:123], v[48:51]\n"
    "v_mfma_f32_16x16x32_bf16 v[52:55], a[132:135], v[124:127], v[52:55]\n"
    "v_mfma_f32_16x16x32_bf16 v[56:59], a[132:135], v[128:131], v[56:59]\n"
    "s_waitcnt vmcnt(12)\n"
    "v_mfma_f32_16x16x32_bf16 v[0:3], a[136:139], v[60:63], v[0:3]\n"
    "s_add_u32 s91, s55, 12288\n"
    "v_mfma_f32_16x16x32_bf16 v[4:7], a[136:139], v[64:67], v[4:7]\n"
    "ds_read_b128 v[212:215], v233 offset:4096\n"
    "buffer_load_dwordx4 v[120:123], v230, s[56:59], s91 offen offset:0 sc1\n"
    "v_mfma_f32_16x16x32_bf16 v[8:11], a[136:139], v[68:71], v[8:11]\n"
    "ds_read_b128 v[216:219], v233 offset:20480\n"
    "buffer_load_dwordx4 v[124:127], v230, s[56:59], s91 offen offset:1024 sc1\n"
    "v_mfma_f32_16x16x32_bf16 v[12:15], a[140:143], v[60:63], v[12:15]\n"
    "ds_read_b128 v[220:223], v233 offset:36864\n"
    "buffer_load_dwordx4 v[128:131], v230, s[56:59], s91 offen offset:2048 sc1\n"
    "v_mfma_f32_16x16x32_bf16 v[16:19], a[140:143], v[64:67], v[16:19]\n"
    "ds_read_b128 v[224:227], v233 offset:53248\n"
    "v_mfma_f32_16x16x32_bf16 v[20:23], a[140:143], v[68:71], v[20:23]\n"
    "v_mfma_f32_16x16x32_bf16 v[24:27], a[144:147], v[60:63], v[24:27]\n"
    "s_waitcnt lgkmcnt(4)\n"
    "v_pk_add_f32 v[196:197], v[196:197], v[200:201]\n"
    "v_mfma_f32_16x16x32_bf16 v[28:31], a[144:147], v[64:67], v[28:31]\n"
    "v_pk_add_f32 v[204:205], v[204:205], v[208:209]\n"
    "v_mfma_f32_16x16x32_bf16 v[32:35], a[144:147], v[68:71], v[32:35]\n"
    "v_pk_add_f32 v[198:199], v[198:199], v[202:203]\n"
    "v_mfma_f32_16x16x32_bf16 v[36:39], a[148:151], v[60:63], v[36:39]\n"
    "v_pk_add_f32 v[206:207], v[206:207], v[210:211]\n"
    "v_mfma_f32_16x16x32_bf16 v[40:43], a[148:151], v[64:67], v[40:43]\n"
    "v_mfma_f32_16x16x32_bf16 v[44:47], a[148:151], v[68:71], v[44:47]\n"
    "v_pk_add_f32 v[196:197], v[196:197], v[204:205]\n"
    "v_mfma_f32_16x16x32_bf16 v[48:51], a[152:155], v[60:63], v[48:51]\n"
    "v_pk_add_f32 v[198:199], v[198:199], v[206:207]\n"
    "v_mfma_f32_16x16x32_bf16 v[52:55], a[152:155], v[64:67], v[52:55]\n"
    "s_add_u32 s92, s47, 0\n"
    "buffer_store_dwordx4 v[196:199], v230, s[80:83], s92 offen offset:0 sc1\n"
    "v_mfma_f32_16x16x32_bf16 v[56:59], a[152:155], v[68:71], v[56:59]\n"
    "s_waitcnt vmcnt(13)\n"
    "v_mfma_f32_16x16x32_bf16 v[0:3], a[156:159], v[72:75], v[0:3]\n"
    "s_add_u32 s91, s52, 24576\n"
    "v_mfma_f32_16x16x32_bf16 v[4:7], a[156:159], v[76:79], v[4:7]\n"
    "ds_read_b128 v[196:199], v233 offset:8192\n"
    "buffer_load_dwordx4 v[60:63], v230, s[56:59], s91 offen offset:0 sc1\n"
    "v_mfma_f32_16x16x32_bf16 v[8:11], a[156:159], v[80:83], v[8:11]\n"
    "ds_read_b128 v[200:203], v233 offset:24576\n"
    "buffer_load_dwordx4 v[64:67], v230, s[56:59], s91 offen offset:1024 sc1\n"
    "v_mfma_f32_16x16x32_bf16 v[12:15], a[160:163], v[72:75], v[12:15]\n"
    "ds_read_b128 v[204:207], v233 offset:40960\n"
    "buffer_load_dwordx4 v[68:71], v230, s[56:59], s91 offen offset:2048 sc1\n"
    "v_mfma_f32_16x16x32_bf16 v[16:19], a[160:163], v[76:79], v[16:19]\n"
    "ds_read_b128 v[208:211], v233 offset:57344\n"
    "v_mfma_f32_16x16x32_bf16 v[20:23], a[160:163], v[80:83], v[20:23]\n"
    "v_mfma_f32_16x16x32_bf16 v[24:27], a[164:167], v[72:75], v[24:27]\n"
    "s_waitcnt lgkmcnt(4)\n"
    "v_pk_add_f32 v[212:213], v[212:213], v[216:217]\n"
    "v_mfma_f32_16x16x32_bf16 v[28:31], a[164:167], v[76:79], v[28:31]\n"
    "v_pk_add_f32 v[220:221], v[220:221], v[224:225]\n"
    "v_mfma_f32_16x16x32_bf16 v[32:35], a[164:167], v[80:83], v[32:35]\n"
    "v_pk_add_f32 v[214:215], v[214:215], v[218:219]\n"
    "v_mfma_f32_16x16x32_bf16 v[36:39], a[168:171], v[72:75], v[36:39]\n"
    "v_pk_add_f32 v[222:223], v[222:223], v[226:227]\n"
    "v_mfma_f32_16x16x32_bf16 v[40:43], a[168:171], v[76:79], v[40:43]\n"
    "v_mfma_f32_16x16x32_bf16 v[44:47], a[168:171], v[80:83], v[44:47]\n"
    "v_pk_add_f32 v[212:213], v[212:213], v[220:221]\n"
    "v_mfma_f32_16x16x32_bf16 v[48:51], a[172:175], v[72:75], v[48:51]\n"
    "v_pk_add_f32 v[214:215], v[214:215], v[222:223]\n"
    "v_mfma_f32_16x16x32_bf16 v[52:55], a[172:175], v[76:79], v[52:55]\n"
    "s_add_u32 s92, s47, 4096\n"
    "buffer_store_dwordx4 v[212:215], v230, s[80:83], s92 offen offset:0 sc1\n"
    "v_mfma_f32_16x16x32_bf16 v[56:59], a[172:175], v[80:83], v[56:59]\n"
    "s_waitcnt vmcnt(14)\n"
    "v_mfma_f32_16x16x32_bf16 v[0:3], a[176:179], v[84:87], v[0:3]\n"
    "s_add_u32 s91, s53, 24576\n"
    "v_mfma_f32_16x16x32_bf16 v[4:7], a[176:179], v[88:91], v[4:7]\n"
    "ds_read_b128 v[212:215], v233 offset:12288\n"
    "buffer_load_dwordx4 v[72:75], v230, s[56:59], s91 offen offset:0 sc1\n"
    "v_mfma_f32_16x16x32_bf16 v[8:11], a[176:179], v[92:95], v[8:11]\n"
    "ds_read_b128 v[216:219], v233 offset:28672\n"
    "buffer_load_dwordx4 v[76:79], v230, s[56:59], s91 offen offset:1024 sc1\n"
    "v_mfma_f32_16x16x32_bf16 v[12:15], a[180:183], v[84:87], v[12:15]\n"
    "ds_read_b128 v[220:223], v233 offset:45056\n"
    "buffer_load_dwordx4 v[80:83], v230, s[56:59], s91 offen offset:2048 sc1\n"
    "v_mfma_f32_16x16x32_bf16 v[16:19], a[180:183], v[88:91], v[16:19]\n"
    "ds_read_b128 v[224:227], v233 offset:61440\n"
    "v_mfma_f32_16x16x32_bf16 v[20:23], a[180:183], v[92:95], v[20:23]\n"
    "v_mfma_f32_16x16x32_bf16 v[24:27], a[184:187], v[84:87], v[24:27]\n"
    "s_waitcnt lgkmcnt(4)\n"
    "v_pk_add_f32 v[196:197], v[196:197], v[200:201]\n"
    "v_mfma_f32_16x16x32_bf16 v[28:31], a[184:187], v[88:91], v[28:31]\n"
    "v_pk_add_f32 v[204:205], v[204:205], v[208:209]\n"
    "v_mfma_f32_16x16x32_bf16 v[32:35], a[184:187], v[92:95], v[32:35]\n"
    "v_pk_add_f32 v[198:199], v[198:199], v[202:203]\n"
    "v_mfma_f32_16x16x32_bf16 v[36:39], a[188:191], v[84:87], v[36:39]\n"
    "v_pk_add_f32 v[206:207], v[206:207], v[210:211]\n"
    "v_mfma_f32_16x16x32_bf16 v[40:43], a[188:191], v[88:91], v[40:43]\n"
    "v_mfma_f32_16x16x32_bf16 v[44:47], a[188:191], v[92:95], v[44:47]\n"
    "v_pk_add_f32 v[196:197], v[196:197], v[204:205]\n"
    "v_mfma_f32_16x16x32_bf16 v[48:51], a[192:195], v[84:87], v[48:51]\n"
    "v_pk_add_f32 v[198:199], v[198:199], v[206:207]\n"
    "v_mfma_f32_16x16x32_bf16 v[52:55], a[192:195], v[88:91], v[52:55]\n"
    "s_add_u32 s92, s47, 8192\n"
    "buffer_store_dwordx4 v[196:199], v230, s[80:83], s92 offen offset:0 sc1\n"
    "v_mfma_f32_16x16x32_bf16 v[56:59], a[192:195], v[92:95], v[56:59]\n"
    "s_waitcnt vmcnt(15)\n"
    "v_mfma_f32_16x16x32_bf16 v[0:3], a[196:199], v[96:99], v[0:3]\n"
    "s_add_u32 s91, s54, 24576\n"
    "v_mfma_f32_16x16x32_bf16 v[4:7], a[196:199], v[100:103], v[4:7]\n"
    "buffer_load_dwordx4 v[84:87], v230, s[56:59], s91 offen offset:0 sc1\n"
    "v_mfma_f32_16x16x32_bf16 v[8:11], a[196:199], v[104:107], v[8:11]\n"
    "buffer_load_dwordx4 v[88:91], v230, s[56:59], s91 offen offset:1024 sc1\n"
    "v_mfma_f32_16x16x32_bf16 v[12:15], a[200:203], v[96:99], v[12:15]\n"
    "buffer_load_dwordx4 v[92:95], v230, s[56:59], s91 offen offset:2048 sc1\n"
    "v_mfma_f32_16x16x32_bf16 v[16:19], a[200:203], v[100:103], v[16:19]\n"
    "v_mfma_f32_16x16x32_bf16 v[20:23], a[200:203], v[104:107], v[20:23]\n"
    "v_mfma_f32_16x16x32_bf16 v[24:27], a[204:207], v[96:99], v[24:27]\n"
    "s_waitcnt lgkmcnt(0)\n"
    "v_pk_add_f32 v[212:213], v[212:213], v[216:217]\n"
    "v_mfma_f32_16x16x32_bf16 v[28:31], a[204:207], v[100:103], v[28:31]\n"
    "v_pk_add_f32 v[220:221], v[220:221], v[224:225]\n"
    "v_mfma_f32_16x16x32_bf16 v[32:35], a[204:207], v[104:107], v[32:35]\n"
    "v_pk_add_f32 v[214:215], v[214:215], v[218:219]\n"
    "v_mfma_f32_16x16x32_bf16 v[36:39], a[208:211], v[96:99], v[36:39]\n"
    "v_pk_add_f32 v[222:223], v[222:223], v[226:227]\n"
    "v_mfma_f32_16x16x32_bf16 v[40:43], a[208:211], v[100:103], v[40:43]\n"
    "v_mfma_f32_16x16x32_bf16 v[44:47], a[208:211], v[104:107], v[44:47]\n"
    "v_pk_add_f32 v[212:213], v[212:213], v[220:221]\n"
    "v_mfma_f32_16x16x32_bf16 v[48:51], a[212:215], v[96:99], v[48:51]\n"
    "v_pk_add_f32 v[214:215], v[214:215], v[222:223]\n"
    "v_mfma_f32_16x16x32_bf16 v[52:55], a[212:215], v[100:103], v[52:55]\n"
    "s_add_u32 s92, s47, 12288\n"
    "s_cmp_eq_u32 s66, 3\n"
    "s_cselect_b32 s92, 0x4000000, s92\n"
    "buffer_store_dwordx4 v[212:215], v230, s[80:83], s92 offen offset:0 sc1\n"
    "v_mfma_f32_16x16x32_bf16 v[56:59], a[212:215], v[104:107], v[56:59]\n"
    "s_waitcnt vmcnt(16)\n"
    "v_mfma_f32_16x16x32_bf16 v[0:3], a[216:219], v[108:111], v[0:3]\n"
    "s_add_u32 s91, s55, 24576\n"
    "v_mfma_f32_16x16x32_bf16 v[4:7], a[216:219], v[112:115], v[4:7]\n"
    "buffer_load_dwordx4 v[96:99], v230, s[56:59], s91 offen offset:0 sc1\n"
    "v_mfma_f32_16x16x32_bf16 v[8:11], a[216:219], v[116:119], v[8:11]\n"
    "buffer_load_dwordx4 v[100:103], v230, s[56:59], s91 offen offset:1024 sc1\n"
    "v_mfma_f32_16x16x32_bf16 v[12:15], a[220:223], v[108:111], v[12:15]\n"
    "buffer_load_dwordx4 v[104:107], v230, s[56:59], s91 offen offset:2048 sc1\n"
    "v_mfma_f32_16x16x32_bf16 v[16:19], a[220:223], v[112:115], v[16:19]\n"
    "v_mfma_f32_16x16x32_bf16 v[20:23], a[220:223], v[116:119], v[20:23]\n"
    "v_mfma_f32_16x16x32_bf16 v[24:27], a[224:227], v[108:111], v[24:27]\n"
    "v_mfma_f32_16x16x32_bf16 v[28:31], a[224:227], v[112:115], v[28:31]\n"
    "v_mfma_f32_16x16x32_bf16 v[32:35], a[224:227], v[116:119], v[32:35]\n"
    "v_mfma_f32_16x16x32_bf16 v[36:39], a[228:231], v[108:111], v[36:39]\n"
    "v_mfma_f32_16x16x32_bf16 v[40:43], a[228:231], v[112:115], v[40:43]\n"
    "v_mfma_f32_16x16x32_bf16 v[44:47], a[228:231], v[116:119], v[44:47]\n"
    "v_mfma_f32_16x16x32_bf16 v[48:51], a[232:235], v[108:111], v[48:51]\n"
    "v_mfma_f32_16x16x32_bf16 v[52:55], a[232:235], v[112:115], v[52:55]\n"
    "v_mfma_f32_16x16x32_bf16 v[56:59], a[232:235], v[116:119], v[56:59]\n"
    "s_waitcnt vmcnt(16)\n"
    "v_mfma_f32_16x16x32_bf16 v[0:3], a[236:239], v[120:123], v[0:3]\n"
    "s_add_u32 s91, s52, 36864\n"
    "v_mfma_f32_16x16x32_bf16 v[4:7], a[236:239], v[124:127], v[4:7]\n"
    "buffer_load_dwordx4 v[108:111], v230, s[56:59], s91 offen offset:0 sc1\n"
    "v_mfma_f32_16x16x32_bf16 v[8:11], a[236:239], v[128:131], v[8:11]\n"
    "buffer_load_dwordx4 v[112:115], v230, s[56:59], s91 offen offset:1024 sc1\n"
    "v_mfma_f32_16x16x32_bf16 v[12:15], a[240:243], v[120:123], v[12:15]\n"
    "buffer_load_dwordx4 v[116:119], v230, s[56:59], s91 offen offset:2048 sc1\n"
    "v_mfma_f32_16x16x32_bf16 v[16:19], a[240:243], v[124:127], v[16:19]\n"
    "v_mfma_f32_16x16x32_bf16 v[20:23], a[240:243], v[128:131], v[20:23]\n"
    "v_mfma_f32_16x16x32_bf16 v[24:27], a[244:247], v[120:123], v[24:27]\n"
    "v_mfma_f32_16x16x32_bf16 v[28:31], a[244:247], v[124:127], v[28:31]\n"
    "v_mfma_f32_16x16x32_bf16 v[32:35], a[244:247], v[128:131], v[32:35]\n"
    "v_mfma_f32_16x16x32_bf16 v[36:39], a[248:251], v[120:123], v[36:39]\n"
    "v_mfma_f32_16x16x32_bf16 v[40:43], a[248:251], v[124:127], v[40:43]\n"
    "v_mfma_f32_16x16x32_bf16 v[44:47], a[248:251], v[128:131], v[44:47]\n"
    "v_mfma_f32_16x16x32_bf16 v[48:51], a[252:255], v[120:123], v[48:51]\n"
    "v_mfma_f32_16x16x32_bf16 v[52:55], a[252:255], v[124:127], v[52:55]\n"
    "v_mfma_f32_16x16x32_bf16 v[56:59], a[252:255], v[128:131], v[56:59]\n"
    "s_waitcnt vmcnt(15)\n"
    "v_mfma_f32_16x16x32_bf16 v[0:3], v[132:135], v[60:63], v[0:3]\n"
    "s_add_u32 s91, s53, 36864\n"
    "v_mfma_f32_16x16x32_bf16 v[4:7], v[132:135], v[64:67], v[4:7]\n"
    "buffer_load_dwordx4 v[120:123], v230, s[56:59], s91 offen offset:0 sc1\n"
    "v_mfma_f32_16x16x32_bf16 v[8:11], v[132:135], v[68:71], v[8:11]\n"
    "buffer_load_dwordx4 v[124:127], v230, s[56:59], s91 offen offset:1024 sc1\n"
    "v_mfma_f32_16x16x32_bf16 v[12:15], v[136:139], v[60:63], v[12:15]\n"
    "buffer_load_dwordx4 v[128:131], v230, s[56:59], s91 offen offset:2048 sc1\n"
    "v_mfma_f32_16x16x32_bf16 v[16:19], v[136:139], v[64:67], v[16:19]\n"
    "v_mfma_f32_16x16x32_bf16 v[20:23], v[136:139], v[68:71], v[20:23]\n"
    "v_mfma_f32_16x16x32_bf16 v[24:27], v[140:143], v[60:63], v[24:27]\n"
    "v_mfma_f32_16x16x32_bf16 v[28:31], v[140:143], v[64:67], v[28:31]\n"
    "v_mfma_f32_16x16x32_bf16 v[32:35], v[140:143], v[68:71], v[32:35]\n"
    "v_mfma_f32_16x16x32_bf16 v[36:39], v[144:147], v[60:63], v[36:39]\n"
    "v_mfma_f32_16x16x32_bf16 v[40:43], v[144:147], v[64:67], v[40:43]\n"
    "v_mfma_f32_16x16x32_bf16 v[44:47], v[144:147], v[68:71], v[44:47]\n"
    "v_mfma_f32_16x16x32_bf16 v[48:51], v[148:151], v[60:63], v[48:51]\n"
    "v_mfma_f32_16x16x32_bf16 v[52:55], v[148:151], v[64:67], v[52:55]\n"
    "v_mfma_f32_16x16x32_bf16 v[56:59], v[148:151], v[68:71], v[56:59]\n"
    "s_waitcnt vmcnt(14)\n"
    "v_mfma_f32_16x16x32_bf16 v[0:3], v[152:155], v[72:75], v[0:3]\n"
    "s_add_u32 s91, s54, 36864\n"
    "v_mfma_f32_16x16x32_bf16 v[4:7], v[152:155], v[76:79], v[4:7]\n"
    "buffer_load_dwordx4 v[60:63], v230, s[56:59], s91 offen offset:0 sc1\n"
    "v_mfma_f32_16x16x32_bf16 v[8:11], v[152:155], v[80:83], v[8:11]\n"
    "buffer_load_dwordx4 v[64:67], v230, s[56:59], s91 offen offset:1024 sc1\n"
    "v_mfma_f32_16x16x32_bf16 v[12:15], v[156:159], v[72:75], v[12:15]\n"
    "buffer_load_dwordx4 v[68:71], v230, s[56:59], s91 offen offset:2048 sc1\n"
    "v_mfma_f32_16x16x32_bf16 v[16:19], v[156:159], v[76:79], v[16:19]\n"
    "v_mfma_f32_16x16x32_bf16 v[20:23], v[156:159], v[80:83], v[20:23]\n"
    "v_mfma_f32_16x16x32_bf16 v[24:27], v[160:163], v[72:75], v[24:27]\n"
    "v_mfma_f32_16x16x32_bf16 v[28:31], v[160:163], v[76:79], v[28:31]\n"
    "v_mfma_f32_16x16x32_bf16 v[32:35], v[160:163], v[80:83], v[32:35]\n"
    "v_mfma_f32_16x16x32_bf16 v[36:39], v[164:167], v[72:75], v[36:39]\n"
    "v_mfma_f32_16x16x32_bf16 v[40:43], v[164:167], v[76:79], v[40:43]\n"
    "v_mfma_f32_16x16x32_bf16 v[44:47], v[164:167], v[80:83], v[44:47]\n"
    "v_mfma_f32_16x16x32_bf16 v[48:51], v[168:171], v[72:75], v[48:51]\n"
    "v_mfma_f32_16x16x32_bf16 v[52:55], v[168:171], v[76:79], v[52:55]\n"
    "v_mfma_f32_16x16x32_bf16 v[56:59], v[168:171], v[80:83], v[56:59]\n"
    "s_waitcnt vmcnt(13)\n"
    "v_mfma_f32_16x16x32_bf16 v[0:3], v[172:175], v[84:87], v[0:3]\n"
    "s_add_u32 s91, s55, 36864\n"
    "v_mfma_f32_16x16x32_bf16 v[4:7], v[172:175], v[88:91], v[4:7]\n"
    "buffer_load_dwordx4 v[72:75], v230, s[56:59], s91 offen offset:0 sc1\n"
    "v_mfma_f32_16x16x32_bf16 v[8:11], v[172:175], v[92:95], v[8:11]\n"
    "buffer_load_dwordx4 v[76:79], v230, s[56:59], s91 offen offset:1024 sc1\n"
    "v_mfma_f32_16x16x32_bf16 v[12:15], v[176:179], v[84:87], v[12:15]\n"
    "buffer_load_dwordx4 v[80:83], v230, s[56:59], s91 offen offset:2048 sc1\n"
    "v_mfma_f32_16x16x32_bf16 v[16:19], v[176:179], v[88:91], v[16:19]\n"
    "v_mfma_f32_16x16x32_bf16 v[20:23], v[176:179], v[92:95], v[20:23]\n"
    "v_mfma_f32_16x16x32_bf16 v[24:27], v[180:183], v[84:87], v[24:27]\n"
    "s_waitcnt vmcnt(15)\n"
    "v_mov_b32 v250, s39\n"
    "v_mov_b32 v252, s94\n"
    "v_mov_b32 v253, s95\n"
    "s_mov_b64 exec, 1\n"
    "global_store_dwordx2 v250, v[252:253], s[64:65] offset:0 sc1\n"
    "s_mov_b64 exec, -1\n"
    "v_mfma_f32_16x16x32_bf16 v[28:31], v[180:183], v[88:91], v[28:31]\n"
    "v_mfma_f32_16x16x32_bf16 v[32:35], v[180:183], v[92:95], v[32:35]\n"
    "v_mfma_f32_16x16x32_bf16 v[36:39], v[184:187], v[84:87], v[36:39]\n"
    "v_mfma_f32_16x16x32_bf16 v[40:43], v[184:187], v[88:91], v[40:43]\n"
    "v_mfma_f32_16x16x32_bf16 v[44:47], v[184:187], v[92:95], v[44:47]\n"
    "v_mfma_f32_16x16x32_bf16 v[48:51], v[188:191], v[84:87], v[48:51]\n"
    "v_mfma_f32_16x16x32_bf16 v[52:55], v[188:191], v[88:91], v[52:55]\n"
    "v_mfma_f32_16x16x32_bf16 v[56:59], v[188:191], v[92:95], v[56:59]\n"
    "s_waitcnt vmcnt(13)\n"
    "v_mfma_f32_16x16x32_bf16 v[0:3], a[0:3], v[96:99], v[0:3]\n"
    "v_mfma_f32_16x16x32_bf16 v[4:7], a[0:3], v[100:103], v[4:7]\n"
    "v_mfma_f32_16x16x32_bf16 v[8:11], a[0:3], v[104:107], v[8:11]\n"
    "v_mfma_f32_16x16x32_bf16 v[12:15], a[4:7], v[96:99], v[12:15]\n"
    "v_mfma_f32_16x16x32_bf16 v[16:19], a[4:7], v[100:103], v[16:19]\n"
    "v_mfma_f32_16x16x32_bf16 v[20:23], a[4:7], v[104:107], v[20:23]\n"
    "v_mfma_f32_16x16x32_bf16 v[24:27], a[8:11], v[96:99], v[24:27]\n"
    "v_mfma_f32_16x16x32_bf16 v[28:31], a[8:11], v[100:103], v[28:31]\n"
    "v_mfma_f32_16x16x32_bf16 v[32:35], a[8:11], v[104:107], v[32:35]\n"
    "v_mfma_f32_16x16x32_bf16 v[36:39], a[12:15], v[96:99], v[36:39]\n"
    "v_mfma_f32_16x16x32_bf16 v[40:43], a[12:15], v[100:103], v[40:43]\n"
    "v_mfma_f32_16x16x32_bf16 v[44:47], a[12:15], v[104:107], v[44:47]\n"
    "v_mfma_f32_16x16x32_bf16 v[48:51], a[16:19], v[96:99], v[48:51]\n"
    "v_mfma_f32_16x16x32_bf16 v[52:55], a[16:19], v[100:103], v[52:55]\n"
    "v_mfma_f32_16x16x32_bf16 v[56:59], a[16:19], v[104:107], v[56:59]\n"
    "s_waitcnt vmcnt(10)\n"
    "v_mfma_f32_16x16x32_bf16 v[0:3], a[20:23], v[108:111], v[0:3]\n"
    "v_mfma_f32_16x16x32_bf16 v[4:7], a[20:23], v[112:115], v[4:7]\n"
    "v_mfma_f32_16x16x32_bf16 v[8:11], a[20:23], v[116:119], v[8:11]\n"
    "v_mfma_f32_16x16x32_bf16 v[12:15], a[24:27], v[108:111], v[12:15]\n"
    "v_mfma_f32_16x16x32_bf16 v[16:19], a[24:27], v[112:115], v[16:19]\n"
    "v_mfma_f32_16x16x32_bf16 v[20:23], a[24:27], v[116:119], v[20:23]\n"
    "v_mfma_f32_16x16x32_bf16 v[24:27], a[28:31], v[108:111], v[24:27]\n"
    "v_mfma_f32_16x16x32_bf16 v[28:31], a[28:31], v[112:115], v[28:31]\n"
    "v_mfma_f32_16x16x32_bf16 v[32:35], a[28:31], v[116:119], v[32:35]\n"
    "v_mfma_f32_16x16x32_bf16 v[36:39], a[32:35], v[108:111], v[36:39]\n"
    "v_mfma_f32_16x16x32_bf16 v[40:43], a[32:35], v[112:115], v[40:43]\n"
    "v_mfma_f32_16x16x32_bf16 v[44:47], a[32:35], v[116:119], v[44:47]\n"
    "v_mfma_f32_16x16x32_bf16 v[48:51], a[36:39], v[108:111], v[48:51]\n"
    "v_mfma_f32_16x16x32_bf16 v[52:55], a[36:39], v[112:115], v[52:55]\n"
    "v_mfma_f32_16x16x32_bf16 v[56:59], a[36:39], v[116:119], v[56:59]\n"
    "s_waitcnt vmcnt(7)\n"
    "v_mfma_f32_16x16x32_bf16 v[0:3], a[40:43], v[120:123], v[0:3]\n"
    "v_mfma_f32_16x16x32_bf16 v[4:7], a[40:43], v[124:127], v[4:7]\n"
    "v_mfma_f32_16x16x32_bf16 v[8:11], a[40:43], v[128:131], v[8:11]\n"
    "v_mfma_f32_16x16x32_bf16 v[12:15], a[44:47], v[120:123], v[12:15]\n"
    "v_mfma_f32_16x16x32_bf16 v[16:19], a[44:47], v[124:127], v[16:19]\n"
    "v_mfma_f32_16x16x32_bf16 v[20:23], a[44:47], v[128:131], v[20:23]\n"
    "v_mfma_f32_16x16x32_bf16 v[24:27], a[48:51], v[120:123], v[24:27]\n"
    "v_mfma_f32_16x16x32_bf16 v[28:31], a[48:51], v[124:127], v[28:31]\n"
    "v_mfma_f32_16x16x32_bf16 v[32:35], a[48:51], v[128:131], v[32:35]\n"
    "v_mfma_f32_16x16x32_bf16 v[36:39], a[52:55], v[120:123], v[36:39]\n"
    "v_mfma_f32_16x16x32_bf16 v[40:43], a[52:55], v[124:127], v[40:43]\n"
    "v_mfma_f32_16x16x32_bf16 v[44:47], a[52:55], v[128:131], v[44:47]\n"
    "v_mfma_f32_16x16x32_bf16 v[48:51], a[56:59], v[120:123], v[48:51]\n"
    "v_mfma_f32_16x16x32_bf16 v[52:55], a[56:59], v[124:127], v[52:55]\n"
    "v_mfma_f32_16x16x32_bf16 v[56:59], a[56:59], v[128:131], v[56:59]\n"
    "s_waitcnt vmcnt(4)\n"
    "v_mfma_f32_16x16x32_bf16 v[0:3], a[60:63], v[60:63], v[0:3]\n"
    "v_mfma_f32_16x16x32_bf16 v[4:7], a[60:63], v[64:67], v[4:7]\n"
    "v_mfma_f32_16x16x32_bf16 v[8:11], a[60:63], v[68:71], v[8:11]\n"
    "v_mfma_f32_16x16x32_bf16 v[12:15], a[64:67], v[60:63], v[12:15]\n"
    "v_mfma_f32_16x16x32_bf16 v[16:19], a[64:67], v[64:67], v[16:19]\n"
    "v_mfma_f32_16x16x32_bf16 v[20:23], a[64:67], v[68:71], v[20:23]\n"
    "v_mfma_f32_16x16x32_bf16 v[24:27], a[68:71], v[60:63], v[24:27]\n"
    "v_mov_b32 v236, s74\n"
    "global_load_dwordx2 v[240:241], v236, s[64:65] offset:0 sc1\n"
    "v_mfma_f32_16x16x32_bf16 v[28:31], a[68:71], v[64:67], v[28:31]\n"
    "v_mfma_f32_16x16x32_bf16 v[32:35], a[68:71], v[68:71], v[32:35]\n"
    "v_mfma_f32_16x16x32_bf16 v[36:39], a[72:75], v[60:63], v[36:39]\n"
    "v_mfma_f32_16x16x32_bf16 v[40:43], a[72:75], v[64:67], v[40:43]\n"
    "v_mfma_f32_16x16x32_bf16 v[44:47], a[72:75], v[68:71], v[44:47]\n"
    "v_mfma_f32_16x16x32_bf16 v[48:51], a[76:79], v[60:63], v[48:51]\n"
    "v_mfma_f32_16x16x32_bf16 v[52:55], a[76:79], v[64:67], v[52:55]\n"
    "v_mfma_f32_16x16x32_bf16 v[56:59], a[76:79], v[68:71], v[56:59]\n"
    "s_waitcnt vmcnt(2)\n"
    "v_mfma_f32_16x16x32_bf16 v[0:3], a[80:83], v[72:75], v[0:3]\n"
    "v_mfma_f32_16x16x32_bf16 v[4:7], a[80:83], v[76:79], v[4:7]\n"
    "v_mfma_f32_16x16x32_bf16 v[8:11], a[80:83], v[80:83], v[8:11]\n"
    "v_mfma_f32_16x16x32_bf16 v[12:15], a[84:87], v[72:75], v[12:15]\n"
    "v_mfma_f32_16x16x32_bf16 v[16:19], a[84:87], v[76:79], v[16:19]\n"
    "v_mfma_f32_16x16x32_bf16 v[20:23], a[84:87], v[80:83], v[20:23]\n"
    "v_mfma_f32_16x16x32_bf16 v[24:27], a[88:91], v[72:75], v[24:27]\n"
    "v_mfma_f32_16x16x32_bf16 v[28:31], a[88:91], v[76:79], v[28:31]\n"
    "v_mfma_f32_16x16x32_bf16 v[32:35], a[88:91], v[80:83], v[32:35]\n"
    "v_mfma_f32_16x16x32_bf16 v[36:39], a[92:95], v[72:75], v[36:39]\n"
    "v_mfma_f32_16x16x32_bf16 v[40:43], a[92:95], v[76:79], v[40:43]\n"
    "v_mfma_f32_16x16x32_bf16 v[44:47], a[92:95], v[80:83], v[44:47]\n"
    "v_mfma_f32_16x16x32_bf16 v[48:51], v[192:195], v[72:75], v[48:51]\n"
    "v_mfma_f32_16x16x32_bf16 v[52:55], v[192:195], v[76:79], v[52:55]\n"
    "v_mfma_f32_16x16x32_bf16 v[56:59], v[192:195], v[80:83], v[56:59]\n"
    "s_waitcnt vmcnt(0)\n"
    "v_cmp_eq_u64 vcc, v[240:241], s[94:95]\n"
    "s_nop 3\n"
    "s_cmp_eq_u64 vcc, -1\n"
    "s_cbranch_scc0 L_POLL3\n"
    "L_POLL3_OK:\n"
    "s_add_u32 s92, s97, 0\n"
    "buffer_load_dwordx4 v[132:135], v230, s[80:83], s92 offen offset:0 sc1\n"
    "s_add_u32 s92, s97, 4096\n"
    "buffer_load_dwordx4 v[136:139], v230, s[80:83], s92 offen offset:0 sc1\n"
    "s_add_u32 s92, s97, 8192\n"
    "buffer_load_dwordx4 v[140:143], v230, s[80:83], s92 offen offset:0 sc1\n"
    "s_add_u32 s92, s97, 12288\n"
    "s_cmp_eq_u32 s66, 3\n"
    "s_cselect_b32 s92, 0x4000000, s92\n"
    "buffer_load_dwordx4 v[144:147], v230, s[80:83], s92 offen offset:0 sc1\n"
    "v_add_u32 v232, s96, v230\n"
    "ds_write_b128 v232, v[0:3] offset:0\n"
    "ds_write_b128 v232, v[4:7] offset:1024\n"
    "ds_write_b128 v232, v[8:11] offset:2048\n"
    "ds_write_b128 v232, v[12:15] offset:3072\n"
    "ds_write_b128 v232, v[16:19] offset:4096\n"
    "ds_write_b128 v232, v[20:23] offset:5120\n"
    "ds_write_b128 v232, v[24:27] offset:6144\n"
    "ds_write_b128 v232, v[28:31] offset:7168\n"
    "ds_write_b128 v232, v[32:35] offset:8192\n"
    "ds_write_b128 v232, v[36:39] offset:9216\n"
    "ds_write_b128 v232, v[40:43] offset:10240\n"
    "ds_write_b128 v232, v[44:47] offset:11264\n"
    "ds_write_b128 v232, v[48:51] offset:12288\n"
    "ds_write_b128 v232, v[52:55] offset:13312\n"
    "ds_write_b128 v232, v[56:59] offset:14336\n"
    "v_and_b32 v236, 15, v254\n"
    "v_mul_u32_u24 v236, 1152, v236\n"
    "v_lshrrev_b32 v237, 4, v254\n"
    "v_lshl_add_u32 v234, v237, 3, v236\n"
    "v_add_u32 v233, 65536, v233\n"
    "s_waitcnt lgkmcnt(0)\n"
    "s_barrier\n"
    "ds_read_b128 v[64:67], v233 offset:0\n"
    "ds_read_b128 v[68:71], v233 offset:16384\n"
    "ds_read_b128 v[72:75], v233 offset:32768\n"
    "ds_read_b128 v[76:79], v233 offset:49152\n"
    "ds_read_b128 v[80:83], v233 offset:4096\n"
    "ds_read_b128 v[84:87], v233 offset:20480\n"
    "ds_read_b128 v[88:91], v233 offset:36864\n"
    "ds_read_b128 v[92:95], v233 offset:53248\n"
    "ds_read_b128 v[96:99], v233 offset:8192\n"
    "ds_read_b128 v[100:103], v233 offset:24576\n"
    "ds_read_b128 v[104:107], v233 offset:40960\n"
    "ds_read_b128 v[108:111], v233 offset:57344\n"
    "ds_read_b128 v[112:115], v233 offset:12288\n"
    "ds_read_b128 v[116:119], v233 offset:28672\n"
    "ds_read_b128 v[120:123], v233 offset:45056\n"
    "ds_read_b128 v[124:127], v233 offset:61440\n"
    "s_waitcnt lgkmcnt(12)\n"
    "v_pk_add_f32 v[64:65], v[64:65], v[68:69]\n"
    "v_pk_add_f32 v[72:73], v[72:73], v[76:77]\n"
    "v_pk_add_f32 v[66:67], v[66:67], v[70:71]\n"
    "v_pk_add_f32 v[74:75], v[74:75], v[78:79]\n"
    "v_pk_add_f32 v[64:65], v[64:65], v[72:73]\n"
    "v_pk_add_f32 v[66:67], v[66:67], v[74:75]\n"
    "s_waitcnt vmcnt(3)\n"
    "v_pk_add_f32 v[64:65], v[64:65], v[132:133]\n"
    "v_pk_add_f32 v[66:67], v[66:67], v[134:135]\n"
    "v_cvt_pk_bf16_f32 v200, v64, v65\n"
    "v_cvt_pk_bf16_f32 v201, v66, v67\n"
    "buffer_store_dwordx2 v[200:201], v234, s[60:63], s40 offen offset:0\n"
    "s_waitcnt lgkmcnt(8)\n"
    "v_pk_add_f32 v[80:81], v[80:81], v[84:85]\n"
    "v_pk_add_f32 v[88:89], v[88:89], v[92:93]\n"
    "v_pk_add_f32 v[82:83], v[82:83], v[86:87]\n"
    "v_pk_add_f32 v[90:91], v[90:91], v[94:95]\n"
    "v_pk_add_f32 v[80:81], v[80:81], v[88:89]\n"
    "v_pk_add_f32 v[82:83], v[82:83], v[90:91]\n"
    "s_waitcnt vmcnt(3)\n"
    "v_pk_add_f32 v[80:81], v[80:81], v[136:137]\n"
    "v_pk_add_f32 v[82:83], v[82:83], v[138:139]\n"
    "v_cvt_pk_bf16_f32 v202, v80, v81\n"
    "v_cvt_pk_bf16_f32 v203, v82, v83\n"
    "buffer_store_dwordx2 v[202:203], v234, s[60:63], s41 offen offset:0\n"
    "s_waitcnt lgkmcnt(0)\n"
    "v_pk_add_f32 v[96:97], v[96:97], v[100:101]\n"
    "v_pk_add_f32 v[104:105], v[104:105], v[108:109]\n"
    "v_pk_add_f32 v[98:99], v[98:99], v[102:103]\n"
    "v_pk_add_f32 v[106:107], v[106:107], v[110:111]\n"
    "v_pk_add_f32 v[96:97], v[96:97], v[104:105]\n"
    "v_pk_add_f32 v[98:99], v[98:99], v[106:107]\n"
    "s_waitcnt vmcnt(3)\n"
    "v_pk_add_f32 v[96:97], v[96:97], v[140:141]\n"
    "v_pk_add_f32 v[98:99], v[98:99], v[142:143]\n"
    "v_cvt_pk_bf16_f32 v204, v96, v97\n"
    "v_cvt_pk_bf16_f32 v205, v98, v99\n"
    "buffer_store_dwordx2 v[204:205], v234, s[60:63], s42 offen offset:0\n"
    "v_pk_add_f32 v[112:113], v[112:113], v[116:117]\n"
    "v_pk_add_f32 v[120:121], v[120:121], v[124:125]\n"
    "v_pk_add_f32 v[114:115], v[114:115], v[118:119]\n"
    "v_pk_add_f32 v[122:123], v[122:123], v[126:127]\n"
    "v_pk_add_f32 v[112:113], v[112:113], v[120:121]\n"
    "v_pk_add_f32 v[114:115], v[114:115], v[122:123]\n"
    "s_waitcnt vmcnt(3)\n"
    "v_pk_add_f32 v[112:113], v[112:113], v[144:145]\n"
    "v_pk_add_f32 v[114:115], v[114:115], v[146:147]\n"
    "v_cvt_pk_bf16_f32 v206, v112, v113\n"
    "v_cvt_pk_bf16_f32 v207, v114, v115\n"
    "buffer_store_dwordx2 v[206:207], v234, s[60:63], s43 offen offset:0\n"
    "s_cmp_lg_u32 s99, 255\n"
    "s_cbranch_scc1 L_END\n"
    "s_cmp_lg_u32 s66, 0\n"
    "s_cbranch_scc1 L_END\n"
    "s_waitcnt vmcnt(4)\n"
    "v_cmp_eq_u64 vcc, v[242:243], s[94:95]\n"
    "s_nop 3\n"
    "s_mov_b64 s[90:91], vcc\n"
    "v_cmp_eq_u64 vcc, v[244:245], s[94:95]\n"
    "s_nop 3\n"
    "s_and_b64 s[90:91], s[90:91], vcc\n"
    "v_cmp_eq_u64 vcc, v[246:247], s[94:95]\n"
    "s_nop 3\n"
    "s_and_b64 s[90:91], s[90:91], vcc\n"
    "v_cmp_eq_u64 vcc, v[248:249], s[94:95]\n"
    "s_nop 3\n"
    "s_and_b64 s[90:91], s[90:91], vcc\n"
    "s_cmp_eq_u64 s[90:91], -1\n"
    "s_cbranch_scc1 L_POLLA_OK\n"
    "s_mov_b32 s37, 0\n"
    "L_POLLA:\n"
    "s_nop 7\n"
    "s_nop 3\n"
    "global_load_dwordx2 v[240:241], v238, s[64:65] offset:0 sc1\n"
    "global_load_dwordx2 v[242:243], v238, s[64:65] offset:512 sc1\n"
    "global_load_dwordx2 v[244:245], v238, s[64:65] offset:1024 sc1\n"
    "global_load_dwordx2 v[246:247], v238, s[64:65] offset:1536 sc1\n"
    "s_waitcnt vmcnt(0)\n"
    "v_cmp_eq_u64 vcc, v[240:241], s[94:95]\n"
    "s_nop 3\n"
    "s_mov_b64 s[90:91], vcc\n"
    "v_cmp_eq_u64 vcc, v[242:243], s[94:95]\n"
    "s_nop 3\n"
    "s_and_b64 s[90:91], s[90:91], vcc\n"
    "v_cmp_eq_u64 vcc, v[244:245], s[94:95]\n"
    "s_nop 3\n"
    "s_and_b64 s[90:91], s[90:91], vcc\n"
    "v_cmp_eq_u64 vcc, v[246:247], s[94:95]\n"
    "s_nop 3\n"
    "s_and_b64 s[90:91], s[90:91], vcc\n"
    "s_cmp_eq_u64 s[90:91], -1\n"
    "s_cbranch_scc1 L_POLLA_OK\n"
    "s_sleep 1\n"
    "s_add_u32 s37, s37, 1\n"
    "s_cmp_lt_u32 s37, 40000\n"
    "s_cbranch_scc1 L_POLLA\n"
    "L_POLLA_OK:\n"
    "s_nop 7\n"
    "s_nop 3\n"
    "v_mov_b32 v244, 0\n"
    "v_mov_b32 v246, s94\n"
    "v_mov_b32 v247, s95\n"
    "s_mov_b64 exec, 1\n"
    "global_store_dwordx2 v244, v[246:247], s[64:65] offset:0 sc1\n"
    "s_mov_b64 exec, -1\n"
    "s_branch L_END\n"
    "L_FALLBACK:\n"
    "s_nop 7\n"
    "s_nop 3\n"
    "v_subrev_u32 v234, 32768, v234\n"
    "v_subrev_u32 v235, 32768, v235\n"
    "v_subrev_u32 v236, 32768, v236\n"
    "v_subrev_u32 v237, 32768, v237\n"
    "v_mov_b32 v216, 4286578688\n"
    "v_mov_b32 v217, 4286578688\n"
    "v_mov_b32 v218, 4286578688\n"
    "v_mov_b32 v250, 0\n"
    "v_mov_b32 v0, 0\n"
    "v_mov_b32 v1, 0\n"
    "v_mov_b32 v2, 0\n"
    "v_mov_b32 v3, 0\n"
    "v_mov_b32 v4, 0\n"
    "v_mov_b32 v5, 0\n"
    "v_mov_b32 v6, 0\n"
    "v_mov_b32 v7, 0\n"
    "v_mov_b32 v8, 0\n"
    "v_mov_b32 v9, 0\n"
    "v_mov_b32 v10, 0\n"
    "v_mov_b32 v11, 0\n"
    "v_mov_b32 v12, 0\n"
    "v_mov_b32 v13, 0\n"
    "v_mov_b32 v14, 0\n"
    "v_mov_b32 v15, 0\n"
    "v_mov_b32 v16, 0\n"
    "v_mov_b32 v17, 0\n"
    "v_mov_b32 v18, 0\n"
    "v_mov_b32 v19, 0\n"
    "v_mov_b32 v20, 0\n"
    "v_mov_b32 v21, 0\n"
    "v_mov_b32 v22, 0\n"
    "v_mov_b32 v23, 0\n"
    "v_mov_b32 v24, 0\n"
    "v_mov_b32 v25, 0\n"
    "v_mov_b32 v26, 0\n"
    "v_mov_b32 v27, 0\n"
    "v_mov_b32 v28, 0\n"
    "v_mov_b32 v29, 0\n"
    "v_mov_b32 v30, 0\n"
    "v_mov_b32 v31, 0\n"
    "v_mov_b32 v32, 0\n"
    "v_mov_b32 v33, 0\n"
    "v_mov_b32 v34, 0\n"
    "v_mov_b32 v35, 0\n"
    "v_mov_b32 v36, 0\n"
    "v_mov_b32 v37, 0\n"
    "v_mov_b32 v38, 0\n"
    "v_mov_b32 v39, 0\n"
    "v_mov_b32 v40, 0\n"
    "v_mov_b32 v41, 0\n"
    "v_mov_b32 v42, 0\n"
    "v_mov_b32 v43, 0\n"
    "v_mov_b32 v44, 0\n"
    "v_mov_b32 v45, 0\n"
    "v_mov_b32 v46, 0\n"
    "v_mov_b32 v47, 0\n"
    "v_mov_b32 v48, 0\n"
    "v_mov_b32 v49, 0\n"
    "v_mov_b32 v50, 0\n"
    "v_mov_b32 v51, 0\n"
    "v_mov_b32 v52, 0\n"
    "v_mov_b32 v53, 0\n"
    "v_mov_b32 v54, 0\n"
    "v_mov_b32 v55, 0\n"
    "v_mov_b32 v56, 0\n"
    "v_mov_b32 v57, 0\n"
    "v_mov_b32 v58, 0\n"
    "v_mov_b32 v59, 0\n"
    "v_mov_b32 v60, 0\n"
    "v_mov_b32 v61, 0\n"
    "v_mov_b32 v62, 0\n"
    "v_mov_b32 v63, 0\n"
    "v_mov_b32 v64, 0\n"
    "v_mov_b32 v65, 0\n"
    "v_mov_b32 v66, 0\n"
    "v_mov_b32 v67, 0\n"
    "v_mov_b32 v68, 0\n"
    "v_mov_b32 v69, 0\n"
    "v_mov_b32 v70, 0\n"
    "v_mov_b32 v71, 0\n"
    "v_mov_b32 v72, 0\n"
    "v_mov_b32 v73, 0\n"
    "v_mov_b32 v74, 0\n"
    "v_mov_b32 v75, 0\n"
    "v_mov_b32 v76, 0\n"
    "v_mov_b32 v77, 0\n"
    "v_mov_b32 v78, 0\n"
    "v_mov_b32 v79, 0\n"
    "v_mov_b32 v80, 0\n"
    "v_mov_b32 v81, 0\n"
    "v_mov_b32 v82, 0\n"
    "v_mov_b32 v83, 0\n"
    "v_mov_b32 v84, 0\n"
    "v_mov_b32 v85, 0\n"
    "v_mov_b32 v86, 0\n"
    "v_mov_b32 v87, 0\n"
    "v_mov_b32 v88, 0\n"
    "v_mov_b32 v89, 0\n"
    "v_mov_b32 v90, 0\n"
    "v_mov_b32 v91, 0\n"
    "v_mov_b32 v92, 0\n"
    "v_mov_b32 v93, 0\n"
    "v_mov_b32 v94, 0\n"
    "v_mov_b32 v95, 0\n"
    "v_accvgpr_write_b32 a64, v250\n"
    "v_accvgpr_write_b32 a65, v250\n"
    "v_accvgpr_write_b32 a66, v250\n"
    "v_accvgpr_write_b32 a67, v250\n"
    "v_accvgpr_write_b32 a68, v250\n"
    "v_accvgpr_write_b32 a69, v250\n"
    "v_accvgpr_write_b32 a70, v250\n"
    "v_accvgpr_write_b32 a71, v250\n"
    "v_accvgpr_write_b32 a72, v250\n"
    "v_accvgpr_write_b32 a73, v250\n"
    "v_accvgpr_write_b32 a74, v250\n"
    "v_accvgpr_write_b32 a75, v250\n"
    "s_mov_b32 s92, 0\n"
    "L_FB_LOOP:\n"
    "s_nop 7\n"
    "s_nop 3\n"
    "s_barrier\n"
    "s_add_u32 s88, s87, s92\n"
    "s_add_u32 s88, s88, 0\n"
    "s_add_u32 m0, s86, 0\n"
    "s_nop 0\n"
    "buffer_load_dwordx4 v238, s[48:51], s88 offen offset:0 lds\n"
    "buffer_load_dwordx4 v239, s[48:51], s88 offen offset:1024 lds\n"
    "buffer_load_dwordx4 v240, s[48:51], s88 offen offset:2048 lds\n"
    "buffer_load_dwordx4 v241, s[48:51], s88 offen offset:3072 lds\n"
    "s_add_u32 s88, s87, s92\n"
    "s_add_u32 s88, s88, 4096\n"
    "s_add_u32 m0, s86, 4096\n"
    "s_nop 0\n"
    "buffer_load_dwordx4 v238, s[48:51], s88 offen offset:0 lds\n"
    "buffer_load_dwordx4 v239, s[48:51], s88 offen offset:1024 lds\n"
    "buffer_load_dwordx4 v240, s[48:51], s88 offen offset:2048 lds\n"
    "buffer_load_dwordx4 v241, s[48:51], s88 offen offset:3072 lds\n"
    "s_waitcnt vmcnt(0)\n"
    "s_barrier\n"
    "ds_read_b128 a[0:3], v234 offset:0\n"
    "ds_read_b128 a[4:7], v234 offset:4096\n"
    "ds_read_b128 a[8:11], v235 offset:0\n"
    "ds_read_b128 a[12:15], v235 offset:4096\n"
    "ds_read_b128 a[16:19], v234 offset:2048\n"
    "ds_read_b128 a[20:23], v234 offset:6144\n"
    "ds_read_b128 a[24:27], v235 offset:2048\n"
    "ds_read_b128 a[28:31], v235 offset:6144\n"
    "s_waitcnt lgkmcnt(7)\n"
    "v_mfma_f32_16x16x32_bf16 v[144:147], a[0:3], v[96:99], 0\n"
    "v_mfma_f32_16x16x32_bf16 v[148:151], a[0:3], v[100:103], 0\n"
    "v_mfma_f32_16x16x32_bf16 v[152:155], a[0:3], v[104:107], 0\n"
    "s_waitcnt lgkmcnt(6)\n"
    "v_mfma_f32_16x16x32_bf16 v[156:159], a[4:7], v[96:99], 0\n"
    "v_mfma_f32_16x16x32_bf16 v[160:163], a[4:7], v[100:103], 0\n"
    "v_mfma_f32_16x16x32_bf16 v[164:167], a[4:7], v[104:107], 0\n"
    "s_waitcnt lgkmcnt(5)\n"
    "v_mfma_f32_16x16x32_bf16 v[144:147], a[8:11], v[108:111], v[144:147]\n"
    "v_mfma_f32_16x16x32_bf16 v[148:151], a[8:11], v[112:115], v[148:151]\n"
    "v_mfma_f32_16x16x32_bf16 v[152:155], a[8:11], v[116:119], v[152:155]\n"
    "s_waitcnt lgkmcnt(4)\n"
    "v_mfma_f32_16x16x32_bf16 v[156:159], a[12:15], v[108:111], v[156:159]\n"
    "v_mfma_f32_16x16x32_bf16 v[160:163], a[12:15], v[112:115], v[160:163]\n"
    "v_mfma_f32_16x16x32_bf16 v[164:167], a[12:15], v[116:119], v[164:167]\n"
    "s_waitcnt lgkmcnt(3)\n"
    "v_mfma_f32_16x16x32_bf16 v[144:147], a[16:19], v[120:123], v[144:147]\n"
    "v_mfma_f32_16x16x32_bf16 v[148:151], a[16:19], v[124:127], v[148:151]\n"
    "v_mfma_f32_16x16x32_bf16 v[152:155], a[16:19], v[128:131], v[152:155]\n"
    "s_waitcnt lgkmcnt(2)\n"
    "v_mfma_f32_16x16x32_bf16 v[156:159], a[20:23], v[120:123], v[156:159]\n"
    "v_mfma_f32_16x16x32_bf16 v[160:163], a[20:23], v[124:127], v[160:163]\n"
    "v_mfma_f32_16x16x32_bf16 v[164:167], a[20:23], v[128:131], v[164:167]\n"
    "s_waitcnt lgkmcnt(0)\n"
    "v_mfma_f32_16x16x32_bf16 v[144:147], a[24:27], v[132:135], v[144:147]\n"
    "v_mfma_f32_16x16x32_bf16 v[148:151], a[24:27], v[136:139], v[148:151]\n"
    "v_mfma_f32_16x16x32_bf16 v[152:155], a[24:27], v[140:143], v[152:155]\n"
    "v_mfma_f32_16x16x32_bf16 v[156:159], a[28:31], v[132:135], v[156:159]\n"
    "v_mfma_f32_16x16x32_bf16 v[160:163], a[28:31], v[136:139], v[160:163]\n"
    "v_mfma_f32_16x16x32_bf16 v[164:167], a[28:31], v[140:143], v[164:167]\n"
    "ds_read_b64_tr_b16 a[32:33], v236 offset:0\n"
    "ds_read_b64_tr_b16 a[34:35], v236 offset:4096\n"
    "ds_read_b64_tr_b16 a[36:37], v237 offset:0\n"
    "ds_read_b64_tr_b16 a[38:39], v237 offset:4096\n"
    "ds_read_b64_tr_b16 a[40:41], v236 offset:512\n"
    "ds_read_b64_tr_b16 a[42:43], v236 offset:4608\n"
    "ds_read_b64_tr_b16 a[44:45], v237 offset:512\n"
    "ds_read_b64_tr_b16 a[46:47], v237 offset:4608\n"
    "ds_read_b64_tr_b16 a[48:49], v236 offset:1024\n"
    "ds_read_b64_tr_b16 a[50:51], v236 offset:5120\n"
    "ds_read_b64_tr_b16 a[52:53], v237 offset:1024\n"
    "ds_read_b64_tr_b16 a[54:55], v237 offset:5120\n"
    "ds_read_b64_tr_b16 a[56:57], v236 offset:1536\n"
    "ds_read_b64_tr_b16 a[58:59], v236 offset:5632\n"
    "ds_read_b64_tr_b16 a[60:61], v237 offset:1536\n"
    "ds_read_b64_tr_b16 a[62:63], v237 offset:5632\n"
    "v_max3_f32 v250, v144, v145, v146\n"
    "v_max3_f32 v250, v250, v147, v156\n"
    "v_max3_f32 v250, v250, v157, v158\n"
    "v_max_f32 v250, v250, v159\n"
    "v_mov_b32 v251, v250\n"
    "s_nop 1\n"
    "v_permlane32_swap_b32 v250, v251\n"
    "v_max_f32 v250, v250, v251\n"
    "v_mov_b32 v251, v250\n"
    "s_nop 1\n"
    "v_permlane16_swap_b32 v250, v251\n"
    "v_max_f32 v250, v250, v251\n"
    "v_max_f32 v250, v250, v216\n"
    "v_sub_f32 v219, v216, v250\n"
    "v_exp_f32 v219, v219\n"
    "v_mov_b32 v216, v250\n"
    "v_sub_f32 v144, v144, v250\n"
    "v_exp_f32 v144, v144\n"
    "v_sub_f32 v145, v145, v250\n"
    "v_exp_f32 v145, v145\n"
    "v_sub_f32 v146, v146, v250\n"
    "v_exp_f32 v146, v146\n"
    "v_sub_f32 v147, v147, v250\n"
    "v_exp_f32 v147, v147\n"
    "v_sub_f32 v156, v156, v250\n"
    "v_exp_f32 v156, v156\n"
    "v_sub_f32 v157, v157, v250\n"
    "v_exp_f32 v157, v157\n"
    "v_sub_f32 v158, v158, v250\n"
    "v_exp_f32 v158, v158\n"
    "v_sub_f32 v159, v159, v250\n"
    "v_exp_f32 v159, v159\n"
    "v_cvt_pk_bf16_f32 v192, v144, v145\n"
    "v_cvt_pk_bf16_f32 v193, v146, v147\n"
    "v_cvt_pk_bf16_f32 v194, v156, v157\n"
    "v_cvt_pk_bf16_f32 v195, v158, v159\n"
    "v_mul_f32 v0, v0, v219\n"
    "v_mul_f32 v1, v1, v219\n"
    "v_mul_f32 v2, v2, v219\n"
    "v_mul_f32 v3, v3, v219\n"
    "v_mul_f32 v12, v12, v219\n"
    "v_mul_f32 v13, v13, v219\n"
    "v_mul_f32 v14, v14, v219\n"
    "v_mul_f32 v15, v15, v219\n"
    "v_mul_f32 v24, v24, v219\n"
    "v_mul_f32 v25, v25, v219\n"
    "v_mul_f32 v26, v26, v219\n"
    "v_mul_f32 v27, v27, v219\n"
    "v_mul_f32 v36, v36, v219\n"
    "v_mul_f32 v37, v37, v219\n"
    "v_mul_f32 v38, v38, v219\n"
    "v_mul_f32 v39, v39, v219\n"
    "v_mul_f32 v48, v48, v219\n"
    "v_mul_f32 v49, v49, v219\n"
    "v_mul_f32 v50, v50, v219\n"
    "v_mul_f32 v51, v51, v219\n"
    "v_mul_f32 v60, v60, v219\n"
    "v_mul_f32 v61, v61, v219\n"
    "v_mul_f32 v62, v62, v219\n"
    "v_mul_f32 v63, v63, v219\n"
    "v_mul_f32 v72, v72, v219\n"
    "v_mul_f32 v73, v73, v219\n"
    "v_mul_f32 v74, v74, v219\n"
    "v_mul_f32 v75, v75, v219\n"
    "v_mul_f32 v84, v84, v219\n"
    "v_mul_f32 v85, v85, v219\n"
    "v_mul_f32 v86, v86, v219\n"
    "v_mul_f32 v87, v87, v219\n"
    "v_accvgpr_read_b32 v251, a64\n"
    "v_mul_f32 v251, v251, v219\n"
    "v_accvgpr_write_b32 a64, v251\n"
    "v_accvgpr_read_b32 v251, a65\n"
    "v_mul_f32 v251, v251, v219\n"
    "v_accvgpr_write_b32 a65, v251\n"
    "v_accvgpr_read_b32 v251, a66\n"
    "v_mul_f32 v251, v251, v219\n"
    "v_accvgpr_write_b32 a66, v251\n"
    "v_accvgpr_read_b32 v251, a67\n"
    "v_mul_f32 v251, v251, v219\n"
    "v_accvgpr_write_b32 a67, v251\n"
    "v_max3_f32 v250, v148, v149, v150\n"
    "v_max3_f32 v250, v250, v151, v160\n"
    "v_max3_f32 v250, v250, v161, v162\n"
    "v_max_f32 v250, v250, v163\n"
    "v_mov_b32 v251, v250\n"
    "s_nop 1\n"
    "v_permlane32_swap_b32 v250, v251\n"
    "v_max_f32 v250, v250, v251\n"
    "v_mov_b32 v251, v250\n"
    "s_nop 1\n"
    "v_permlane16_swap_b32 v250, v251\n"
    "v_max_f32 v250, v250, v251\n"
    "v_max_f32 v250, v250, v217\n"
    "v_sub_f32 v220, v217, v250\n"
    "v_exp_f32 v220, v220\n"
    "v_mov_b32 v217, v250\n"
    "v_sub_f32 v148, v148, v250\n"
    "v_exp_f32 v148, v148\n"
    "v_sub_f32 v149, v149, v250\n"
    "v_exp_f32 v149, v149\n"
    "v_sub_f32 v150, v150, v250\n"
    "v_exp_f32 v150, v150\n"
    "v_sub_f32 v151, v151, v250\n"
    "v_exp_f32 v151, v151\n"
    "v_sub_f32 v160, v160, v250\n"
    "v_exp_f32 v160, v160\n"
    "v_sub_f32 v161, v161, v250\n"
    "v_exp_f32 v161, v161\n"
    "v_sub_f32 v162, v162, v250\n"
    "v_exp_f32 v162, v162\n"
    "v_sub_f32 v163, v163, v250\n"
    "v_exp_f32 v163, v163\n"
    "v_cvt_pk_bf16_f32 v196, v148, v149\n"
    "v_cvt_pk_bf16_f32 v197, v150, v151\n"
    "v_cvt_pk_bf16_f32 v198, v160, v161\n"
    "v_cvt_pk_bf16_f32 v199, v162, v163\n"
    "v_mul_f32 v4, v4, v220\n"
    "v_mul_f32 v5, v5, v220\n"
    "v_mul_f32 v6, v6, v220\n"
    "v_mul_f32 v7, v7, v220\n"
    "v_mul_f32 v16, v16, v220\n"
    "v_mul_f32 v17, v17, v220\n"
    "v_mul_f32 v18, v18, v220\n"
    "v_mul_f32 v19, v19, v220\n"
    "v_mul_f32 v28, v28, v220\n"
    "v_mul_f32 v29, v29, v220\n"
    "v_mul_f32 v30, v30, v220\n"
    "v_mul_f32 v31, v31, v220\n"
    "v_mul_f32 v40, v40, v220\n"
    "v_mul_f32 v41, v41, v220\n"
    "v_mul_f32 v42, v42, v220\n"
    "v_mul_f32 v43, v43, v220\n"
    "v_mul_f32 v52, v52, v220\n"
    "v_mul_f32 v53, v53, v220\n"
    "v_mul_f32 v54, v54, v220\n"
    "v_mul_f32 v55, v55, v220\n"
    "v_mul_f32 v64, v64, v220\n"
    "v_mul_f32 v65, v65, v220\n"
    "v_mul_f32 v66, v66, v220\n"
    "v_mul_f32 v67, v67, v220\n"
    "v_mul_f32 v76, v76, v220\n"
    "v_mul_f32 v77, v77, v220\n"
    "v_mul_f32 v78, v78, v220\n"
    "v_mul_f32 v79, v79, v220\n"
    "v_mul_f32 v88, v88, v220\n"
    "v_mul_f32 v89, v89, v220\n"
    "v_mul_f32 v90, v90, v220\n"
    "v_mul_f32 v91, v91, v220\n"
    "v_accvgpr_read_b32 v251, a68\n"
    "v_mul_f32 v251, v251, v220\n"
    "v_accvgpr_write_b32 a68, v251\n"
    "v_accvgpr_read_b32 v251, a69\n"
    "v_mul_f32 v251, v251, v220\n"
    "v_accvgpr_write_b32 a69, v251\n"
    "v_accvgpr_read_b32 v251, a70\n"
    "v_mul_f32 v251, v251, v220\n"
    "v_accvgpr_write_b32 a70, v251\n"
    "v_accvgpr_read_b32 v251, a71\n"
    "v_mul_f32 v251, v251, v220\n"
    "v_accvgpr_write_b32 a71, v251\n"
    "v_max3_f32 v250, v152, v153, v154\n"
    "v_max3_f32 v250, v250, v155, v164\n"
    "v_max3_f32 v250, v250, v165, v166\n"
    "v_max_f32 v250, v250, v167\n"
    "v_mov_b32 v251, v250\n"
    "s_nop 1\n"
    "v_permlane32_swap_b32 v250, v251\n"
    "v_max_f32 v250, v250, v251\n"
    "v_mov_b32 v251, v250\n"
    "s_nop 1\n"
    "v_permlane16_swap_b32 v250, v251\n"
    "v_max_f32 v250, v250, v251\n"
    "v_max_f32 v250, v250, v218\n"
    "v_sub_f32 v221, v218, v250\n"
    "v_exp_f32 v221, v221\n"
    "v_mov_b32 v218, v250\n"
    "v_sub_f32 v152, v152, v250\n"
    "v_exp_f32 v152, v152\n"
    "v_sub_f32 v153, v153, v250\n"
    "v_exp_f32 v153, v153\n"
    "v_sub_f32 v154, v154, v250\n"
    "v_exp_f32 v154, v154\n"
    "v_sub_f32 v155, v155, v250\n"
    "v_exp_f32 v155, v155\n"
    "v_sub_f32 v164, v164, v250\n"
    "v_exp_f32 v164, v164\n"
    "v_sub_f32 v165, v165, v250\n"
    "v_exp_f32 v165, v165\n"
    "v_sub_f32 v166, v166, v250\n"
    "v_exp_f32 v166, v166\n"
    "v_sub_f32 v167, v167, v250\n"
    "v_exp_f32 v167, v167\n"
    "v_cvt_pk_bf16_f32 v200, v152, v153\n"
    "v_cvt_pk_bf16_f32 v201, v154, v155\n"
    "v_cvt_pk_bf16_f32 v202, v164, v165\n"
    "v_cvt_pk_bf16_f32 v203, v166, v167\n"
    "v_mul_f32 v8, v8, v221\n"
    "v_mul_f32 v9, v9, v221\n"
    "v_mul_f32 v10, v10, v221\n"
    "v_mul_f32 v11, v11, v221\n"
    "v_mul_f32 v20, v20, v221\n"
    "v_mul_f32 v21, v21, v221\n"
    "v_mul_f32 v22, v22, v221\n"
    "v_mul_f32 v23, v23, v221\n"
    "v_mul_f32 v32, v32, v221\n"
    "v_mul_f32 v33, v33, v221\n"
    "v_mul_f32 v34, v34, v221\n"
    "v_mul_f32 v35, v35, v221\n"
    "v_mul_f32 v44, v44, v221\n"
    "v_mul_f32 v45, v45, v221\n"
    "v_mul_f32 v46, v46, v221\n"
    "v_mul_f32 v47, v47, v221\n"
    "v_mul_f32 v56, v56, v221\n"
    "v_mul_f32 v57, v57, v221\n"
    "v_mul_f32 v58, v58, v221\n"
    "v_mul_f32 v59, v59, v221\n"
    "v_mul_f32 v68, v68, v221\n"
    "v_mul_f32 v69, v69, v221\n"
    "v_mul_f32 v70, v70, v221\n"
    "v_mul_f32 v71, v71, v221\n"
    "v_mul_f32 v80, v80, v221\n"
    "v_mul_f32 v81, v81, v221\n"
    "v_mul_f32 v82, v82, v221\n"
    "v_mul_f32 v83, v83, v221\n"
    "v_mul_f32 v92, v92, v221\n"
    "v_mul_f32 v93, v93, v221\n"
    "v_mul_f32 v94, v94, v221\n"
    "v_mul_f32 v95, v95, v221\n"
    "v_accvgpr_read_b32 v251, a72\n"
    "v_mul_f32 v251, v251, v221\n"
    "v_accvgpr_write_b32 a72, v251\n"
    "v_accvgpr_read_b32 v251, a73\n"
    "v_mul_f32 v251, v251, v221\n"
    "v_accvgpr_write_b32 a73, v251\n"
    "v_accvgpr_read_b32 v251, a74\n"
    "v_mul_f32 v251, v251, v221\n"
    "v_accvgpr_write_b32 a74, v251\n"
    "v_accvgpr_read_b32 v251, a75\n"
    "v_mul_f32 v251, v251, v221\n"
    "v_accvgpr_write_b32 a75, v251\n"
    "v_mfma_f32_16x16x32_bf16 a[64:67], a[76:79], v[192:195], a[64:67]\n"
    "v_mfma_f32_16x16x32_bf16 a[68:71], a[76:79], v[196:199], a[68:71]\n"
    "v_mfma_f32_16x16x32_bf16 a[72:75], a[76:79], v[200:203], a[72:75]\n"
    "s_waitcnt lgkmcnt(0)\n"
    "v_mfma_f32_16x16x32_bf16 v[0:3], a[32:35], v[192:195], v[0:3]\n"
    "v_mfma_f32_16x16x32_bf16 v[4:7], a[32:35], v[196:199], v[4:7]\n"
    "v_mfma_f32_16x16x32_bf16 v[8:11], a[32:35], v[200:203], v[8:11]\n"
    "v_mfma_f32_16x16x32_bf16 v[12:15], a[36:39], v[192:195], v[12:15]\n"
    "v_mfma_f32_16x16x32_bf16 v[16:19], a[36:39], v[196:199], v[16:19]\n"
    "v_mfma_f32_16x16x32_bf16 v[20:23], a[36:39], v[200:203], v[20:23]\n"
    "v_mfma_f32_16x16x32_bf16 v[24:27], a[40:43], v[192:195], v[24:27]\n"
    "v_mfma_f32_16x16x32_bf16 v[28:31], a[40:43], v[196:199], v[28:31]\n"
    "v_mfma_f32_16x16x32_bf16 v[32:35], a[40:43], v[200:203], v[32:35]\n"
    "v_mfma_f32_16x16x32_bf16 v[36:39], a[44:47], v[192:195], v[36:39]\n"
    "v_mfma_f32_16x16x32_bf16 v[40:43], a[44:47], v[196:199], v[40:43]\n"
    "v_mfma_f32_16x16x32_bf16 v[44:47], a[44:47], v[200:203], v[44:47]\n"
    "v_mfma_f32_16x16x32_bf16 v[48:51], a[48:51], v[192:195], v[48:51]\n"
    "v_mfma_f32_16x16x32_bf16 v[52:55], a[48:51], v[196:199], v[52:55]\n"
    "v_mfma_f32_16x16x32_bf16 v[56:59], a[48:51], v[200:203], v[56:59]\n"
    "v_mfma_f32_16x16x32_bf16 v[60:63], a[52:55], v[192:195], v[60:63]\n"
    "v_mfma_f32_16x16x32_bf16 v[64:67], a[52:55], v[196:199], v[64:67]\n"
    "v_mfma_f32_16x16x32_bf16 v[68:71], a[52:55], v[200:203], v[68:71]\n"
    "v_mfma_f32_16x16x32_bf16 v[72:75], a[56:59], v[192:195], v[72:75]\n"
    "v_mfma_f32_16x16x32_bf16 v[76:79], a[56:59], v[196:199], v[76:79]\n"
    "v_mfma_f32_16x16x32_bf16 v[80:83], a[56:59], v[200:203], v[80:83]\n"
    "v_mfma_f32_16x16x32_bf16 v[84:87], a[60:63], v[192:195], v[84:87]\n"
    "v_mfma_f32_16x16x32_bf16 v[88:91], a[60:63], v[196:199], v[88:91]\n"
    "v_mfma_f32_16x16x32_bf16 v[92:95], a[60:63], v[200:203], v[92:95]\n"
    "ds_read_b128 a[0:3], v234 offset:8192\n"
    "ds_read_b128 a[4:7], v234 offset:12288\n"
    "ds_read_b128 a[8:11], v235 offset:8192\n"
    "ds_read_b128 a[12:15], v235 offset:12288\n"
    "ds_read_b128 a[16:19], v234 offset:10240\n"
    "ds_read_b128 a[20:23], v234 offset:14336\n"
    "ds_read_b128 a[24:27], v235 offset:10240\n"
    "ds_read_b128 a[28:31], v235 offset:14336\n"
    "s_waitcnt lgkmcnt(7)\n"
    "v_mfma_f32_16x16x32_bf16 v[144:147], a[0:3], v[96:99], 0\n"
    "v_mfma_f32_16x16x32_bf16 v[148:151], a[0:3], v[100:103], 0\n"
    "v_mfma_f32_16x16x32_bf16 v[152:155], a[0:3], v[104:107], 0\n"
    "s_waitcnt lgkmcnt(6)\n"
    "v_mfma_f32_16x16x32_bf16 v[156:159], a[4:7], v[96:99], 0\n"
    "v_mfma_f32_16x16x32_bf16 v[160:163], a[4:7], v[100:103], 0\n"
    "v_mfma_f32_16x16x32_bf16 v[164:167], a[4:7], v[104:107], 0\n"
    "s_waitcnt lgkmcnt(5)\n"
    "v_mfma_f32_16x16x32_bf16 v[144:147], a[8:11], v[108:111], v[144:147]\n"
    "v_mfma_f32_16x16x32_bf16 v[148:151], a[8:11], v[112:115], v[148:151]\n"
    "v_mfma_f32_16x16x32_bf16 v[152:155], a[8:11], v[116:119], v[152:155]\n"
    "s_waitcnt lgkmcnt(4)\n"
    "v_mfma_f32_16x16x32_bf16 v[156:159], a[12:15], v[108:111], v[156:159]\n"
    "v_mfma_f32_16x16x32_bf16 v[160:163], a[12:15], v[112:115], v[160:163]\n"
    "v_mfma_f32_16x16x32_bf16 v[164:167], a[12:15], v[116:119], v[164:167]\n"
    "s_waitcnt lgkmcnt(3)\n"
    "v_mfma_f32_16x16x32_bf16 v[144:147], a[16:19], v[120:123], v[144:147]\n"
    "v_mfma_f32_16x16x32_bf16 v[148:151], a[16:19], v[124:127], v[148:151]\n"
    "v_mfma_f32_16x16x32_bf16 v[152:155], a[16:19], v[128:131], v[152:155]\n"
    "s_waitcnt lgkmcnt(2)\n"
    "v_mfma_f32_16x16x32_bf16 v[156:159], a[20:23], v[120:123], v[156:159]\n"
    "v_mfma_f32_16x16x32_bf16 v[160:163], a[20:23], v[124:127], v[160:163]\n"
    "v_mfma_f32_16x16x32_bf16 v[164:167], a[20:23], v[128:131], v[164:167]\n"
    "s_waitcnt lgkmcnt(0)\n"
    "v_mfma_f32_16x16x32_bf16 v[144:147], a[24:27], v[132:135], v[144:147]\n"
    "v_mfma_f32_16x16x32_bf16 v[148:151], a[24:27], v[136:139], v[148:151]\n"
    "v_mfma_f32_16x16x32_bf16 v[152:155], a[24:27], v[140:143], v[152:155]\n"
    "v_mfma_f32_16x16x32_bf16 v[156:159], a[28:31], v[132:135], v[156:159]\n"
    "v_mfma_f32_16x16x32_bf16 v[160:163], a[28:31], v[136:139], v[160:163]\n"
    "v_mfma_f32_16x16x32_bf16 v[164:167], a[28:31], v[140:143], v[164:167]\n"
    "ds_read_b64_tr_b16 a[32:33], v236 offset:8192\n"
    "ds_read_b64_tr_b16 a[34:35], v236 offset:12288\n"
    "ds_read_b64_tr_b16 a[36:37], v237 offset:8192\n"
    "ds_read_b64_tr_b16 a[38:39], v237 offset:12288\n"
    "ds_read_b64_tr_b16 a[40:41], v236 offset:8704\n"
    "ds_read_b64_tr_b16 a[42:43], v236 offset:12800\n"
    "ds_read_b64_tr_b16 a[44:45], v237 offset:8704\n"
    "ds_read_b64_tr_b16 a[46:47], v237 offset:12800\n"
    "ds_read_b64_tr_b16 a[48:49], v236 offset:9216\n"
    "ds_read_b64_tr_b16 a[50:51], v236 offset:13312\n"
    "ds_read_b64_tr_b16 a[52:53], v237 offset:9216\n"
    "ds_read_b64_tr_b16 a[54:55], v237 offset:13312\n"
    "ds_read_b64_tr_b16 a[56:57], v236 offset:9728\n"
    "ds_read_b64_tr_b16 a[58:59], v236 offset:13824\n"
    "ds_read_b64_tr_b16 a[60:61], v237 offset:9728\n"
    "ds_read_b64_tr_b16 a[62:63], v237 offset:13824\n"
    "v_max3_f32 v250, v144, v145, v146\n"
    "v_max3_f32 v250, v250, v147, v156\n"
    "v_max3_f32 v250, v250, v157, v158\n"
    "v_max_f32 v250, v250, v159\n"
    "v_mov_b32 v251, v250\n"
    "s_nop 1\n"
    "v_permlane32_swap_b32 v250, v251\n"
    "v_max_f32 v250, v250, v251\n"
    "v_mov_b32 v251, v250\n"
    "s_nop 1\n"
    "v_permlane16_swap_b32 v250, v251\n"
    "v_max_f32 v250, v250, v251\n"
    "v_max_f32 v250, v250, v216\n"
    "v_sub_f32 v219, v216, v250\n"
    "v_exp_f32 v219, v219\n"
    "v_mov_b32 v216, v250\n"
    "v_sub_f32 v144, v144, v250\n"
    "v_exp_f32 v144, v144\n"
    "v_sub_f32 v145, v145, v250\n"
    "v_exp_f32 v145, v145\n"
    "v_sub_f32 v146, v146, v250\n"
    "v_exp_f32 v146, v146\n"
    "v_sub_f32 v147, v147, v250\n"
    "v_exp_f32 v147, v147\n"
    "v_sub_f32 v156, v156, v250\n"
    "v_exp_f32 v156, v156\n"
    "v_sub_f32 v157, v157, v250\n"
    "v_exp_f32 v157, v157\n"
    "v_sub_f32 v158, v158, v250\n"
    "v_exp_f32 v158, v158\n"
    "v_sub_f32 v159, v159, v250\n"
    "v_exp_f32 v159, v159\n"
    "v_cvt_pk_bf16_f32 v192, v144, v145\n"
    "v_cvt_pk_bf16_f32 v193, v146, v147\n"
    "v_cvt_pk_bf16_f32 v194, v156, v157\n"
    "v_cvt_pk_bf16_f32 v195, v158, v159\n"
    "v_mul_f32 v0, v0, v219\n"
    "v_mul_f32 v1, v1, v219\n"
    "v_mul_f32 v2, v2, v219\n"
    "v_mul_f32 v3, v3, v219\n"
    "v_mul_f32 v12, v12, v219\n"
    "v_mul_f32 v13, v13, v219\n"
    "v_mul_f32 v14, v14, v219\n"
    "v_mul_f32 v15, v15, v219\n"
    "v_mul_f32 v24, v24, v219\n"
    "v_mul_f32 v25, v25, v219\n"
    "v_mul_f32 v26, v26, v219\n"
    "v_mul_f32 v27, v27, v219\n"
    "v_mul_f32 v36, v36, v219\n"
    "v_mul_f32 v37, v37, v219\n"
    "v_mul_f32 v38, v38, v219\n"
    "v_mul_f32 v39, v39, v219\n"
    "v_mul_f32 v48, v48, v219\n"
    "v_mul_f32 v49, v49, v219\n"
    "v_mul_f32 v50, v50, v219\n"
    "v_mul_f32 v51, v51, v219\n"
    "v_mul_f32 v60, v60, v219\n"
    "v_mul_f32 v61, v61, v219\n"
    "v_mul_f32 v62, v62, v219\n"
    "v_mul_f32 v63, v63, v219\n"
    "v_mul_f32 v72, v72, v219\n"
    "v_mul_f32 v73, v73, v219\n"
    "v_mul_f32 v74, v74, v219\n"
    "v_mul_f32 v75, v75, v219\n"
    "v_mul_f32 v84, v84, v219\n"
    "v_mul_f32 v85, v85, v219\n"
    "v_mul_f32 v86, v86, v219\n"
    "v_mul_f32 v87, v87, v219\n"
    "v_accvgpr_read_b32 v251, a64\n"
    "v_mul_f32 v251, v251, v219\n"
    "v_accvgpr_write_b32 a64, v251\n"
    "v_accvgpr_read_b32 v251, a65\n"
    "v_mul_f32 v251, v251, v219\n"
    "v_accvgpr_write_b32 a65, v251\n"
    "v_accvgpr_read_b32 v251, a66\n"
    "v_mul_f32 v251, v251, v219\n"
    "v_accvgpr_write_b32 a66, v251\n"
    "v_accvgpr_read_b32 v251, a67\n"
    "v_mul_f32 v251, v251, v219\n"
    "v_accvgpr_write_b32 a67, v251\n"
    "v_max3_f32 v250, v148, v149, v150\n"
    "v_max3_f32 v250, v250, v151, v160\n"
    "v_max3_f32 v250, v250, v161, v162\n"
    "v_max_f32 v250, v250, v163\n"
    "v_mov_b32 v251, v250\n"
    "s_nop 1\n"
    "v_permlane32_swap_b32 v250, v251\n"
    "v_max_f32 v250, v250, v251\n"
    "v_mov_b32 v251, v250\n"
    "s_nop 1\n"
    "v_permlane16_swap_b32 v250, v251\n"
    "v_max_f32 v250, v250, v251\n"
    "v_max_f32 v250, v250, v217\n"
    "v_sub_f32 v220, v217, v250\n"
    "v_exp_f32 v220, v220\n"
    "v_mov_b32 v217, v250\n"
    "v_sub_f32 v148, v148, v250\n"
    "v_exp_f32 v148, v148\n"
    "v_sub_f32 v149, v149, v250\n"
    "v_exp_f32 v149, v149\n"
    "v_sub_f32 v150, v150, v250\n"
    "v_exp_f32 v150, v150\n"
    "v_sub_f32 v151, v151, v250\n"
    "v_exp_f32 v151, v151\n"
    "v_sub_f32 v160, v160, v250\n"
    "v_exp_f32 v160, v160\n"
    "v_sub_f32 v161, v161, v250\n"
    "v_exp_f32 v161, v161\n"
    "v_sub_f32 v162, v162, v250\n"
    "v_exp_f32 v162, v162\n"
    "v_sub_f32 v163, v163, v250\n"
    "v_exp_f32 v163, v163\n"
    "v_cvt_pk_bf16_f32 v196, v148, v149\n"
    "v_cvt_pk_bf16_f32 v197, v150, v151\n"
    "v_cvt_pk_bf16_f32 v198, v160, v161\n"
    "v_cvt_pk_bf16_f32 v199, v162, v163\n"
    "v_mul_f32 v4, v4, v220\n"
    "v_mul_f32 v5, v5, v220\n"
    "v_mul_f32 v6, v6, v220\n"
    "v_mul_f32 v7, v7, v220\n"
    "v_mul_f32 v16, v16, v220\n"
    "v_mul_f32 v17, v17, v220\n"
    "v_mul_f32 v18, v18, v220\n"
    "v_mul_f32 v19, v19, v220\n"
    "v_mul_f32 v28, v28, v220\n"
    "v_mul_f32 v29, v29, v220\n"
    "v_mul_f32 v30, v30, v220\n"
    "v_mul_f32 v31, v31, v220\n"
    "v_mul_f32 v40, v40, v220\n"
    "v_mul_f32 v41, v41, v220\n"
    "v_mul_f32 v42, v42, v220\n"
    "v_mul_f32 v43, v43, v220\n"
    "v_mul_f32 v52, v52, v220\n"
    "v_mul_f32 v53, v53, v220\n"
    "v_mul_f32 v54, v54, v220\n"
    "v_mul_f32 v55, v55, v220\n"
    "v_mul_f32 v64, v64, v220\n"
    "v_mul_f32 v65, v65, v220\n"
    "v_mul_f32 v66, v66, v220\n"
    "v_mul_f32 v67, v67, v220\n"
    "v_mul_f32 v76, v76, v220\n"
    "v_mul_f32 v77, v77, v220\n"
    "v_mul_f32 v78, v78, v220\n"
    "v_mul_f32 v79, v79, v220\n"
    "v_mul_f32 v88, v88, v220\n"
    "v_mul_f32 v89, v89, v220\n"
    "v_mul_f32 v90, v90, v220\n"
    "v_mul_f32 v91, v91, v220\n"
    "v_accvgpr_read_b32 v251, a68\n"
    "v_mul_f32 v251, v251, v220\n"
    "v_accvgpr_write_b32 a68, v251\n"
    "v_accvgpr_read_b32 v251, a69\n"
    "v_mul_f32 v251, v251, v220\n"
    "v_accvgpr_write_b32 a69, v251\n"
    "v_accvgpr_read_b32 v251, a70\n"
    "v_mul_f32 v251, v251, v220\n"
    "v_accvgpr_write_b32 a70, v251\n"
    "v_accvgpr_read_b32 v251, a71\n"
    "v_mul_f32 v251, v251, v220\n"
    "v_accvgpr_write_b32 a71, v251\n"
    "v_max3_f32 v250, v152, v153, v154\n"
    "v_max3_f32 v250, v250, v155, v164\n"
    "v_max3_f32 v250, v250, v165, v166\n"
    "v_max_f32 v250, v250, v167\n"
    "v_mov_b32 v251, v250\n"
    "s_nop 1\n"
    "v_permlane32_swap_b32 v250, v251\n"
    "v_max_f32 v250, v250, v251\n"
    "v_mov_b32 v251, v250\n"
    "s_nop 1\n"
    "v_permlane16_swap_b32 v250, v251\n"
    "v_max_f32 v250, v250, v251\n"
    "v_max_f32 v250, v250, v218\n"
    "v_sub_f32 v221, v218, v250\n"
    "v_exp_f32 v221, v221\n"
    "v_mov_b32 v218, v250\n"
    "v_sub_f32 v152, v152, v250\n"
    "v_exp_f32 v152, v152\n"
    "v_sub_f32 v153, v153, v250\n"
    "v_exp_f32 v153, v153\n"
    "v_sub_f32 v154, v154, v250\n"
    "v_exp_f32 v154, v154\n"
    "v_sub_f32 v155, v155, v250\n"
    "v_exp_f32 v155, v155\n"
    "v_sub_f32 v164, v164, v250\n"
    "v_exp_f32 v164, v164\n"
    "v_sub_f32 v165, v165, v250\n"
    "v_exp_f32 v165, v165\n"
    "v_sub_f32 v166, v166, v250\n"
    "v_exp_f32 v166, v166\n"
    "v_sub_f32 v167, v167, v250\n"
    "v_exp_f32 v167, v167\n"
    "v_cvt_pk_bf16_f32 v200, v152, v153\n"
    "v_cvt_pk_bf16_f32 v201, v154, v155\n"
    "v_cvt_pk_bf16_f32 v202, v164, v165\n"
    "v_cvt_pk_bf16_f32 v203, v166, v167\n"
    "v_mul_f32 v8, v8, v221\n"
    "v_mul_f32 v9, v9, v221\n"
    "v_mul_f32 v10, v10, v221\n"
    "v_mul_f32 v11, v11, v221\n"
    "v_mul_f32 v20, v20, v221\n"
    "v_mul_f32 v21, v21, v221\n"
    "v_mul_f32 v22, v22, v221\n"
    "v_mul_f32 v23, v23, v221\n"
    "v_mul_f32 v32, v32, v221\n"
    "v_mul_f32 v33, v33, v221\n"
    "v_mul_f32 v34, v34, v221\n"
    "v_mul_f32 v35, v35, v221\n"
    "v_mul_f32 v44, v44, v221\n"
    "v_mul_f32 v45, v45, v221\n"
    "v_mul_f32 v46, v46, v221\n"
    "v_mul_f32 v47, v47, v221\n"
    "v_mul_f32 v56, v56, v221\n"
    "v_mul_f32 v57, v57, v221\n"
    "v_mul_f32 v58, v58, v221\n"
    "v_mul_f32 v59, v59, v221\n"
    "v_mul_f32 v68, v68, v221\n"
    "v_mul_f32 v69, v69, v221\n"
    "v_mul_f32 v70, v70, v221\n"
    "v_mul_f32 v71, v71, v221\n"
    "v_mul_f32 v80, v80, v221\n"
    "v_mul_f32 v81, v81, v221\n"
    "v_mul_f32 v82, v82, v221\n"
    "v_mul_f32 v83, v83, v221\n"
    "v_mul_f32 v92, v92, v221\n"
    "v_mul_f32 v93, v93, v221\n"
    "v_mul_f32 v94, v94, v221\n"
    "v_mul_f32 v95, v95, v221\n"
    "v_accvgpr_read_b32 v251, a72\n"
    "v_mul_f32 v251, v251, v221\n"
    "v_accvgpr_write_b32 a72, v251\n"
    "v_accvgpr_read_b32 v251, a73\n"
    "v_mul_f32 v251, v251, v221\n"
    "v_accvgpr_write_b32 a73, v251\n"
    "v_accvgpr_read_b32 v251, a74\n"
    "v_mul_f32 v251, v251, v221\n"
    "v_accvgpr_write_b32 a74, v251\n"
    "v_accvgpr_read_b32 v251, a75\n"
    "v_mul_f32 v251, v251, v221\n"
    "v_accvgpr_write_b32 a75, v251\n"
    "v_mfma_f32_16x16x32_bf16 a[64:67], a[76:79], v[192:195], a[64:67]\n"
    "v_mfma_f32_16x16x32_bf16 a[68:71], a[76:79], v[196:199], a[68:71]\n"
    "v_mfma_f32_16x16x32_bf16 a[72:75], a[76:79], v[200:203], a[72:75]\n"
    "s_waitcnt lgkmcnt(0)\n"
    "v_mfma_f32_16x16x32_bf16 v[0:3], a[32:35], v[192:195], v[0:3]\n"
    "v_mfma_f32_16x16x32_bf16 v[4:7], a[32:35], v[196:199], v[4:7]\n"
    "v_mfma_f32_16x16x32_bf16 v[8:11], a[32:35], v[200:203], v[8:11]\n"
    "v_mfma_f32_16x16x32_bf16 v[12:15], a[36:39], v[192:195], v[12:15]\n"
    "v_mfma_f32_16x16x32_bf16 v[16:19], a[36:39], v[196:199], v[16:19]\n"
    "v_mfma_f32_16x16x32_bf16 v[20:23], a[36:39], v[200:203], v[20:23]\n"
    "v_mfma_f32_16x16x32_bf16 v[24:27], a[40:43], v[192:195], v[24:27]\n"
    "v_mfma_f32_16x16x32_bf16 v[28:31], a[40:43], v[196:199], v[28:31]\n"
    "v_mfma_f32_16x16x32_bf16 v[32:35], a[40:43], v[200:203], v[32:35]\n"
    "v_mfma_f32_16x16x32_bf16 v[36:39], a[44:47], v[192:195], v[36:39]\n"
    "v_mfma_f32_16x16x32_bf16 v[40:43], a[44:47], v[196:199], v[40:43]\n"
    "v_mfma_f32_16x16x32_bf16 v[44:47], a[44:47], v[200:203], v[44:47]\n"
    "v_mfma_f32_16x16x32_bf16 v[48:51], a[48:51], v[192:195], v[48:51]\n"
    "v_mfma_f32_16x16x32_bf16 v[52:55], a[48:51], v[196:199], v[52:55]\n"
    "v_mfma_f32_16x16x32_bf16 v[56:59], a[48:51], v[200:203], v[56:59]\n"
    "v_mfma_f32_16x16x32_bf16 v[60:63], a[52:55], v[192:195], v[60:63]\n"
    "v_mfma_f32_16x16x32_bf16 v[64:67], a[52:55], v[196:199], v[64:67]\n"
    "v_mfma_f32_16x16x32_bf16 v[68:71], a[52:55], v[200:203], v[68:71]\n"
    "v_mfma_f32_16x16x32_bf16 v[72:75], a[56:59], v[192:195], v[72:75]\n"
    "v_mfma_f32_16x16x32_bf16 v[76:79], a[56:59], v[196:199], v[76:79]\n"
    "v_mfma_f32_16x16x32_bf16 v[80:83], a[56:59], v[200:203], v[80:83]\n"
    "v_mfma_f32_16x16x32_bf16 v[84:87], a[60:63], v[192:195], v[84:87]\n"
    "v_mfma_f32_16x16x32_bf16 v[88:91], a[60:63], v[196:199], v[88:91]\n"
    "v_mfma_f32_16x16x32_bf16 v[92:95], a[60:63], v[200:203], v[92:95]\n"
    "s_waitcnt lgkmcnt(0)\n"
    "s_add_u32 s92, s92, 16384\n"
    "s_cmp_lt_u32 s92, 131072\n"
    "s_cbranch_scc1 L_FB_LOOP\n"
    "s_nop 7\n"
    "s_nop 7\n"
    "v_accvgpr_read_b32 v244, a64\n"
    "v_rcp_f32 v244, v244\n"
    "s_nop 0\n"
    "v_mov_b32 v245, v244\n"
    "v_accvgpr_read_b32 v246, a68\n"
    "v_rcp_f32 v246, v246\n"
    "s_nop 0\n"
    "v_mov_b32 v247, v246\n"
    "v_accvgpr_read_b32 v248, a72\n"
    "v_rcp_f32 v248, v248\n"
    "s_nop 0\n"
    "v_mov_b32 v249, v248\n"
    "v_pk_mul_f32 v[0:1], v[0:1], v[244:245]\n"
    "v_pk_mul_f32 v[2:3], v[2:3], v[244:245]\n"
    "v_cvt_pk_bf16_f32 v144, v0, v1\n"
    "v_cvt_pk_bf16_f32 v145, v2, v3\n"
    "v_pk_mul_f32 v[12:13], v[12:13], v[244:245]\n"
    "v_pk_mul_f32 v[14:15], v[14:15], v[244:245]\n"
    "v_cvt_pk_bf16_f32 v146, v12, v13\n"
    "v_cvt_pk_bf16_f32 v147, v14, v15\n"
    "s_add_u32 s91, s98, 0\n"
    "buffer_store_dwordx4 v[144:147], v230, s[56:59], s91 offen offset:0 sc1\n"
    "v_pk_mul_f32 v[4:5], v[4:5], v[246:247]\n"
    "v_pk_mul_f32 v[6:7], v[6:7], v[246:247]\n"
    "v_cvt_pk_bf16_f32 v148, v4, v5\n"
    "v_cvt_pk_bf16_f32 v149, v6, v7\n"
    "v_pk_mul_f32 v[16:17], v[16:17], v[246:247]\n"
    "v_pk_mul_f32 v[18:19], v[18:19], v[246:247]\n"
    "v_cvt_pk_bf16_f32 v150, v16, v17\n"
    "v_cvt_pk_bf16_f32 v151, v18, v19\n"
    "s_add_u32 s91, s98, 1024\n"
    "buffer_store_dwordx4 v[148:151], v230, s[56:59], s91 offen offset:0 sc1\n"
    "v_pk_mul_f32 v[8:9], v[8:9], v[248:249]\n"
    "v_pk_mul_f32 v[10:11], v[10:11], v[248:249]\n"
    "v_cvt_pk_bf16_f32 v152, v8, v9\n"
    "v_cvt_pk_bf16_f32 v153, v10, v11\n"
    "v_pk_mul_f32 v[20:21], v[20:21], v[248:249]\n"
    "v_pk_mul_f32 v[22:23], v[22:23], v[248:249]\n"
    "v_cvt_pk_bf16_f32 v154, v20, v21\n"
    "v_cvt_pk_bf16_f32 v155, v22, v23\n"
    "s_add_u32 s91, s98, 2048\n"
    "buffer_store_dwordx4 v[152:155], v230, s[56:59], s91 offen offset:0 sc1\n"
    "v_pk_mul_f32 v[24:25], v[24:25], v[244:245]\n"
    "v_pk_mul_f32 v[26:27], v[26:27], v[244:245]\n"
    "v_cvt_pk_bf16_f32 v156, v24, v25\n"
    "v_cvt_pk_bf16_f32 v157, v26, v27\n"
    "v_pk_mul_f32 v[36:37], v[36:37], v[244:245]\n"
    "v_pk_mul_f32 v[38:39], v[38:39], v[244:245]\n"
    "v_cvt_pk_bf16_f32 v158, v36, v37\n"
    "v_cvt_pk_bf16_f32 v159, v38, v39\n"
    "s_add_u32 s91, s98, 3072\n"
    "buffer_store_dwordx4 v[156:159], v230, s[56:59], s91 offen offset:0 sc1\n"
    "v_pk_mul_f32 v[28:29], v[28:29], v[246:247]\n"
    "v_pk_mul_f32 v[30:31], v[30:31], v[246:247]\n"
    "v_cvt_pk_bf16_f32 v160, v28, v29\n"
    "v_cvt_pk_bf16_f32 v161, v30, v31\n"
    "v_pk_mul_f32 v[40:41], v[40:41], v[246:247]\n"
    "v_pk_mul_f32 v[42:43], v[42:43], v[246:247]\n"
    "v_cvt_pk_bf16_f32 v162, v40, v41\n"
    "v_cvt_pk_bf16_f32 v163, v42, v43\n"
    "s_add_u32 s91, s98, 4096\n"
    "buffer_store_dwordx4 v[160:163], v230, s[56:59], s91 offen offset:0 sc1\n"
    "v_pk_mul_f32 v[32:33], v[32:33], v[248:249]\n"
    "v_pk_mul_f32 v[34:35], v[34:35], v[248:249]\n"
    "v_cvt_pk_bf16_f32 v164, v32, v33\n"
    "v_cvt_pk_bf16_f32 v165, v34, v35\n"
    "v_pk_mul_f32 v[44:45], v[44:45], v[248:249]\n"
    "v_pk_mul_f32 v[46:47], v[46:47], v[248:249]\n"
    "v_cvt_pk_bf16_f32 v166, v44, v45\n"
    "v_cvt_pk_bf16_f32 v167, v46, v47\n"
    "s_add_u32 s91, s98, 5120\n"
    "buffer_store_dwordx4 v[164:167], v230, s[56:59], s91 offen offset:0 sc1\n"
    "v_pk_mul_f32 v[48:49], v[48:49], v[244:245]\n"
    "v_pk_mul_f32 v[50:51], v[50:51], v[244:245]\n"
    "v_cvt_pk_bf16_f32 v168, v48, v49\n"
    "v_cvt_pk_bf16_f32 v169, v50, v51\n"
    "v_pk_mul_f32 v[60:61], v[60:61], v[244:245]\n"
    "v_pk_mul_f32 v[62:63], v[62:63], v[244:245]\n"
    "v_cvt_pk_bf16_f32 v170, v60, v61\n"
    "v_cvt_pk_bf16_f32 v171, v62, v63\n"
    "s_add_u32 s91, s98, 6144\n"
    "buffer_store_dwordx4 v[168:171], v230, s[56:59], s91 offen offset:0 sc1\n"
    "v_pk_mul_f32 v[52:53], v[52:53], v[246:247]\n"
    "v_pk_mul_f32 v[54:55], v[54:55], v[246:247]\n"
    "v_cvt_pk_bf16_f32 v172, v52, v53\n"
    "v_cvt_pk_bf16_f32 v173, v54, v55\n"
    "v_pk_mul_f32 v[64:65], v[64:65], v[246:247]\n"
    "v_pk_mul_f32 v[66:67], v[66:67], v[246:247]\n"
    "v_cvt_pk_bf16_f32 v174, v64, v65\n"
    "v_cvt_pk_bf16_f32 v175, v66, v67\n"
    "s_add_u32 s91, s98, 7168\n"
    "buffer_store_dwordx4 v[172:175], v230, s[56:59], s91 offen offset:0 sc1\n"
    "v_pk_mul_f32 v[56:57], v[56:57], v[248:249]\n"
    "v_pk_mul_f32 v[58:59], v[58:59], v[248:249]\n"
    "v_cvt_pk_bf16_f32 v176, v56, v57\n"
    "v_cvt_pk_bf16_f32 v177, v58, v59\n"
    "v_pk_mul_f32 v[68:69], v[68:69], v[248:249]\n"
    "v_pk_mul_f32 v[70:71], v[70:71], v[248:249]\n"
    "v_cvt_pk_bf16_f32 v178, v68, v69\n"
    "v_cvt_pk_bf16_f32 v179, v70, v71\n"
    "s_add_u32 s91, s98, 8192\n"
    "buffer_store_dwordx4 v[176:179], v230, s[56:59], s91 offen offset:0 sc1\n"
    "v_pk_mul_f32 v[72:73], v[72:73], v[244:245]\n"
    "v_pk_mul_f32 v[74:75], v[74:75], v[244:245]\n"
    "v_cvt_pk_bf16_f32 v180, v72, v73\n"
    "v_cvt_pk_bf16_f32 v181, v74, v75\n"
    "v_pk_mul_f32 v[84:85], v[84:85], v[244:245]\n"
    "v_pk_mul_f32 v[86:87], v[86:87], v[244:245]\n"
    "v_cvt_pk_bf16_f32 v182, v84, v85\n"
    "v_cvt_pk_bf16_f32 v183, v86, v87\n"
    "s_add_u32 s91, s98, 9216\n"
    "buffer_store_dwordx4 v[180:183], v230, s[56:59], s91 offen offset:0 sc1\n"
    "v_pk_mul_f32 v[76:77], v[76:77], v[246:247]\n"
    "v_pk_mul_f32 v[78:79], v[78:79], v[246:247]\n"
    "v_cvt_pk_bf16_f32 v184, v76, v77\n"
    "v_cvt_pk_bf16_f32 v185, v78, v79\n"
    "v_pk_mul_f32 v[88:89], v[88:89], v[246:247]\n"
    "v_pk_mul_f32 v[90:91], v[90:91], v[246:247]\n"
    "v_cvt_pk_bf16_f32 v186, v88, v89\n"
    "v_cvt_pk_bf16_f32 v187, v90, v91\n"
    "s_add_u32 s91, s98, 10240\n"
    "buffer_store_dwordx4 v[184:187], v230, s[56:59], s91 offen offset:0 sc1\n"
    "v_pk_mul_f32 v[80:81], v[80:81], v[248:249]\n"
    "v_pk_mul_f32 v[82:83], v[82:83], v[248:249]\n"
    "v_cvt_pk_bf16_f32 v188, v80, v81\n"
    "v_cvt_pk_bf16_f32 v189, v82, v83\n"
    "v_pk_mul_f32 v[92:93], v[92:93], v[248:249]\n"
    "v_pk_mul_f32 v[94:95], v[94:95], v[248:249]\n"
    "v_cvt_pk_bf16_f32 v190, v92, v93\n"
    "v_cvt_pk_bf16_f32 v191, v94, v95\n"
    "s_add_u32 s91, s98, 11264\n"
    "buffer_store_dwordx4 v[188:191], v230, s[56:59], s91 offen offset:0 sc1\n"
    "s_waitcnt vmcnt(0)\n"
    "s_barrier\n"
    "s_branch L_PUBLISH\n"
    "L_SPIN2:\n"
    "s_nop 7\n"
    "s_nop 3\n"
    "s_mov_b32 s37, 0\n"
    "L_SPIN2_LOOP:\n"
    "s_nop 7\n"
    "s_nop 3\n"
    "global_load_dwordx2 v[244:245], v232, s[64:65] offset:0 sc1\n"
    "global_load_dwordx2 v[246:247], v232, s[64:65] offset:512 sc1\n"
    "global_load_dwordx2 v[248:249], v232, s[64:65] offset:1024 sc1\n"
    "global_load_dwordx2 v[250:251], v232, s[64:65] offset:1536 sc1\n"
    "s_waitcnt vmcnt(0)\n"
    "v_cmp_eq_u64 vcc, v[244:245], s[94:95]\n"
    "s_nop 3\n"
    "s_mov_b64 s[38:39], vcc\n"
    "v_cmp_eq_u64 vcc, v[246:247], s[94:95]\n"
    "s_nop 3\n"
    "s_and_b64 s[38:39], s[38:39], vcc\n"
    "v_cmp_eq_u64 vcc, v[248:249], s[94:95]\n"
    "s_nop 3\n"
    "s_and_b64 s[38:39], s[38:39], vcc\n"
    "v_cmp_eq_u64 vcc, v[250:251], s[94:95]\n"
    "s_nop 3\n"
    "s_and_b64 s[38:39], s[38:39], vcc\n"
    "s_cmp_eq_u64 s[38:39], -1\n"
    "s_cbranch_scc1 L_SPIN2_OK\n"
    "s_sleep 1\n"
    "s_add_u32 s37, s37, 1\n"
    "s_cmp_lt_u32 s37, 40000\n"
    "s_cbranch_scc1 L_SPIN2_LOOP\n"
    "s_branch L_SPIN2_OK\n"
    "L_POLL:\n"
    "s_nop 7\n"
    "s_nop 3\n"
    "s_mov_b32 s37, 0\n"
    "L_POLL_LOOP:\n"
    "s_nop 7\n"
    "s_nop 3\n"
    "global_load_dwordx2 v[240:241], v236, s[64:65] offset:0 sc1\n"
    "s_waitcnt vmcnt(0)\n"
    "v_cmp_eq_u64 vcc, v[240:241], s[94:95]\n"
    "s_nop 3\n"
    "s_mov_b64 s[90:91], vcc\n"
    "s_cmp_eq_u64 s[90:91], -1\n"
    "s_cbranch_scc1 L_POLL_OK\n"
    "s_sleep 1\n"
    "s_add_u32 s37, s37, 1\n"
    "s_cmp_lt_u32 s37, 40000\n"
    "s_cbranch_scc1 L_POLL_LOOP\n"
    "s_branch L_POLL_OK\n"
    "L_POLL3:\n"
    "s_nop 7\n"
    "s_nop 3\n"
    "s_mov_b32 s37, 0\n"
    "L_POLL3_LOOP:\n"
    "s_nop 7\n"
    "s_nop 3\n"
    "global_load_dwordx2 v[240:241], v236, s[64:65] offset:0 sc1\n"
    "s_waitcnt vmcnt(0)\n"
    "v_cmp_eq_u64 vcc, v[240:241], s[94:95]\n"
    "s_nop 3\n"
    "s_mov_b64 s[90:91], vcc\n"
    "s_cmp_eq_u64 s[90:91], -1\n"
    "s_cbranch_scc1 L_POLL3_OK\n"
    "s_sleep 1\n"
    "s_add_u32 s37, s37, 1\n"
    "s_cmp_lt_u32 s37, 40000\n"
    "s_cbranch_scc1 L_POLL3_LOOP\n"
    "s_branch L_POLL3_OK\n"
    "L_END:\n"
    "s_nop 7\n"
    "s_nop 3\n"
    :
    : "s"(__builtin_amdgcn_kernarg_segment_ptr()), "s"(blockIdx.x), "v"(threadIdx.x)
    : "s8", "s9", "s10", "s11", "s12", "s13", "s14", "s15", "s16", "s17", "s18", "s19", "s20", "s21", "s22", "v1", "v2", "v3", "v4", "v5", "v6", "v7", "v8", "v9", "v10", "v11", "v12", "v13", "v14", "v15", "v16", "v17", "v18", "v19", "v20", "v21", "v22", "v23", "v24", "v25", "v26", "v27", "v28", "v29", "v30", "v31", "v32", "v33", "v34", "v35", "v36", "v37", "v38", "v39", "v40", "v41", "v42", "v43", "v44", "v45", "v46", "v47", "v48", "v49", "v50", "v51", "v52", "v53", "v54", "v55", "v56", "v57", "v58", "v59", "v60", "v61", "v62", "v63", "v64", "v65", "v66", "v67", "v68", "v69", "v70", "v71", "v72", "v73", "v74", "v75", "v76", "v77", "v78", "v79", "v80", "v81", "v82", "v83", "v84", "v85", "v86", "v87", "v88", "v89", "v90", "v91", "v92", "v93", "v94", "v95", "v96", "v97", "v98", "v99", "v100", "v101", "v102", "v103", "v104", "v105", "v106", "v107", "v108", "v109", "v110", "v111", "v112", "v113", "v114", "v115", "v116", "v117", "v118", "v119", "v120", "v121", "v122", "v123", "v124", "v125", "v126", "v127", "v128", "v129", "v130", "v131", "v132", "v133", "v134", "v135", "v136", "v137", "v138", "v139", "v140", "v141", "v142", "v143", "v144", "v145", "v146", "v147", "v148", "v149", "v150", "v151", "v152", "v153", "v154", "v155", "v156", "v157", "v158", "v159", "v160", "v161", "v162", "v163", "v164", "v165", "v166", "v167", "v168", "v169", "v170", "v171", "v172", "v173", "v174", "v175", "v176", "v177", "v178", "v179", "v180", "v181", "v182", "v183", "v184", "v185", "v186", "v187", "v188", "v189", "v190", "v191", "v192", "v193", "v194", "v195", "v196", "v197", "v198", "v199", "v200", "v201", "v202", "v203", "v204", "v205", "v206", "v207", "v208", "v209", "v210", "v211", "v212", "v213", "v214", "v215", "v216", "v217", "v218", "v219", "v220", "v221", "v222", "v223", "v224", "v225", "v226", "v227", "v228", "v229", "v230", "v231", "v232", "v233", "v234", "v235", "v236", "v237", "v238", "v239", "v240", "v241", "v242", "v243", "v244", "v245", "v246", "v247", "v248", "v249", "v250", "v251", "v252", "v253", "v254", "v255", "a0", "a1", "a2", "a3", "a4", "a5", "a6", "a7", "a8", "a9", "a10", "a11", "a12", "a13", "a14", "a15", "a16", "a17", "a18", "a19", "a20", "a21", "a22", "a23", "a24", "a25", "a26", "a27", "a28", "a29", "a30", "a31", "a32", "a33", "a34", "a35", "a36", "a37", "a38", "a39", "a40", "a41", "a42", "a43", "a44", "a45", "a46", "a47", "a48", "a49", "a50", "a51", "a52", "a53", "a54", "a55", "a56", "a57", "a58", "a59", "a60", "a61", "a62", "a63", "a64", "a65", "a66", "a67", "a68", "a69", "a70", "a71", "a72", "a73", "a74", "a75", "a76", "a77", "a78", "a79", "a80", "a81", "a82", "a83", "a84", "a85", "a86", "a87", "a88", "a89", "a90", "a91", "a92", "a93", "a94", "a95", "a96", "a97", "a98", "a99", "a100", "a101", "a102", "a103", "a104", "a105", "a106", "a107", "a108", "a109", "a110", "a111", "a112", "a113", "a114", "a115", "a116", "a117", "a118", "a119", "a120", "a121", "a122", "a123", "a124", "a125", "a126", "a127", "a128", "a129", "a130", "a131", "a132", "a133", "a134", "a135", "a136", "a137", "a138", "a139", "a140", "a141", "a142", "a143", "a144", "a145", "a146", "a147", "a148", "a149", "a150", "a151", "a152", "a153", "a154", "a155", "a156", "a157", "a158", "a159", "a160", "a161", "a162", "a163", "a164", "a165", "a166", "a167", "a168", "a169", "a170", "a171", "a172", "a173", "a174", "a175", "a176", "a177", "a178", "a179", "a180", "a181", "a182", "a183", "a184", "a185", "a186", "a187", "a188", "a189", "a190", "a191", "a192", "a193", "a194", "a195", "a196", "a197", "a198", "a199", "a200", "a201", "a202", "a203", "a204", "a205", "a206", "a207", "a208", "a209", "a210", "a211", "a212", "a213", "a214", "a215", "a216", "a217", "a218", "a219", "a220", "a221", "a222", "a223", "a224", "a225", "a226", "a227", "a228", "a229", "a230", "a231", "a232", "a233", "a234", "a235", "a236", "a237", "a238", "a239", "a240", "a241", "a242", "a243", "a244", "a245", "a246", "a247", "a248", "a249", "a250", "a251", "a252", "a253", "a254", "a255", "s36", "s37", "s38", "s39", "s40", "s41", "s42", "s43", "s44", "s45", "s46", "s47", "s48", "s49", "s50", "s51", "s52", "s53", "s54", "s55", "s56", "s57", "s58", "s59", "s60", "s61", "s62", "s63", "s64", "s65", "s66", "s67", "s68", "s69", "s70", "s71", "s72", "s73", "s74", "s75", "s76", "s77", "s78", "s79", "s80", "s81", "s82", "s83", "s84", "s85", "s86", "s87", "s88", "s89", "s90", "s91", "s92", "s93", "s94", "s95", "s96", "s97", "s98", "s99", "vcc", "scc", "memory");
}

extern "C" void kernel_launch(void* const* d_in, const int* in_sizes, int n_in, void* d_out, int out_size, void* d_ws, size_t ws_size, hipStream_t stream) {
  (void)hipFuncSetAttribute((const void*)mha_fused, hipFuncAttributeMaxDynamicSharedMemorySize, 163840);
  mha_fused<<<256, 256, 163840, stream>>>(d_in[0], d_in[1], d_in[2], d_in[3], d_in[4], d_out, d_ws);
}
```
